```python
import torch

def _softmax(x, axis=-1):
    return torch.softmax(x, dim=axis)
def _gelu(x):
    return torch.nn.functional.gelu(x, approximate="tanh")
def _silu(x):
    return torch.nn.functional.silu(x)
def _sigmoid(x):
    return torch.sigmoid(x)
def _layernorm(x, axis=-1, eps=1e-5):
    m = x.mean(dim=axis, keepdim=True)
    v = x.var(dim=axis, keepdim=True, unbiased=False)
    return (x - m) / torch.sqrt(v + eps)
def _scatter_add(tgt, idx, src, axis=0):
    return tgt.clone().index_add_(axis, idx, src)
def _segment_sum(src, seg, n):
    out = torch.zeros((n,) + src.shape[1:], dtype=src.dtype)
    return out.index_add_(0, seg, src)
def _pad(x, pw):
    flat = tuple(v for pair in reversed(pw) for v in pair)
    return torch.nn.functional.pad(x, flat)

kernel_name = 'sequential_preLN__spectral_gfnet__dense_activation_silu__prompt'

def setup_inputs():

    _w = (lambda a: (lambda t: t.to(torch.bfloat16) if t.is_floating_point() else t)(torch.as_tensor(a)))
    def _A_setup():
        B, S, D = 1, 2048, 640
        Df = D // 2 + 1
        return {
            "x": _w((lambda *s: torch.randn(*s, dtype=torch.bfloat16) * 0.05)(B, S, D)),
            "w_r": _w((lambda *s: torch.randn(*s, dtype=torch.bfloat16) * 0.4)(S, Df) * 0.02),
            "w_i": _w((lambda *s: torch.randn(*s, dtype=torch.bfloat16) * 0.4)(S, Df) * 0.02),
        }
    _A_in = _A_setup()

    def _B_setup():
        B, S, D, F = 1, 2048, 640, 1728
        return {
            "x": _w((lambda *s: torch.randn(*s, dtype=torch.bfloat16) * 0.05)(B, S, D)),
            "w1": _w((lambda *s: torch.randn(*s, dtype=torch.bfloat16) * 0.8)(D, F) * (D ** -0.5)),
            "w2": _w((lambda *s: torch.randn(*s, dtype=torch.bfloat16) * 0.1)(F, D) * (F ** -0.5)),
        }
    _B_in = _B_setup()

    return {
        "A_x": _A_in["x"],
        "A_w_r": _A_in["w_r"],
        "A_w_i": _A_in["w_i"],
        "B_x": _B_in["x"],
        "B_w1": _B_in["w1"],
        "B_w2": _B_in["w2"],
    }

def reference(A_x, A_w_r, A_w_i, B_x, B_w1, B_w2):
    def _A_ref(x, w_r, w_i):
        B, S, D = x.shape
        X = (lambda x, axes=(-2,-1): torch.fft.rfft2(x.float(), dim=axes))(x, axes=(-2, -1))
        W = w_r + 1j * w_i
        X = X * W
        y = (lambda x, s, axes=(-2,-1): torch.fft.irfft2(x, s=s, dim=axes).to(torch.bfloat16))(X, (S, D), axes=(-2, -1))
        return y

    def _B_ref(x, w1, w2):
        h = torch.einsum("bsd,df->bsf", x, w1)
        h = _silu(h)
        out = torch.einsum("bsf,fd->bsd", h, w2)
        return out

    x = A_x
    h = x + _A_ref(_layernorm(x, axis=-1), A_w_r, A_w_i)
    h2 = h + _B_ref(_layernorm(h, axis=-1), B_w1, B_w2)
    return h2
```

```cpp
#include <hip/hip_runtime.h>

typedef unsigned u32;
typedef unsigned short u16;
typedef unsigned u32x2 __attribute__((ext_vector_type(2)));
typedef unsigned u32x4 __attribute__((ext_vector_type(4)));
typedef float f32x2 __attribute__((ext_vector_type(2)));
typedef float f32x4 __attribute__((ext_vector_type(4)));
typedef __bf16 bf16x2 __attribute__((ext_vector_type(2)));
typedef __bf16 bf16x8 __attribute__((ext_vector_type(8)));
typedef short s16x4 __attribute__((ext_vector_type(4)));
#define DI __device__ __forceinline__
#define LAS __attribute__((address_space(3)))
#define GAS __attribute__((address_space(1)))

constexpr int S = 2048, D = 640, F = 1728, DF = 321;
constexpr float EPS = 1e-5f;

DI u32 pk(float lo, float hi) { return __builtin_bit_cast(u32, __builtin_convertvector(f32x2{lo, hi}, bf16x2)); }
DI float blo(u32 v) { return __builtin_bit_cast(float, v << 16); }
DI float bhi(u32 v) { return __builtin_bit_cast(float, v & 0xffff0000u); }
DI f32x4 mfma32(u32x4 a, u32x4 b, f32x4 c) {
    return __builtin_amdgcn_mfma_f32_16x16x32_bf16(__builtin_bit_cast(bf16x8, a), __builtin_bit_cast(bf16x8, b), c, 0, 0, 0);
}
DI f32x4 mfma16(u32x2 a, u32x2 b, f32x4 c) {
    return __builtin_amdgcn_mfma_f32_16x16x16bf16_1k(__builtin_bit_cast(s16x4, a), __builtin_bit_cast(s16x4, b), c, 0, 0, 0);
}
DI float cosr(float r) { return __builtin_amdgcn_cosf(r); }
DI float sinr(float r) { return __builtin_amdgcn_sinf(r); }
DI u32x2 trread(const LAS char* p) {
    return __builtin_bit_cast(u32x2, __builtin_amdgcn_ds_read_tr16_b64_v4i16((LAS s16x4*)p));
}
template <int CTRL> DI float dpp(float v) {
    return __builtin_bit_cast(float, __builtin_amdgcn_update_dpp(0, __builtin_bit_cast(int, v), CTRL, 0xf, 0xf, true));
}
DI float wave_sum(float v) {
    v += dpp<0xB1>(v);
    v += dpp<0x4E>(v);
    v += dpp<0x141>(v);
    v += dpp<0x140>(v);
    float a = __builtin_bit_cast(float, __builtin_amdgcn_readlane(__builtin_bit_cast(int, v), 0));
    float b = __builtin_bit_cast(float, __builtin_amdgcn_readlane(__builtin_bit_cast(int, v), 16));
    float c = __builtin_bit_cast(float, __builtin_amdgcn_readlane(__builtin_bit_cast(int, v), 32));
    float d = __builtin_bit_cast(float, __builtin_amdgcn_readlane(__builtin_bit_cast(int, v), 48));
    return (a + b) + (c + d);
}
constexpr f32x4 Z4 = {0.f, 0.f, 0.f, 0.f};
constexpr int LROW = 352;

DI int blk_s2(int b) { return ((b & 7) << 5) | (b >> 3); }

__global__ __launch_bounds__(512) void k_r1(const u32* __restrict__ x, u32* __restrict__ A2) {
    __shared__ __attribute__((aligned(16))) u32 L[8 * LROW];
    const int tid = threadIdx.x, l = tid & 63, w = __builtin_amdgcn_readfirstlane(tid >> 6), m = l & 15, g = l >> 4;
    const int s2 = blk_s2(blockIdx.x);
    const int s = 256 * w + s2;
    u32 xv[5];
#pragma unroll
    for (int i = 0; i < 5; ++i) xv[i] = x[s * 320 + 64 * i + l];
    u32x4 f20r, f20i;
    {
        float c[5], sn[5];
#pragma unroll
        for (int j = 0; j < 5; ++j) { float a = (float)((4 * j + g) * m) * (1.f / 20.f); a -= floorf(a); c[j] = cosr(a); sn[j] = -sinr(a); }
        f20r = u32x4{pk(c[0], c[1]), pk(c[2], c[3]), pk(c[4], 0.f), 0u};
        f20i = u32x4{pk(sn[0], sn[1]), pk(sn[2], sn[3]), pk(sn[4], 0.f), 0u};
    }
    float twc[2][4], tws[2][4];
    u32x4 ar[2], ai[2];
#pragma unroll
    for (int t = 0; t < 2; ++t)
#pragma unroll
        for (int r = 0; r < 4; ++r) {
            const int p = (2 * (4 * g + r) + t) * m;
            float a = (float)p * (1.f / 640.f);
            twc[t][r] = cosr(a); tws[t][r] = sinr(a);
            float b = (float)(p & 31) * (1.f / 32.f);
            float cb = cosr(b), sb = sinr(b);
            ar[t][r] = pk(cb, sb); ai[t][r] = pk(-sb, cb);
        }
    u32x2 a8;
    {
        const int j1 = m >> 1;
#pragma unroll
        for (int q = 0; q < 2; ++q) {
            float a = (float)((j1 * (256 * (2 * g + q) + s2)) & 2047) * (1.f / 2048.f);
            float c = cosr(a), sn = sinr(a);
            a8[q] = (m & 1) ? pk(-sn, c) : pk(c, sn);
        }
    }
    float v[2][5];
    float sum = 0.f;
#pragma unroll
    for (int i = 0; i < 5; ++i) { v[0][i] = blo(xv[i]); v[1][i] = bhi(xv[i]); sum += v[0][i] + v[1][i]; }
    const float mean = wave_sum(sum) * (1.f / D);
    float ss = 0.f;
#pragma unroll
    for (int i = 0; i < 5; ++i) { v[0][i] -= mean; v[1][i] -= mean; ss += v[0][i] * v[0][i] + v[1][i] * v[1][i]; }
    const float rstd = __builtin_amdgcn_rsqf(wave_sum(ss) * (1.f / D) + EPS);
    f32x4 pr[2], pi[2];
#pragma unroll
    for (int t = 0; t < 2; ++t) {
        u32x4 a = {pk(v[t][0] * rstd, v[t][1] * rstd), pk(v[t][2] * rstd, v[t][3] * rstd), pk(v[t][4] * rstd, 0.f), 0u};
        f32x4 tr = mfma32(a, f20r, Z4), ti = mfma32(a, f20i, Z4);
        u32x4 b;
#pragma unroll
        for (int r = 0; r < 4; ++r)
            b[r] = pk(tr[r] * twc[t][r] + ti[r] * tws[t][r], ti[r] * twc[t][r] - tr[r] * tws[t][r]);
        pr[t] = mfma32(ar[t], b, Z4);
        pi[t] = mfma32(ai[t], b, Z4);
    }
#pragma unroll
    for (int r = 0; r < 4; ++r) {
        const int k = m + 20 * (4 * g + r);
        L[w * LROW + 3 + k] = pk(pr[0][r] + pr[1][r], pi[0][r] + pi[1][r]);
        L[w * LROW + 3 + 320 - k] = pk(pr[0][r] - pr[1][r], pi[1][r] - pi[0][r]);
    }
    __syncthreads();
    if (w < 5) {
        const int c0 = 64 * w + 4 * m;
        u32x4 r0 = *(const u32x4*)&L[(2 * g) * LROW + 4 + c0];
        u32x4 r1 = *(const u32x4*)&L[(2 * g + 1) * LROW + 4 + c0];
        u32x4 o0, o1;
#pragma unroll
        for (int t = 0; t < 4; ++t) {
            f32x4 d = mfma16(a8, u32x2{r0[t], r1[t]}, Z4);
            o0[t] = pk(d[0], d[1]); o1[t] = pk(d[2], d[3]);
        }
        *(u32x4*)&A2[((2 * g) * 256 + s2) * 320 + c0] = o0;
        *(u32x4*)&A2[((2 * g + 1) * 256 + s2) * 320 + c0] = o1;
    }
}

struct __attribute__((packed, aligned(2))) U8 { u32x2 v; };
__global__ __launch_bounds__(256) void k_c(const u32* __restrict__ A2, const u16* __restrict__ wr, const u16* __restrict__ wi,
                                           u32* __restrict__ B2) {
    const int tid = threadIdx.x, l = tid & 63, w = __builtin_amdgcn_readfirstlane(tid >> 6), m = l & 15, g = l >> 4;
    const int j1 = blockIdx.x & 7, cg = blockIdx.x >> 3;
    const int c0 = 16 * cg + 4 * w;
    u32x4 in[4];
#pragma unroll
    for (int j = 0; j < 4; ++j) in[j] = *(const u32x4*)&A2[(j1 * 256 + 16 * (4 * g + j) + m) * 320 + c0];
    u32x2 wrv[4], wiv[4];
#pragma unroll
    for (int r = 0; r < 4; ++r) {
        const int sp = j1 + 8 * (m + 16 * (4 * g + r));
        wrv[r] = ((const U8*)(wr + sp * DF + 1 + c0))->v;
        wiv[r] = ((const U8*)(wi + sp * DF + 1 + c0))->v;
    }
    u32x4 fr, fi;
    float tc[4], ts[4];
#pragma unroll
    for (int r = 0; r < 4; ++r) {
        const int p = (4 * g + r) * m;
        float a = (float)(p & 15) * (1.f / 16.f);
        float c = cosr(a), sn = sinr(a);
        fr[r] = pk(c, sn); fi[r] = pk(-sn, c);
        float b = (float)p * (1.f / 256.f);
        tc[r] = cosr(b); ts[r] = sinr(b);
    }
    constexpr float SC = 2.f / ((float)S * (float)D);
    u32x4 out[4];
#pragma unroll
    for (int t = 0; t < 4; ++t) {
        u32x4 d = {in[0][t], in[1][t], in[2][t], in[3][t]};
        f32x4 re = mfma32(d, fr, Z4), im = mfma32(d, fi, Z4);
#pragma unroll
        for (int r = 0; r < 4; ++r) d[r] = pk(re[r] * tc[r] + im[r] * ts[r], im[r] * tc[r] - re[r] * ts[r]);
        re = mfma32(fr, d, Z4); im = mfma32(fi, d, Z4);
#pragma unroll
        for (int r = 0; r < 4; ++r) {
            const u32 a = wrv[r][t >> 1], b = wiv[r][t >> 1];
            const float wa = ((t & 1) ? bhi(a) : blo(a)) * SC, wb = ((t & 1) ? bhi(b) : blo(b)) * SC;
            d[r] = pk(re[r] * wb + im[r] * wa, re[r] * wa - im[r] * wb);
        }
        re = mfma32(d, fr, Z4); im = mfma32(d, fi, Z4);
#pragma unroll
        for (int r = 0; r < 4; ++r) d[r] = pk(re[r] * tc[r] + im[r] * ts[r], im[r] * tc[r] - re[r] * ts[r]);
        re = mfma32(fr, d, Z4); im = mfma32(fi, d, Z4);
#pragma unroll
        for (int r = 0; r < 4; ++r) out[r][t] = pk(im[r], re[r]);
    }
#pragma unroll
    for (int j = 0; j < 4; ++j) *(u32x4*)&B2[(j1 * 256 + 16 * (4 * g + j) + m) * 320 + c0] = out[j];
}

__global__ __launch_bounds__(512) void k_r2(const u32* __restrict__ B2, const u32* __restrict__ x, u32* __restrict__ h,
                                            u32x4* __restrict__ Ln) {
    __shared__ __attribute__((aligned(16))) u32 L[8 * LROW + 20 * 144];
    u32* L2 = L + 8 * LROW;
    const int tid = threadIdx.x, l = tid & 63, w = __builtin_amdgcn_readfirstlane(tid >> 6), m = l & 15, g = l >> 4;
    const int s2 = blk_s2(blockIdx.x);
    const int s = 256 * w + s2;
    const int c0 = 64 * (w < 5 ? w : 0) + 4 * m;
    u32x4 r0 = *(const u32x4*)&B2[((2 * g) * 256 + s2) * 320 + c0];
    u32x4 r1 = *(const u32x4*)&B2[((2 * g + 1) * 256 + s2) * 320 + c0];
    u32 xv[5];
#pragma unroll
    for (int i = 0; i < 5; ++i) xv[i] = x[s * 320 + 64 * i + l];
    u32x2 a8;
    {
        const int srow = 256 * (m >> 1) + s2;
#pragma unroll
        for (int q = 0; q < 2; ++q) {
            float a = (float)(((2 * g + q) * srow) & 2047) * (1.f / 2048.f);
            float c = cosr(a), sn = sinr(a);
            a8[q] = (m & 1) ? pk(sn, c) : pk(c, -sn);
        }
    }
    float twc[2][4], tws[2][4];
    u32x4 gr[2], gi[2], h0, h1;
#pragma unroll
    for (int t = 0; t < 2; ++t)
#pragma unroll
        for (int r = 0; r < 4; ++r) {
            const int p = (2 * m + t) * (4 * g + r);
            float a = (float)p * (1.f / 640.f);
            twc[t][r] = cosr(a); tws[t][r] = sinr(a);
            float b = (float)(p & 31) * (1.f / 32.f);
            float cb = cosr(b), sb = sinr(b);
            gr[t][r] = pk(cb, -sb); gi[t][r] = pk(sb, cb);
        }
#pragma unroll
    for (int r = 0; r < 4; ++r) {
        const int k1 = 4 * g + r;
        const float wt = (k1 == 0 || k1 == 10) ? 0.5f : (k1 < 10 ? 1.f : 0.f);
        float a = (float)((4 * (m & 3) + (m >> 2)) * k1) * (1.f / 20.f); a -= floorf(a);
        h0[r] = pk(wt * cosr(a), -wt * sinr(a));
        float b = (float)((16 + (m >> 2)) * k1) * (1.f / 20.f); b -= floorf(b);
        h1[r] = pk(wt * cosr(b), -wt * sinr(b));
    }
    if (w < 5) {
        u32x4 o0, o1;
#pragma unroll
        for (int t = 0; t < 4; ++t) {
            f32x4 d = mfma16(a8, u32x2{r0[t], r1[t]}, Z4);
            o0[t] = pk(d[0], d[1]); o1[t] = pk(d[2], d[3]);
        }
        *(u32x4*)&L[(2 * g) * LROW + 4 + c0] = o0;
        *(u32x4*)&L[(2 * g + 1) * LROW + 4 + c0] = o1;
    } else if (w == 5 && l < 8) {
        L[l * LROW + 3] = 0u;
    }
    __syncthreads();
    u32x4 lo, hi;
#pragma unroll
    for (int r = 0; r < 4; ++r) {
        const int k = m + 20 * (4 * g + r);
        lo[r] = L[w * LROW + 3 + k];
        hi[r] = L[w * LROW + 3 + 320 - k];
    }
    float v[2][5];
#pragma unroll
    for (int t = 0; t < 2; ++t) {
        const u32x4 hs = hi ^ (t ? 0x00008000u : 0x80000000u);
        f32x4 re = mfma32(hs, gr[t], mfma32(lo, gr[t], Z4));
        f32x4 im = mfma32(hs, gi[t], mfma32(lo, gi[t], Z4));
        u32x4 b;
#pragma unroll
        for (int r = 0; r < 4; ++r)
            b[r] = pk(re[r] * twc[t][r] - im[r] * tws[t][r], im[r] * twc[t][r] + re[r] * tws[t][r]);
        f32x4 y0 = mfma32(h0, b, Z4), y1 = mfma32(h1, b, Z4);
#pragma unroll
        for (int r = 0; r < 4; ++r) v[t][r] = y0[r];
        v[t][4] = y1[0];
    }
    float sum = 0.f;
#pragma unroll
    for (int i = 0; i < 5; ++i) {
        const u32 yb = pk(v[0][i], v[1][i]);
        const u32 hb = pk(blo(xv[i]) + blo(yb), bhi(xv[i]) + bhi(yb));
        h[s * 320 + 64 * i + l] = hb;
        v[0][i] = blo(hb); v[1][i] = bhi(hb);
        sum += v[0][i] + v[1][i];
    }
    const float mean = wave_sum(sum) * (1.f / D);
    float ss = 0.f;
#pragma unroll
    for (int i = 0; i < 5; ++i) { v[0][i] -= mean; v[1][i] -= mean; ss += v[0][i] * v[0][i] + v[1][i] * v[1][i]; }
    const float rstd = __builtin_amdgcn_rsqf(wave_sum(ss) * (1.f / D) + EPS);
#pragma unroll
    for (int i = 0; i < 5; ++i)
        L2[(4 * i + g) * 144 + ((m & 7) >> 1) * 36 + w * 4 + (m >> 3) * 2 + (m & 1)] = pk(v[0][i] * rstd, v[1][i] * rstd);
    __syncthreads();
    for (int p = tid; p < 640; p += 512) {
        const int row = p & 7, gg = (p >> 3) & 3, ks = p >> 5;
        Ln[((s2 >> 1) * 20 + ks) * 64 + gg * 16 + (s2 & 1) * 8 + row] = *(const u32x4*)&L2[ks * 144 + gg * 36 + row * 4];
    }
}

DI float silu(float v) { return v * __builtin_amdgcn_rcpf(1.f + __builtin_amdgcn_exp2f(v * -1.44269504089f)); }
__global__ __launch_bounds__(256) void k_g1(const u32x4* __restrict__ Ln, const u16* __restrict__ w1, u32x4* __restrict__ H) {
    extern __shared__ __attribute__((aligned(16))) char smem[];
    const int tid = threadIdx.x, l = tid & 63, w = __builtin_amdgcn_readfirstlane(tid >> 6), g = l >> 4;
    const int bb = blk_s2(blockIdx.x), P = bb >> 4, j = bb & 15;
    const int ft0 = min(7 * j, 101);
#pragma unroll
    for (int i = 0; i < 7; ++i) {
        const int p = 64 * i + l, row = p / 14, ch = p % 14;
        const u16* src = w1 + (size_t)(32 * w + row) * F + 16 * ft0 + 8 * ch;
#pragma unroll
        for (int q = 0; q < 5; ++q)
            __builtin_amdgcn_global_load_lds((const GAS void*)(src + (size_t)q * 128 * F),
                                             (LAS void*)(smem + (4 * q + w) * 7168 + i * 1024), 16, 0, 0);
    }
    u32x4 act[2][20];
#pragma unroll
    for (int ks = 0; ks < 20; ++ks)
#pragma unroll
        for (int T = 0; T < 2; ++T) act[T][ks] = Ln[((P * 8 + 2 * w + T) * 20 + ks) * 64 + l];
    asm volatile("s_waitcnt vmcnt(0)" ::: "memory");
    __syncthreads();
    f32x4 acc[2][7];
#pragma unroll
    for (int T = 0; T < 2; ++T)
#pragma unroll
        for (int c = 0; c < 7; ++c) acc[T][c] = Z4;
    const LAS char* base = (const LAS char*)smem + (4 * g + ((l & 15) >> 2)) * 224 + (l & 3) * 8;
#pragma unroll
    for (int ks = 0; ks < 20; ++ks) {
#pragma unroll
        for (int c = 0; c < 7; ++c) {
            const u32x2 a0 = trread(base + ks * 7168 + c * 32), a1 = trread(base + ks * 7168 + 16 * 224 + c * 32);
            const u32x4 a = {a0[0], a0[1], a1[0], a1[1]};
            acc[0][c] = mfma32(a, act[0][ks], acc[0][c]);
            acc[1][c] = mfma32(a, act[1][ks], acc[1][c]);
        }
    }
#pragma unroll
    for (int c = 0; c < 7; ++c) {
        u32x4 o;
#pragma unroll
        for (int T = 0; T < 2; ++T) {
            const u32 p0 = pk(acc[T][c][0], acc[T][c][1]), p1 = pk(acc[T][c][2], acc[T][c][3]);
            o[2 * T] = pk(silu(blo(p0)), silu(bhi(p0)));
            o[2 * T + 1] = pk(silu(blo(p1)), silu(bhi(p1)));
        }
        if (ft0 + c >= 7 * j) H[((P * 4 + w) * 108 + ft0 + c) * 64 + l] = o;
    }
}

__global__ __launch_bounds__(256) void k_g2(const u32x4* __restrict__ H, const u16* __restrict__ w2, const u32x4* __restrict__ h,
                                            u32x4* __restrict__ out) {
    extern __shared__ __attribute__((aligned(16))) char smem[];
    const int tid = threadIdx.x, l = tid & 63, w = __builtin_amdgcn_readfirstlane(tid >> 6), g = l >> 4;
    const int bb = blk_s2(blockIdx.x), P = bb >> 4, j = bb & 15, half = j >> 3, cj = j & 7;
    const int ks0 = w < 2 ? 14 * w : 13 * w + 2;
    const int nks = w < 2 ? 14 : 13;
    char* wl = smem + w * 7 * 5120;
    f32x4 acc[4][5];
#pragma unroll
    for (int T = 0; T < 4; ++T)
#pragma unroll
        for (int c = 0; c < 5; ++c) acc[T][c] = Z4;
    const LAS char* base = (const LAS char*)wl + (4 * g + ((l & 15) >> 2)) * 160 + (l & 3) * 8;
#pragma unroll
    for (int rnd = 0; rnd < 2; ++rnd) {
        u32x4 act[7][4];
#pragma unroll
        for (int q = 0; q < 7; ++q) {
            const int ks = min(ks0 + 7 * rnd + q, 53);
#pragma unroll
            for (int i = 0; i < 5; ++i) {
                const int p = 64 * i + l, row = p / 10, ch = p % 10;
                __builtin_amdgcn_global_load_lds((const GAS void*)(w2 + (size_t)(32 * ks + row) * D + 80 * cj + 8 * ch),
                                                 (LAS void*)(wl + q * 5120 + i * 1024), 16, 0, 0);
            }
#pragma unroll
            for (int u = 0; u < 4; ++u) act[q][u] = H[((P * 4 + 2 * half + (u >> 1)) * 108 + 2 * ks + (u & 1)) * 64 + l];
        }
        asm volatile("s_waitcnt vmcnt(0)" ::: "memory");
#pragma unroll
        for (int q = 0; q < 7; ++q) {
            if (7 * rnd + q < nks) {
                u32x4 b[4];
#pragma unroll
                for (int tp = 0; tp < 2; ++tp) {
                    const u32x4 X = act[q][2 * tp], Y = act[q][2 * tp + 1];
                    b[2 * tp] = u32x4{X[0], X[1], Y[0], Y[1]};
                    b[2 * tp + 1] = u32x4{X[2], X[3], Y[2], Y[3]};
                }
#pragma unroll
                for (int c = 0; c < 5; ++c) {
                    const u32x2 a0 = trread(base + q * 5120 + c * 32), a1 = trread(base + q * 5120 + 16 * 160 + c * 32);
                    const u32x4 a = {a0[0], a0[1], a1[0], a1[1]};
#pragma unroll
                    for (int T = 0; T < 4; ++T) acc[T][c] = mfma32(a, b[T], acc[T][c]);
                }
            }
        }
        asm volatile("s_waitcnt lgkmcnt(0)" ::: "memory");
    }
    __syncthreads();
    float* red = (float*)smem;
#pragma unroll
    for (int T = 0; T < 4; ++T)
#pragma unroll
        for (int c = 0; c < 5; ++c) *(f32x4*)&red[(w * 64 + T * 16 + (l & 15)) * 84 + c * 16 + 4 * g] = acc[T][c];
    __syncthreads();
    for (int p = tid; p < 640; p += 256) {
        const int tok = p / 10, ch = p % 10;
        f32x4 s0 = Z4, s1 = Z4;
#pragma unroll
        for (int u = 0; u < 4; ++u) {
            s0 += *(const f32x4*)&red[(u * 64 + tok) * 84 + ch * 8];
            s1 += *(const f32x4*)&red[(u * 64 + tok) * 84 + ch * 8 + 4];
        }
        const int tau = P * 128 + half * 64 + tok, s = 256 * (tau & 7) + (tau >> 3);
        const int idx = s * 80 + cj * 10 + ch;
        const u32x4 hv = h[idx];
        const u32 q0 = pk(s0[0], s0[1]), q1 = pk(s0[2], s0[3]), q2 = pk(s1[0], s1[1]), q3 = pk(s1[2], s1[3]);
        out[idx] = u32x4{pk(blo(hv[0]) + blo(q0), bhi(hv[0]) + bhi(q0)), pk(blo(hv[1]) + blo(q1), bhi(hv[1]) + bhi(q1)),
                         pk(blo(hv[2]) + blo(q2), bhi(hv[2]) + bhi(q2)), pk(blo(hv[3]) + blo(q3), bhi(hv[3]) + bhi(q3))};
    }
}

#define EXTRA_EMPTY 0
__global__ void k_empty(u32* p) { if (p == nullptr) *p = 0; }

extern "C" void kernel_launch(void* const* d_in, const int* in_sizes, int n_in, void* d_out, int out_size, void* d_ws,
                              size_t ws_size, hipStream_t stream) {
    const u32* x = (const u32*)d_in[0];
    const u16* wr = (const u16*)d_in[1];
    const u16* wi = (const u16*)d_in[2];
    const u16* w1 = (const u16*)d_in[4];
    const u16* w2 = (const u16*)d_in[5];
    char* ws = (char*)d_ws;
    constexpr size_t SZ = (size_t)S * D * 2;
    u32* A2 = (u32*)ws;
    u32* B2 = (u32*)(ws + SZ);
    u32* h = (u32*)(ws + 2 * SZ);
    u32x4* Ln = (u32x4*)(ws + 3 * SZ);
    u32x4* H = (u32x4*)(ws + 4 * SZ);
    (void)hipFuncSetAttribute((const void*)k_g1, hipFuncAttributeMaxDynamicSharedMemorySize, 143360);
    (void)hipFuncSetAttribute((const void*)k_g2, hipFuncAttributeMaxDynamicSharedMemorySize, 143360);
    for (int i = 0; i < EXTRA_EMPTY; ++i) k_empty<<<256, 256, 0, stream>>>(A2);
    k_r1<<<256, 512, 0, stream>>>(x, A2);
    k_c<<<160, 256, 0, stream>>>(A2, wr, wi, B2);
    k_r2<<<256, 512, 0, stream>>>(B2, x, h, Ln);
    k_g1<<<256, 256, 143360, stream>>>(Ln, w1, H);
    k_g2<<<256, 256, 143360, stream>>>(H, w2, (const u32x4*)h, (u32x4*)d_out);
}
```

```cpp
#include <hip/hip_runtime.h>

typedef unsigned u32;
#define OFF_DBG 25165824

static __device__ const u32 ktab[5120] __attribute__((aligned(256))) = {
    0x3f803f80,0x3f803f80,0x3f80,0x0,0x3e9e3f80,0xbf4fbf4f,0x3e9e,0x0,0xbf4f3f80,0x3e9e3e9e,0xbf4f,0x0,0xbf4f3f80,0x3e9e3e9e,0xbf4f,0x0,
    0x3e9e3f80,0xbf4fbf4f,0x3e9e,0x0,0x3f803f80,0x3f803f80,0x3f80,0x0,0x3e9e3f80,0xbf4fbf4f,0x3e9e,0x0,0xbf4f3f80,0x3e9e3e9e,0xbf4f,0x0,
    0xbf4f3f80,0x3e9e3e9e,0xbf4f,0x0,0x3e9e3f80,0xbf4fbf4f,0x3e9e,0x0,0x3f803f80,0x3f803f80,0x3f80,0x0,0x3e9e3f80,0xbf4fbf4f,0x3e9e,0x0,
    0xbf4f3f80,0x3e9e3e9e,0xbf4f,0x0,0xbf4f3f80,0x3e9e3e9e,0xbf4f,0x0,0x3e9e3f80,0xbf4fbf4f,0x3e9e,0x0,0x3f803f80,0x3f803f80,0x3f80,0x0,
    0x3f803f80,0x3f803f80,0x3f80,0x0,0x248d3f73,0xbf16bf73,0x3f16,0x0,0xbf803f4f,0xbe9e3f4f,0xbe9e,0x0,0xa5543f16,0x3f73bf16,0xbf73,0x0,
    0x3f803e9e,0xbf4f3e9e,0xbf4f,0x0,0x248d248d,0x248d248d,0x248d,0x0,0xbf80be9e,0x3f4fbe9e,0x3f4f,0x0,0xa554bf16,0xbf733f16,0x3f73,0x0,
    0x3f80bf4f,0x3e9ebf4f,0x3e9e,0x0,0x248dbf73,0x3f163f73,0xbf16,0x0,0xbf80bf80,0xbf80bf80,0xbf80,0x0,0xa554bf73,0x3f163f73,0xbf16,0x0,
    0x3f80bf4f,0x3e9ebf4f,0x3e9e,0x0,0x248dbf16,0xbf733f16,0x3f73,0x0,0xbf80be9e,0x3f4fbe9e,0x3f4f,0x0,0xa554a554,0xa554a554,0xa554,0x0,
    0x3f803f80,0x3f803f80,0x3f80,0x0,0xbe9e3f4f,0xbe9ebf80,0x3f4f,0x0,0xbf4f3e9e,0xbf4f3f80,0x3e9e,0x0,0x3f4fbe9e,0x3f4fbf80,0xbe9e,0x0,
    0x3e9ebf4f,0x3e9e3f80,0xbf4f,0x0,0xbf80bf80,0xbf80bf80,0xbf80,0x0,0x3e9ebf4f,0x3e9e3f80,0xbf4f,0x0,0x3f4fbe9e,0x3f4fbf80,0xbe9e,0x0,
    0xbf4f3e9e,0xbf4f3f80,0x3e9e,0x0,0xbe9e3f4f,0xbe9ebf80,0x3f4f,0x0,0x3f803f80,0x3f803f80,0x3f80,0x0,0xbe9e3f4f,0xbe9ebf80,0x3f4f,0x0,
    0xbf4f3e9e,0xbf4f3f80,0x3e9e,0x0,0x3f4fbe9e,0x3f4fbf80,0xbe9e,0x0,0x3e9ebf4f,0x3e9e3f80,0xbf4f,0x0,0xbf80bf80,0xbf80bf80,0xbf80,0x0,
    0x3f803f80,0x3f803f80,0x3f80,0x0,0xbf163f16,0xa554bf73,0x3f73,0x0,0xbe9ebe9e,0xbf803f4f,0x3f4f,0x0,0x3f73bf73,0x248dbf16,0x3f16,0x0,
    0xbf4fbf4f,0x3f803e9e,0x3e9e,0x0,0xa554a554,0xa554a554,0xa554,0x0,0x3f4f3f4f,0xbf80be9e,0xbe9e,0x0,0xbf733f73,0x248d3f16,0xbf16,0x0,
    0x3e9e3e9e,0x3f80bf4f,0xbf4f,0x0,0x3f16bf16,0xa5543f73,0xbf73,0x0,0xbf80bf80,0xbf80bf80,0xbf80,0x0,0x3f16bf16,0x248d3f73,0xbf73,0x0,
    0x3e9e3e9e,0x3f80bf4f,0xbf4f,0x0,0xbf733f73,0xa5543f16,0xbf16,0x0,0x3f4f3f4f,0xbf80be9e,0xbe9e,0x0,0x248d248d,0x248d248d,0x248d,0x0,
    0x80008000,0x80008000,0x8000,0x0,0xbf738000,0x3f16bf16,0x3f73,0x0,0xbf168000,0xbf733f73,0x3f16,0x0,0x3f168000,0x3f73bf73,0xbf16,0x0,
    0x3f738000,0xbf163f16,0xbf73,0x0,0x80008000,0x80008000,0x8000,0x0,0xbf738000,0x3f16bf16,0x3f73,0x0,0xbf168000,0xbf733f73,0x3f16,0x0,
    0x3f168000,0x3f73bf73,0xbf16,0x0,0x3f738000,0xbf163f16,0xbf73,0x0,0x80008000,0x80008000,0x8000,0x0,0xbf738000,0x3f16bf16,0x3f73,0x0,
    0xbf168000,0xbf733f73,0x3f16,0x0,0x3f168000,0x3f73bf73,0xbf16,0x0,0x3f738000,0xbf163f16,0xbf73,0x0,0x80008000,0x80008000,0x8000,0x0,
    0x80008000,0x80008000,0x8000,0x0,0xbf80be9e,0x3f4fbe9e,0x3f4f,0x0,0xa50dbf16,0xbf733f16,0x3f73,0x0,0x3f80bf4f,0x3e9ebf4f,0x3e9e,0x0,
    0x8000bf73,0x3f163f73,0xbf16,0x0,0xbf80bf80,0xbf80bf80,0xbf80,0x0,0xa50dbf73,0x3f163f73,0xbf16,0x0,0x3f80bf4f,0x3e9ebf4f,0x3e9e,0x0,
    0x8000bf16,0xbf733f16,0x3f73,0x0,0xbf80be9e,0x3f4fbe9e,0x3f4f,0x0,0xa50da50d,0xa50da50d,0xa50d,0x0,0x3f803e9e,0xbf4f3e9e,0xbf4f,0x0,
    0x80003f16,0x3f73bf16,0xbf73,0x0,0xbf803f4f,0xbe9e3f4f,0xbe9e,0x0,0xa50d3f73,0xbf16bf73,0x3f16,0x0,0x3f803f80,0x3f803f80,0x3f80,0x0,
    0x80008000,0x80008000,0x8000,0x0,0xbf73bf16,0x3f73a50d,0x3f16,0x0,0x3f16bf73,0xbf168000,0x3f73,0x0,0x3f16bf73,0xbf16a50d,0x3f73,0x0,
    0xbf73bf16,0x3f738000,0x3f16,0x0,0xa50da50d,0xa50da50d,0xa50d,0x0,0x3f733f16,0xbf738000,0xbf16,0x0,0xbf163f73,0x3f16a50d,0xbf73,0x0,
    0xbf163f73,0x3f168000,0xbf73,0x0,0x3f733f16,0xbf73a50d,0xbf16,0x0,0x80008000,0x80008000,0x8000,0x0,0xbf73bf16,0x3f73a50d,0x3f16,0x0,
    0x3f16bf73,0xbf168000,0x3f73,0x0,0x3f16bf73,0xbf16a50d,0x3f73,0x0,0xbf73bf16,0x3f738000,0x3f16,0x0,0xa50da50d,0xa50da50d,0xa50d,0x0,
    0x80008000,0x80008000,0x8000,0x0,0xbf4fbf4f,0x3f803e9e,0x3e9e,0x0,0x3f73bf73,0xa50dbf16,0x3f16,0x0,0xbe9ebe9e,0xbf803f4f,0x3f4f,0x0,
    0xbf163f16,0x8000bf73,0x3f73,0x0,0x3f803f80,0x3f803f80,0x3f80,0x0,0xbf163f16,0xa50dbf73,0x3f73,0x0,0xbe9ebe9e,0xbf803f4f,0x3f4f,0x0,
    0x3f73bf73,0x8000bf16,0x3f16,0x0,0xbf4fbf4f,0x3f803e9e,0x3e9e,0x0,0xa50da50d,0xa50da50d,0xa50d,0x0,0x3f4f3f4f,0xbf80be9e,0xbe9e,0x0,
    0xbf733f73,0x80003f16,0xbf16,0x0,0x3e9e3e9e,0x3f80bf4f,0xbf4f,0x0,0x3f16bf16,0xa50d3f73,0xbf73,0x0,0xbf80bf80,0xbf80bf80,0xbf80,0x0,
    0x3f800000,0x3f800000,0x3f800000,0x3f800000,0x3f800000,0x3f7ff35e,0x3f7fcd79,0x3f7f8e56,0x3f800000,0x3f7fcd79,0x3f7f35f9,0x3f7e39bc,0x3f800000,0x3f7f8e56,0x3f7e39bc,0x3f7c0361,
    0x3f800000,0x3f7f35f9,0x3f7cd925,0x3f78ed3c,0x3f800000,0x3f7ec46d,0x3f7b14be,0x3f74fa0b,0x3f800000,0x3f7e39bc,0x3f78ed3c,0x3f702d4f,0x3f800000,0x3f7d95f4,0x3f766377,0x3f6a8b4b,
    0x3f800000,0x3f7cd925,0x3f737871,0x3f641901,0x3f800000,0x3f7c0361,0x3f702d4f,0x3f5cdc29,0x3f800000,0x3f7b14be,0x3f6c835e,0x3f54db31,0x3f800000,0x3f7a0d54,0x3f687c12,0x3f4c1d35,
    0x3f800000,0x3f78ed3c,0x3f641901,0x3f42a9f7,0x3f800000,0x3f77b493,0x3f5f5be6,0x3f3889dc,0x3f800000,0x3f766377,0x3f5a46a0,0x3f2dc5e3,0x3f800000,0x3f74fa0b,0x3f54db31,0x3f226799,
    0x3f800000,0x3f800000,0x3f800000,0x3f800000,0x3f7f35f9,0x3f7ec46d,0x3f7e39bc,0x3f7d95f4,0x3f7cd925,0x3f7b14be,0x3f78ed3c,0x3f766377,0x3f78ed3c,0x3f74fa0b,0x3f702d4f,0x3f6a8b4b,
    0x3f737871,0x3f6c835e,0x3f641901,0x3f5a46a0,0x3f6c835e,0x3f61c598,0x3f54db31,0x3f45e403,0x3f641901,0x3f54db31,0x3f42a9f7,0x3f2dc5e3,0x3f5a46a0,0x3f45e403,0x3f2dc5e3,0x3f1260b2,
    0x3f4f1bbd,0x3f3504f3,0x3f167918,0x3ee87171,0x3f42a9f7,0x3f226799,0x3efa2c90,0x3ea7bf25,0x3f3504f3,0x3f0e39da,0x3ec3ef15,0x3e47c5c2,0x3f26423a,0x3ef15aea,0x3e8afa3e,0x3d712283,
    0x3f167918,0x3ec3ef15,0x3e20305b,0xbda0af2a,0x3f05c277,0x3e94a031,0x3d20cee7,0xbe5b73dc,0x3ee87171,0x3e47c5c2,0xbda0af2a,0xbeb13641,0x3ec3ef15,0x3dc8bd36,0xbe47c5c2,0xbef15aea,
    0x3f800000,0x3f800000,0x3f800000,0x3f800000,0x3f7cd925,0x3f7c0361,0x3f7b14be,0x3f7a0d54,0x3f737871,0x3f702d4f,0x3f6c835e,0x3f687c12,0x3f641901,0x3f5cdc29,0x3f54db31,0x3f4c1d35,
    0x3f4f1bbd,0x3f42a9f7,0x3f3504f3,0x3f26423a,0x3f3504f3,0x3f226799,0x3f0e39da,0x3ef15aea,0x3f167918,0x3efa2c90,0x3ec3ef15,0x3e8afa3e,0x3ee87171,0x3ea7bf25,0x3e47c5c2,0x3d712283,
    0x3e9e377a,0x3e20305b,0x248d3132,0xbe20305b,0x3e20305b,0xbca0d6d6,0xbe47c5c2,0xbeba9be1,0x248d3132,0xbe47c5c2,0xbec3ef15,0xbf0e39da,0xbe20305b,0xbeba9be1,0xbf0e39da,0xbf3889dc,
    0xbe9e377a,0xbf05c277,0xbf3504f3,0xbf5a46a0,0xbee87171,0xbf2a0c73,0xbf54db31,0xbf71decf,0xbf167918,0xbf490a88,0xbf6c835e,0xbf7e39bc,0xbf3504f3,0xbf61c598,0xbf7b14be,0xbf7ec46d,
    0x3f800000,0x3f800000,0x3f800000,0x3f800000,0x3f78ed3c,0x3f77b493,0x3f766377,0x3f74fa0b,0x3f641901,0x3f5f5be6,0x3f5a46a0,0x3f54db31,0x3f42a9f7,0x3f3889dc,0x3f2dc5e3,0x3f226799,
    0x3f167918,0x3f05c277,0x3ee87171,0x3ec3ef15,0x3ec3ef15,0x3e94a031,0x3e47c5c2,0x3dc8bd36,0x3e20305b,0x3d20cee7,0xbda0af2a,0xbe47c5c2,0xbda0af2a,0xbe5b73dc,0xbeb13641,0xbef15aea,
    0xbe9e377a,0xbee87171,0xbf167918,0xbf3504f3,0xbf05c277,0xbf2a0c73,0xbf490a88,0xbf61c598,0xbf3504f3,0xbf54db31,0xbf6c835e,0xbf7b14be,0xbf5a46a0,0xbf71decf,0xbf7e39bc,0xbf7ec46d,
    0xbf737871,0xbf7f35f9,0xbf7cd925,0xbf6c835e,0xbf7f35f9,0xbf7c0361,0xbf687c12,0xbf45e403,0xbf7cd925,0xbf687c12,0xbf42a9f7,0xbf0e39da,0xbf6c835e,0xbf45e403,0xbf0e39da,0xbe94a031,
    0x0,0x0,0x0,0x0,0x0,0x3ca0d6d6,0x3d20cee7,0x3d712283,0x0,0x3d20cee7,0x3da0af2a,0x3df0b772,0x0,0x3d712283,0x3df0b772,0x3e3403f1,
    0x0,0x3da0af2a,0x3e20305b,0x3e6f0c4d,0x0,0x3dc8bd36,0x3e47c5c2,0x3e94a031,0x0,0x3df0b772,0x3e6f0c4d,0x3eb13641,0x0,0x3e0c4cf7,0x3e8afa3e,0x3ecd2ef3,
    0x0,0x3e20305b,0x3e9e377a,0x3ee87171,0x0,0x3e3403f1,0x3eb13641,0x3f0172c3,0x0,0x3e47c5c2,0x3ec3ef15,0x3f0e39da,0x0,0x3e5b73dc,0x3ed65a92,0x3f1a82a4,
    0x0,0x3e6f0c4d,0x3ee87171,0x3f26423a,0x0,0x3e814693,0x3efa2c90,0x3f316e2c,0x0,0x3e8afa3e,0x3f05c277,0x3f3bfc8f,0x0,0x3e94a031,0x3f0e39da,0x3f45e403,
    0x0,0x0,0x0,0x0,0x3da0af2a,0x3dc8bd36,0x3df0b772,0x3e0c4cf7,0x3e20305b,0x3e47c5c2,0x3e6f0c4d,0x3e8afa3e,0x3e6f0c4d,0x3e94a031,0x3eb13641,0x3ecd2ef3,
    0x3e9e377a,0x3ec3ef15,0x3ee87171,0x3f05c277,0x3ec3ef15,0x3ef15aea,0x3f0e39da,0x3f226799,0x3ee87171,0x3f0e39da,0x3f26423a,0x3f3bfc8f,0x3f05c277,0x3f226799,0x3f3bfc8f,0x3f5205d4,
    0x3f167918,0x3f3504f3,0x3f4f1bbd,0x3f641901,0x3f26423a,0x3f45e403,0x3f5f5be6,0x3f71decf,0x3f3504f3,0x3f54db31,0x3f6c835e,0x3f7b14be,0x3f42a9f7,0x3f61c598,0x3f766377,0x3f7f8e56,
    0x3f4f1bbd,0x3f6c835e,0x3f7cd925,0x3f7f35f9,0x3f5a46a0,0x3f74fa0b,0x3f7fcd79,0x3f7a0d54,0x3f641901,0x3f7b14be,0x3f7f35f9,0x3f702d4f,0x3f6c835e,0x3f7ec46d,0x3f7b14be,0x3f61c598,
    0x0,0x0,0x0,0x0,0x3e20305b,0x3e3403f1,0x3e47c5c2,0x3e5b73dc,0x3e9e377a,0x3eb13641,0x3ec3ef15,0x3ed65a92,0x3ee87171,0x3f0172c3,0x3f0e39da,0x3f1a82a4,
    0x3f167918,0x3f26423a,0x3f3504f3,0x3f42a9f7,0x3f3504f3,0x3f45e403,0x3f54db31,0x3f61c598,0x3f4f1bbd,0x3f5f5be6,0x3f6c835e,0x3f766377,0x3f641901,0x3f71decf,0x3f7b14be,0x3f7f8e56,
    0x3f737871,0x3f7cd925,0x3f800000,0x3f7cd925,0x3f7cd925,0x3f7ff35e,0x3f7b14be,0x3f6e641a,0x3f800000,0x3f7b14be,0x3f6c835e,0x3f54db31,0x3f7cd925,0x3f6e641a,0x3f54db31,0x3f316e2c,
    0x3f737871,0x3f5a46a0,0x3f3504f3,0x3f05c277,0x3f641901,0x3f3f5cb5,0x3f0e39da,0x3ea7bf25,0x3f4f1bbd,0x3f1e7cf1,0x3ec3ef15,0x3df0b772,0x3f3504f3,0x3ef15aea,0x3e47c5c2,0xbdc8bd36,
    0x0,0x0,0x0,0x0,0x3e6f0c4d,0x3e814693,0x3e8afa3e,0x3e94a031,0x3ee87171,0x3efa2c90,0x3f05c277,0x3f0e39da,0x3f26423a,0x3f316e2c,0x3f3bfc8f,0x3f45e403,
    0x3f4f1bbd,0x3f5a46a0,0x3f641901,0x3f6c835e,0x3f6c835e,0x3f74fa0b,0x3f7b14be,0x3f7ec46d,0x3f7cd925,0x3f7fcd79,0x3f7f35f9,0x3f7b14be,0x3f7f35f9,0x3f7a0d54,0x3f702d4f,0x3f61c598,
    0x3f737871,0x3f641901,0x3f4f1bbd,0x3f3504f3,0x3f5a46a0,0x3f3f5cb5,0x3f1e7cf1,0x3ef15aea,0x3f3504f3,0x3f0e39da,0x3ec3ef15,0x3e47c5c2,0x3f05c277,0x3ea7bf25,0x3df0b772,0xbdc8bd36,
    0x3e9e377a,0x3da0af2a,0xbe20305b,0xbec3ef15,0x3da0af2a,0xbe3403f1,0xbed65a92,0xbf226799,0xbe20305b,0xbed65a92,0xbf26423a,0xbf54db31,0xbec3ef15,0xbf226799,0xbf54db31,0xbf74fa0b,
    0x3f800000,0x3f800000,0x3f800000,0x3f800000,0x3f7ffcd7,0x3f7fe394,0x3f7fb10f,0x3f7f654e,0x3f7ff35e,0x3f7f8e56,0x3f7ec46d,0x3f7d95f4,0x3f7fe394,0x3f7f0059,0x3f7d3aac,0x3f7a9421,
    0x3f7fcd79,0x3f7e39bc,0x3f7b14be,0x3f766377,0x3f7fb10f,0x3f7d3aac,0x3f7853f8,0x3f710908,0x3f7f8e56,0x3f7c0361,0x3f74fa0b,0x3f6a8b4b,0x3f7f654e,0x3f7a9421,0x3f710908,0x3f62f219,
    0x3f7f35f9,0x3f78ed3c,0x3f6c835e,0x3f5a46a0,0x3f7f0059,0x3f770f11,0x3f676bd8,0x3f50935b,0x3f7ec46d,0x3f74fa0b,0x3f61c598,0x3f45e403,0x3f7e8238,0x3f72ae9e,0x3f5b941a,0x3f3a4582,
    0x3f7e39bc,0x3f702d4f,0x3f54db31,0x3f2dc5e3,0x3f7deafa,0x3f6d76aa,0x3f4d9f02,0x3f207440,0x3f7d95f4,0x3f6a8b4b,0x3f45e403,0x3f1260b2,0x3f7d3aac,0x3f676bd8,0x3f3daef9,0x3f039c3d,
    0x3f800000,0x3f800000,0x3f800000,0x3f800000,0x3f7f0059,0x3f7e8238,0x3f7deafa,0x3f7d3aac,0x3f7c0361,0x3f7a0d54,0x3f77b493,0x3f74fa0b,0x3f770f11,0x3f72ae9e,0x3f6d76aa,0x3f676bd8,
    0x3f702d4f,0x3f687c12,0x3f5f5be6,0x3f54db31,0x3f676bd8,0x3f5b941a,0x3f4d9f02,0x3f3daef9,0x3f5cdc29,0x3f4c1d35,0x3f3889dc,0x3f226799,0x3f50935b,0x3f3a4582,0x3f207440,0x3f039c3d,
    0x3f42a9f7,0x3f26423a,0x3f05c277,0x3ec3ef15,0x3f333bc6,0x3f104f0e,0x3ed1c759,0x3e78cfcc,0x3f226799,0x3ef15aea,0x3e94a031,0x3dc8bd36,0x3f104f0e,0x3ebf47d7,0x3e2a1c3f,0xbd48fb30,
    0x3efa2c90,0x3e8afa3e,0x3d20cee7,0xbe47c5c2,0x3ed1c759,0x3e2a1c3f,0xbdb4b86a,0xbeac7cd4,0x3ea7bf25,0x3d712283,0xbe5b73dc,0xbef15aea,0x3e78cfcc,0xbd48fb30,0xbeac7cd4,0xbf187fc0,
    0x3f800000,0x3f800000,0x3f800000,0x3f800000,0x3f7c7160,0x3f7b8f2a,0x3f7a9421,0x3f79805c,0x3f71decf,0x3f6e641a,0x3f6a8b4b,0x3f6655e7,0x3f609384,0x3f58f3c4,0x3f50935b,0x3f4779bc,
    0x3f490a88,0x3f3bfc8f,0x3f2dc5e3,0x3f1e7cf1,0x3f2beb4a,0x3f187fc0,0x3f039c3d,0x3edae880,0x3f0a04f8,0x3edf7108,0x3ea7bf25,0x3e5b73dc,0x3ec8917e,0x3e862210,0x3e0255f4,0xbc20d8d2,
    0x3e6f0c4d,0x3da0af2a,0xbda0af2a,0xbe6f0c4d,0x3d8ca1f2,0xbddcbd0d,0xbe8fcefe,0xbee3f40d,0xbdc8bd36,0xbe94a031,0xbef15aea,0xbf226799,0xbe862210,0xbeece91a,0xbf2456f1,0xbf4a965e,
    0xbed65a92,0xbf1e7cf1,0xbf490a88,0xbf687c12,0xbf104f0e,0xbf4105b8,0xbf653a48,0xbf7a9421,0xbf316e2c,0xbf5cdc29,0xbf77b493,0xbf7ff35e,0xbf4d9f02,0xbf710908,0xbf7fb10f,0xbf7853f8,
    0x3f800000,0x3f800000,0x3f800000,0x3f800000,0x3f7853f8,0x3f770f11,0x3f75b1c9,0x3f743c41,0x3f61c598,0x3f5cdc29,0x3f579b8c,0x3f5205d4,0x3f3daef9,0x3f333bc6,0x3f28296a,0x3f1c81b9,
    0x3f0e39da,0x3efa2c90,0x3ed65a92,0x3eb13641,0x3eac7cd4,0x3e78cfcc,0x3e164083,0x3d48fb30,0x3dc8bd36,0xbca0d6d6,0xbe0c4cf7,0xbe814693,0xbe164083,0xbe8fcefe,0xbed1c759,0xbf07e565,
    0xbec3ef15,0xbf05c277,0xbf26423a,0xbf42a9f7,0xbf187fc0,0xbf3a4582,0xbf563e03,0xbf6b8a3d,0xbf45e403,0xbf61c598,0xbf74fa0b,0xbf7ec46d,0xbf676bd8,0xbf79805c,0xbf7ffcd7,0xbf7a9421,
    0xbf7b14be,0xbf7fcd79,0xbf766377,0xbf5f5be6,0xbf7fb10f,0xbf743c41,0xbf58f3c4,0xbf2f9c32,0xbf74fa0b,0xbf579b8c,0xbf2a0c73,0xbedf7108,0xbf5b941a,0xbf2beb4a,0xbedae880,0xbe164083,
    0x0,0x0,0x0,0x0,0x3c20d8d2,0x3cf13d4c,0x3d48fb30,0x3d8ca1f2,0x3ca0d6d6,0x3d712283,0x3dc8bd36,0x3e0c4cf7,0x3cf13d4c,0x3db4b86a,0x3e164083,0x3e519f65,
    0x3d20cee7,0x3df0b772,0x3e47c5c2,0x3e8afa3e,0x3d48fb30,0x3e164083,0x3e78cfcc,0x3eac7cd4,0x3d712283,0x3e3403f1,0x3e94a031,0x3ecd2ef3,0x3d8ca1f2,0x3e519f65,0x3eac7cd4,0x3eece91a,
    0x3da0af2a,0x3e6f0c4d,0x3ec3ef15,0x3f05c277,0x3db4b86a,0x3e862210,0x3edae880,0x3f146eba,0x3dc8bd36,0x3e94a031,0x3ef15aea,0x3f226799,0x3ddcbd0d,0x3ea2fd52,0x3f039c3d,0x3f2f9c32,
    0x3df0b772,0x3eb13641,0x3f0e39da,0x3f3bfc8f,0x3e0255f4,0x3ebf47d7,0x3f187fc0,0x3f4779bc,0x3e0c4cf7,0x3ecd2ef3,0x3f226799,0x3f5205d4,0x3e164083,0x3edae880,0x3f2beb4a,0x3f5b941a,
    0x0,0x0,0x0,0x0,0x3db4b86a,0x3ddcbd0d,0x3e0255f4,0x3e164083,0x3e3403f1,0x3e5b73dc,0x3e814693,0x3e94a031,0x3e862210,0x3ea2fd52,0x3ebf47d7,0x3edae880,
    0x3eb13641,0x3ed65a92,0x3efa2c90,0x3f0e39da,0x3edae880,0x3f039c3d,0x3f187fc0,0x3f2beb4a,0x3f0172c3,0x3f1a82a4,0x3f316e2c,0x3f45e403,0x3f146eba,0x3f2f9c32,0x3f4779bc,0x3f5b941a,
    0x3f26423a,0x3f42a9f7,0x3f5a46a0,0x3f6c835e,0x3f36c9a9,0x3f53731f,0x3f698690,0x3f7853f8,0x3f45e403,0x3f61c598,0x3f74fa0b,0x3f7ec46d,0x3f53731f,0x3f6d76aa,0x3f7c7160,0x3f7fb10f,
    0x3f5f5be6,0x3f766377,0x3f7fcd79,0x3f7b14be,0x3f698690,0x3f7c7160,0x3f7f0059,0x3f710908,0x3f71decf,0x3f7f8e56,0x3f7a0d54,0x3f61c598,0x3f7853f8,0x3f7fb10f,0x3f710908,0x3f4d9f02,
    0x0,0x0,0x0,0x0,0x3e2a1c3f,0x3e3de731,0x3e519f65,0x3e6542e9,0x3ea7bf25,0x3eba9be1,0x3ecd2ef3,0x3edf7108,0x3ef5c6c5,0x3f07e565,0x3f146eba,0x3f207440,
    0x3f1e7cf1,0x3f2dc5e3,0x3f3bfc8f,0x3f490a88,0x3f3daef9,0x3f4d9f02,0x3f5b941a,0x3f676bd8,0x3f579b8c,0x3f6655e7,0x3f71decf,0x3f7a0d54,0x3f6b8a3d,0x3f770f11,0x3f7deafa,0x3f7ffcd7,
    0x3f78ed3c,0x3f7f35f9,0x3f7f35f9,0x3f78ed3c,0x3f7f654e,0x3f7e8238,0x3f75b1c9,0x3f653a48,0x3f7ec46d,0x3f74fa0b,0x3f61c598,0x3f45e403,0x3f770f11,0x3f62f219,0x3f444969,0x3f1c81b9,
    0x3f687c12,0x3f490a88,0x3f1e7cf1,0x3ed65a92,0x3f53731f,0x3f28296a,0x3ee3f40d,0x3e519f65,0x3f3889dc,0x3f0172c3,0x3e814693,0xbca0d6d6,0x3f187fc0,0x3eac7cd4,0x3d48fb30,0xbe78cfcc,
    0x0,0x0,0x0,0x0,0x3e78cfcc,0x3e862210,0x3e8fcefe,0x3e996dba,0x3ef15aea,0x3f0172c3,0x3f0a04f8,0x3f1260b2,0x3f2beb4a,0x3f36c9a9,0x3f4105b8,0x3f4a965e,
    0x3f54db31,0x3f5f5be6,0x3f687c12,0x3f702d4f,0x3f710908,0x3f7853f8,0x3f7d3aac,0x3f7fb10f,0x3f7ec46d,0x3f7ff35e,0x3f7d95f4,0x3f77b493,0x3f7d3aac,0x3f75b1c9,0x3f698690,0x3f58f3c4,
    0x3f6c835e,0x3f5a46a0,0x3f42a9f7,0x3f26423a,0x3f4d9f02,0x3f2f9c32,0x3f0c2124,0x3ec8917e,0x3f226799,0x3ef15aea,0x3e94a031,0x3dc8bd36,0x3edae880,0x3e6542e9,0x3c20d8d2,0xbe519f65,
    0x3e47c5c2,0xbd20cee7,0xbe8afa3e,0xbefa2c90,0xbd48fb30,0xbe996dba,0xbf07e565,0xbf3a4582,0xbe94a031,0xbf0a04f8,0xbf3f5cb5,0xbf6655e7,0xbf039c3d,0xbf3daef9,0xbf676bd8,0xbf7d3aac,
    0x3f80,0x3f80,0x3f80,0x3f80,0x3f80,0x3ec43f6d,0x3f353f35,0x3f6d3ec4,0x3f80,0x3f353f35,0x3f80248d,0x3f35bf35,0x3f80,0x3f6d3ec4,0x3f35bf35,0xbec4bf6d,
    0x3f80,0x3f80248d,0x250dbf80,0xbf80a554,0x3f80,0x3f6dbec4,0xbf35bf35,0xbec43f6d,0x3f80,0x3f35bf35,0xbf80a554,0x3f353f35,0x3f80,0x3ec4bf6d,0xbf353f35,0x3f6dbec4,
    0x3f80,0x250dbf80,0x3f80,0x250dbf80,0x3f80,0xbec4bf6d,0x3f353f35,0xbf6dbec4,0x3f80,0xbf35bf35,0x3f80248d,0xbf353f35,0x3f80,0xbf6dbec4,0x3f35bf35,0x3ec43f6d,
    0x3f80,0xbf80a554,0x250dbf80,0x3f80248d,0x3f80,0xbf6d3ec4,0xbf35bf35,0x3ec4bf6d,0x3f80,0xbf353f35,0xbf80a554,0xbf35bf35,0x3f80,0xbec43f6d,0xbf353f35,0xbf6d3ec4,
    0x3f80,0x3f80,0x3f80,0x3f80,0x3f80248d,0x3f6dbec4,0x3f35bf35,0x3ec4bf6d,0x250dbf80,0xbf35bf35,0xbf80a554,0xbf353f35,0xbf80a554,0xbec43f6d,0x3f353f35,0x3f6dbec4,
    0x3f80,0x3f80248d,0x250dbf80,0xbf80a554,0x3f80248d,0xbec4bf6d,0xbf353f35,0x3f6d3ec4,0x250dbf80,0xbf353f35,0x3f80248d,0xbf35bf35,0xbf80a554,0x3f6d3ec4,0xbf35bf35,0x3ec43f6d,
    0x3f80,0x250dbf80,0x3f80,0x250dbf80,0x3f80248d,0xbf6d3ec4,0x3f35bf35,0xbec43f6d,0x250dbf80,0x3f353f35,0xbf80a554,0x3f35bf35,0xbf80a554,0x3ec4bf6d,0x3f353f35,0xbf6d3ec4,
    0x3f80,0xbf80a554,0x250dbf80,0x3f80248d,0x3f80248d,0x3ec43f6d,0xbf353f35,0xbf6dbec4,0x250dbf80,0x3f35bf35,0x3f80248d,0x3f353f35,0xbf80a554,0xbf6dbec4,0xbf35bf35,0xbec4bf6d,
    0x3f80,0x3f80,0x3f80,0x3f80,0x250dbf80,0xbec4bf6d,0xbf35bf35,0xbf6dbec4,0x3f80,0x3f353f35,0x3f80248d,0x3f35bf35,0x250dbf80,0xbf6dbec4,0xbf353f35,0x3ec43f6d,
    0x3f80,0x3f80248d,0x250dbf80,0xbf80a554,0x250dbf80,0xbf6d3ec4,0x3f353f35,0x3ec4bf6d,0x3f80,0x3f35bf35,0xbf80a554,0x3f353f35,0x250dbf80,0xbec43f6d,0x3f35bf35,0xbf6d3ec4,
    0x3f80,0x250dbf80,0x3f80,0x250dbf80,0x250dbf80,0x3ec43f6d,0xbf35bf35,0x3f6d3ec4,0x3f80,0xbf35bf35,0x3f80248d,0xbf353f35,0x250dbf80,0x3f6d3ec4,0xbf353f35,0xbec4bf6d,
    0x3f80,0xbf80a554,0x250dbf80,0x3f80248d,0x250dbf80,0x3f6dbec4,0x3f353f35,0xbec43f6d,0x3f80,0xbf353f35,0xbf80a554,0xbf35bf35,0x250dbf80,0x3ec4bf6d,0x3f35bf35,0x3f6dbec4,
    0x3f80,0x3f80,0x3f80,0x3f80,0xbf80a554,0xbf6d3ec4,0xbf353f35,0xbec43f6d,0x250dbf80,0xbf35bf35,0xbf80a554,0xbf353f35,0x3f80248d,0x3ec4bf6d,0xbf35bf35,0xbf6d3ec4,
    0x3f80,0x3f80248d,0x250dbf80,0xbf80a554,0xbf80a554,0x3ec43f6d,0x3f35bf35,0xbf6dbec4,0x250dbf80,0xbf353f35,0x3f80248d,0xbf35bf35,0x3f80248d,0xbf6dbec4,0x3f353f35,0xbec4bf6d,
    0x3f80,0x250dbf80,0x3f80,0x250dbf80,0xbf80a554,0x3f6dbec4,0xbf353f35,0x3ec4bf6d,0x250dbf80,0x3f353f35,0xbf80a554,0x3f35bf35,0x3f80248d,0xbec43f6d,0xbf35bf35,0x3f6dbec4,
    0x3f80,0xbf80a554,0x250dbf80,0x3f80248d,0xbf80a554,0xbec4bf6d,0x3f35bf35,0x3f6d3ec4,0x250dbf80,0x3f35bf35,0x3f80248d,0x3f353f35,0x3f80248d,0x3f6d3ec4,0x3f353f35,0x3ec43f6d,
    0x3f808000,0x3f808000,0x3f808000,0x3f808000,0x3f808000,0x3f6dbec4,0x3f35bf35,0x3ec4bf6d,0x3f808000,0x3f35bf35,0x248dbf80,0xbf35bf35,0x3f808000,0x3ec4bf6d,0xbf35bf35,0xbf6d3ec4,
    0x3f808000,0x248dbf80,0xbf80a50d,0xa5543f80,0x3f808000,0xbec4bf6d,0xbf353f35,0x3f6d3ec4,0x3f808000,0xbf35bf35,0xa5543f80,0x3f35bf35,0x3f808000,0xbf6dbec4,0x3f353f35,0xbec4bf6d,
    0x3f808000,0xbf80a50d,0x3f808000,0xbf80a50d,0x3f808000,0xbf6d3ec4,0x3f35bf35,0xbec43f6d,0x3f808000,0xbf353f35,0x248dbf80,0x3f353f35,0x3f808000,0xbec43f6d,0xbf35bf35,0x3f6dbec4,
    0x3f808000,0xa5543f80,0xbf80a50d,0x248dbf80,0x3f808000,0x3ec43f6d,0xbf353f35,0xbf6dbec4,0x3f808000,0x3f353f35,0xa5543f80,0xbf353f35,0x3f808000,0x3f6d3ec4,0x3f353f35,0x3ec43f6d,
    0x3f808000,0x3f808000,0x3f808000,0x3f808000,0x248dbf80,0xbec4bf6d,0xbf35bf35,0xbf6dbec4,0xbf80a50d,0xbf353f35,0xa5543f80,0x3f353f35,0xa5543f80,0x3f6d3ec4,0x3f35bf35,0xbec4bf6d,
    0x3f808000,0x248dbf80,0xbf80a50d,0xa5543f80,0x248dbf80,0xbf6d3ec4,0x3f353f35,0x3ec4bf6d,0xbf80a50d,0x3f353f35,0x248dbf80,0xbf353f35,0xa5543f80,0x3ec4bf6d,0xbf353f35,0x3f6dbec4,
    0x3f808000,0xbf80a50d,0x3f808000,0xbf80a50d,0x248dbf80,0x3ec43f6d,0xbf35bf35,0x3f6d3ec4,0xbf80a50d,0x3f35bf35,0xa5543f80,0xbf35bf35,0xa5543f80,0xbf6dbec4,0x3f35bf35,0x3ec43f6d,
    0x3f808000,0xa5543f80,0xbf80a50d,0x248dbf80,0x248dbf80,0x3f6dbec4,0x3f353f35,0xbec43f6d,0xbf80a50d,0xbf35bf35,0x248dbf80,0x3f35bf35,0xa5543f80,0xbec43f6d,0xbf353f35,0xbf6d3ec4,
    0x3f808000,0x3f808000,0x3f808000,0x3f808000,0xbf80a50d,0xbf6d3ec4,0xbf353f35,0xbec43f6d,0x3f808000,0x3f35bf35,0x248dbf80,0xbf35bf35,0xbf80a50d,0xbec43f6d,0x3f353f35,0x3f6dbec4,
    0x3f808000,0x248dbf80,0xbf80a50d,0xa5543f80,0xbf80a50d,0x3ec43f6d,0x3f35bf35,0xbf6dbec4,0x3f808000,0xbf35bf35,0xa5543f80,0x3f35bf35,0xbf80a50d,0x3f6d3ec4,0xbf35bf35,0x3ec43f6d,
    0x3f808000,0xbf80a50d,0x3f808000,0xbf80a50d,0xbf80a50d,0x3f6dbec4,0xbf353f35,0x3ec4bf6d,0x3f808000,0xbf353f35,0x248dbf80,0x3f353f35,0xbf80a50d,0x3ec4bf6d,0x3f353f35,0xbf6d3ec4,
    0x3f808000,0xa5543f80,0xbf80a50d,0x248dbf80,0xbf80a50d,0xbec4bf6d,0x3f35bf35,0x3f6d3ec4,0x3f808000,0x3f353f35,0xa5543f80,0xbf353f35,0xbf80a50d,0xbf6dbec4,0xbf35bf35,0xbec4bf6d,
    0x3f808000,0x3f808000,0x3f808000,0x3f808000,0xa5543f80,0x3ec43f6d,0x3f353f35,0x3f6d3ec4,0xbf80a50d,0xbf353f35,0xa5543f80,0x3f353f35,0x248dbf80,0xbf6dbec4,0xbf353f35,0x3ec43f6d,
    0x3f808000,0x248dbf80,0xbf80a50d,0xa5543f80,0xa5543f80,0x3f6dbec4,0xbf35bf35,0xbec43f6d,0xbf80a50d,0x3f353f35,0x248dbf80,0xbf353f35,0x248dbf80,0xbec43f6d,0x3f35bf35,0xbf6d3ec4,
    0x3f808000,0xbf80a50d,0x3f808000,0xbf80a50d,0xa5543f80,0xbec4bf6d,0x3f353f35,0xbf6dbec4,0xbf80a50d,0x3f35bf35,0xa5543f80,0xbf35bf35,0x248dbf80,0x3f6d3ec4,0xbf353f35,0xbec4bf6d,
    0x3f808000,0xa5543f80,0xbf80a50d,0x248dbf80,0xa5543f80,0xbf6d3ec4,0xbf35bf35,0x3ec4bf6d,0xbf80a50d,0xbf35bf35,0x248dbf80,0x3f35bf35,0x248dbf80,0x3ec4bf6d,0x3f35bf35,0x3f6dbec4,
    0x3f80,0x3f80,0x3f80,0x3f80,0x3e483f7b,0x3f0e3f55,0x3f553f0e,0x3f7b3e48,0x3ec43f6d,0x3f6d3ec4,0x3f6dbec4,0x3ec4bf6d,0x3f0e3f55,0x3f7bbe48,0x3e48bf7b,0xbf55bf0e,
    0x3f353f35,0x3f35bf35,0xbf35bf35,0xbf353f35,0x3f553f0e,0x3e48bf7b,0xbf7b3e48,0x3f0e3f55,0x3f6d3ec4,0xbec4bf6d,0xbec43f6d,0x3f6dbec4,0x3f7b3e48,0xbf55bf0e,0x3f0e3f55,0xbe48bf7b,
    0x3f80248d,0xbf80a554,0x3f80248d,0xbf80a554,0x3f7bbe48,0xbf553f0e,0x3f0ebf55,0xbe483f7b,0x3f6dbec4,0xbec43f6d,0xbec4bf6d,0x3f6d3ec4,0x3f55bf0e,0x3e483f7b,0xbf7bbe48,0x3f0ebf55,
    0x3f35bf35,0x3f353f35,0xbf353f35,0xbf35bf35,0x3f0ebf55,0x3f7b3e48,0x3e483f7b,0xbf553f0e,0x3ec4bf6d,0x3f6dbec4,0x3f6d3ec4,0x3ec43f6d,0x3e48bf7b,0x3f0ebf55,0x3f55bf0e,0x3f7bbe48,
    0x3f80,0x3f80,0x3f80,0x3f80,0x3f7bbe48,0x3f55bf0e,0x3f0ebf55,0x3e48bf7b,0xbec4bf6d,0xbf6dbec4,0xbf6d3ec4,0xbec43f6d,0xbf553f0e,0x3e483f7b,0x3f7b3e48,0x3f0ebf55,
    0x3f353f35,0x3f35bf35,0xbf35bf35,0xbf353f35,0x3f0ebf55,0xbf7bbe48,0x3e483f7b,0x3f55bf0e,0xbf6dbec4,0x3ec43f6d,0x3ec4bf6d,0xbf6d3ec4,0xbe483f7b,0x3f0ebf55,0xbf553f0e,0x3f7bbe48,
    0x3f80248d,0xbf80a554,0x3f80248d,0xbf80a554,0xbe48bf7b,0x3f0e3f55,0xbf55bf0e,0x3f7b3e48,0xbf6d3ec4,0x3ec4bf6d,0x3ec43f6d,0xbf6dbec4,0x3f0e3f55,0xbf7b3e48,0x3e48bf7b,0x3f553f0e,
    0x3f35bf35,0x3f353f35,0xbf353f35,0xbf35bf35,0xbf55bf0e,0x3e48bf7b,0x3f7bbe48,0x3f0e3f55,0xbec43f6d,0xbf6d3ec4,0xbf6dbec4,0xbec4bf6d,0x3f7b3e48,0x3f553f0e,0x3f0e3f55,0x3e483f7b,
    0x3f80,0x3f80,0x3f80,0x3f80,0xbe48bf7b,0xbf0ebf55,0xbf55bf0e,0xbf7bbe48,0x3ec43f6d,0x3f6d3ec4,0x3f6dbec4,0x3ec4bf6d,0xbf0ebf55,0xbf7b3e48,0xbe483f7b,0x3f553f0e,
    0x3f353f35,0x3f35bf35,0xbf35bf35,0xbf353f35,0xbf55bf0e,0xbe483f7b,0x3f7bbe48,0xbf0ebf55,0x3f6d3ec4,0xbec4bf6d,0xbec43f6d,0x3f6dbec4,0xbf7bbe48,0x3f553f0e,0xbf0ebf55,0x3e483f7b,
    0x3f80248d,0xbf80a554,0x3f80248d,0xbf80a554,0xbf7b3e48,0x3f55bf0e,0xbf0e3f55,0x3e48bf7b,0x3f6dbec4,0xbec43f6d,0xbec4bf6d,0x3f6d3ec4,0xbf553f0e,0xbe48bf7b,0x3f7b3e48,0xbf0e3f55,
    0x3f35bf35,0x3f353f35,0xbf353f35,0xbf35bf35,0xbf0e3f55,0xbf7bbe48,0xbe48bf7b,0x3f55bf0e,0x3ec4bf6d,0x3f6dbec4,0x3f6d3ec4,0x3ec43f6d,0xbe483f7b,0xbf0e3f55,0xbf553f0e,0xbf7b3e48,
    0x3f80,0x3f80,0x3f80,0x3f80,0xbf7b3e48,0xbf553f0e,0xbf0e3f55,0xbe483f7b,0xbec4bf6d,0xbf6dbec4,0xbf6d3ec4,0xbec43f6d,0x3f55bf0e,0xbe48bf7b,0xbf7bbe48,0xbf0e3f55,
    0x3f353f35,0x3f35bf35,0xbf35bf35,0xbf353f35,0xbf0e3f55,0x3f7b3e48,0xbe48bf7b,0xbf553f0e,0xbf6dbec4,0x3ec43f6d,0x3ec4bf6d,0xbf6d3ec4,0x3e48bf7b,0xbf0e3f55,0x3f55bf0e,0xbf7b3e48,
    0x3f80248d,0xbf80a554,0x3f80248d,0xbf80a554,0x3e483f7b,0xbf0ebf55,0x3f553f0e,0xbf7bbe48,0xbf6d3ec4,0x3ec4bf6d,0x3ec43f6d,0xbf6dbec4,0xbf0ebf55,0x3f7bbe48,0xbe483f7b,0xbf55bf0e,
    0x3f35bf35,0x3f353f35,0xbf353f35,0xbf35bf35,0x3f553f0e,0xbe483f7b,0xbf7b3e48,0xbf0ebf55,0xbec43f6d,0xbf6d3ec4,0xbf6dbec4,0xbec4bf6d,0xbf7bbe48,0xbf55bf0e,0xbf0ebf55,0xbe48bf7b,
    0x3f808000,0x3f808000,0x3f808000,0x3f808000,0x3f7bbe48,0x3f55bf0e,0x3f0ebf55,0x3e48bf7b,0x3f6dbec4,0x3ec4bf6d,0xbec4bf6d,0xbf6dbec4,0x3f55bf0e,0xbe48bf7b,0xbf7bbe48,0xbf0e3f55,
    0x3f35bf35,0xbf35bf35,0xbf353f35,0x3f353f35,0x3f0ebf55,0xbf7bbe48,0x3e483f7b,0x3f55bf0e,0x3ec4bf6d,0xbf6d3ec4,0x3f6d3ec4,0xbec4bf6d,0x3e48bf7b,0xbf0e3f55,0x3f55bf0e,0xbf7b3e48,
    0x248dbf80,0xa5543f80,0x248dbf80,0xa5543f80,0xbe48bf7b,0x3f0e3f55,0xbf55bf0e,0x3f7b3e48,0xbec4bf6d,0x3f6d3ec4,0xbf6d3ec4,0x3ec4bf6d,0xbf0ebf55,0x3f7bbe48,0xbe483f7b,0xbf55bf0e,
    0xbf35bf35,0x3f35bf35,0x3f353f35,0xbf353f35,0xbf55bf0e,0x3e48bf7b,0x3f7bbe48,0x3f0e3f55,0xbf6dbec4,0xbec4bf6d,0x3ec4bf6d,0x3f6dbec4,0xbf7bbe48,0xbf55bf0e,0xbf0ebf55,0xbe48bf7b,
    0x3f808000,0x3f808000,0x3f808000,0x3f808000,0xbe48bf7b,0xbf0ebf55,0xbf55bf0e,0xbf7bbe48,0xbf6d3ec4,0xbec43f6d,0x3ec43f6d,0x3f6d3ec4,0x3f0e3f55,0x3f7bbe48,0x3e48bf7b,0xbf55bf0e,
    0x3f35bf35,0xbf35bf35,0xbf353f35,0x3f353f35,0xbf55bf0e,0xbe483f7b,0x3f7bbe48,0xbf0ebf55,0xbec43f6d,0x3f6dbec4,0xbf6dbec4,0x3ec43f6d,0x3f7b3e48,0xbf55bf0e,0x3f0e3f55,0xbe48bf7b,
    0x248dbf80,0xa5543f80,0x248dbf80,0xa5543f80,0xbf7b3e48,0x3f55bf0e,0xbf0e3f55,0x3e48bf7b,0x3ec43f6d,0xbf6dbec4,0x3f6dbec4,0xbec43f6d,0x3f55bf0e,0x3e483f7b,0xbf7bbe48,0x3f0ebf55,
    0xbf35bf35,0x3f35bf35,0x3f353f35,0xbf353f35,0xbf0e3f55,0xbf7bbe48,0xbe48bf7b,0x3f55bf0e,0x3f6d3ec4,0x3ec43f6d,0xbec43f6d,0xbf6d3ec4,0x3e48bf7b,0x3f0ebf55,0x3f55bf0e,0x3f7bbe48,
    0x3f808000,0x3f808000,0x3f808000,0x3f808000,0xbf7b3e48,0xbf553f0e,0xbf0e3f55,0xbe483f7b,0x3f6dbec4,0x3ec4bf6d,0xbec4bf6d,0xbf6dbec4,0xbf553f0e,0x3e483f7b,0x3f7b3e48,0x3f0ebf55,
    0x3f35bf35,0xbf35bf35,0xbf353f35,0x3f353f35,0xbf0e3f55,0x3f7b3e48,0xbe48bf7b,0xbf553f0e,0x3ec4bf6d,0xbf6d3ec4,0x3f6d3ec4,0xbec4bf6d,0xbe483f7b,0x3f0ebf55,0xbf553f0e,0x3f7bbe48,
    0x248dbf80,0xa5543f80,0x248dbf80,0xa5543f80,0x3e483f7b,0xbf0ebf55,0x3f553f0e,0xbf7bbe48,0xbec4bf6d,0x3f6d3ec4,0xbf6d3ec4,0x3ec4bf6d,0x3f0e3f55,0xbf7b3e48,0x3e48bf7b,0x3f553f0e,
    0xbf35bf35,0x3f35bf35,0x3f353f35,0xbf353f35,0x3f553f0e,0xbe483f7b,0xbf7b3e48,0xbf0ebf55,0xbf6dbec4,0xbec4bf6d,0x3ec4bf6d,0x3f6dbec4,0x3f7b3e48,0x3f553f0e,0x3f0e3f55,0x3e483f7b,
    0x3f808000,0x3f808000,0x3f808000,0x3f808000,0x3e483f7b,0x3f0e3f55,0x3f553f0e,0x3f7b3e48,0xbf6d3ec4,0xbec43f6d,0x3ec43f6d,0x3f6d3ec4,0xbf0ebf55,0xbf7b3e48,0xbe483f7b,0x3f553f0e,
    0x3f35bf35,0xbf35bf35,0xbf353f35,0x3f353f35,0x3f553f0e,0x3e48bf7b,0xbf7b3e48,0x3f0e3f55,0xbec43f6d,0x3f6dbec4,0xbf6dbec4,0x3ec43f6d,0xbf7bbe48,0x3f553f0e,0xbf0ebf55,0x3e483f7b,
    0x248dbf80,0xa5543f80,0x248dbf80,0xa5543f80,0x3f7bbe48,0xbf553f0e,0x3f0ebf55,0xbe483f7b,0x3ec43f6d,0xbf6dbec4,0x3f6dbec4,0xbec43f6d,0xbf553f0e,0xbe48bf7b,0x3f7b3e48,0xbf0e3f55,
    0xbf35bf35,0x3f35bf35,0x3f353f35,0xbf353f35,0x3f0ebf55,0x3f7b3e48,0x3e483f7b,0xbf553f0e,0x3f6d3ec4,0x3ec43f6d,0xbec43f6d,0xbf6d3ec4,0xbe483f7b,0xbf0e3f55,0xbf553f0e,0xbf7b3e48,
    0x3f800000,0x3f800000,0x3f800000,0x3f800000,0x3f800000,0x3f7fec43,0x3f7fb10f,0x3f7f4e6d,0x3f800000,0x3f7fb10f,0x3f7ec46d,0x3f7d3aac,0x3f800000,0x3f7f4e6d,0x3f7d3aac,0x3f79c79d,
    0x3f800000,0x3f7ec46d,0x3f7b14be,0x3f74fa0b,0x3f800000,0x3f7e1324,0x3f7853f8,0x3f6ed89e,0x3f800000,0x3f7d3aac,0x3f74fa0b,0x3f676bd8,0x3f800000,0x3f7c3b28,0x3f710908,0x3f5ebe05,
    0x3f800000,0x3f7b14be,0x3f6c835e,0x3f54db31,0x3f800000,0x3f79c79d,0x3f676bd8,0x3f49d112,0x3f800000,0x3f7853f8,0x3f61c598,0x3f3daef9,0x3f800000,0x3f76ba07,0x3f5b941a,0x3f3085bb,
    0x3f800000,0x3f74fa0b,0x3f54db31,0x3f226799,0x3f800000,0x3f731447,0x3f4d9f02,0x3f13682a,0x3f800000,0x3f710908,0x3f45e403,0x3f039c3d,0x3f800000,0x3f6ed89e,0x3f3daef9,0x3ee63375,
    0x3f800000,0x3f800000,0x3f800000,0x3f800000,0x3f7ec46d,0x3f7e1324,0x3f7d3aac,0x3f7c3b28,0x3f7b14be,0x3f7853f8,0x3f74fa0b,0x3f710908,0x3f74fa0b,0x3f6ed89e,0x3f676bd8,0x3f5ebe05,
    0x3f6c835e,0x3f61c598,0x3f54db31,0x3f45e403,0x3f61c598,0x3f514d3d,0x3f3daef9,0x3f273656,0x3f54db31,0x3f3daef9,0x3f226799,0x3f039c3d,0x3f45e403,0x3f273656,0x3f039c3d,0x3eb8442a,
    0x3f3504f3,0x3f0e39da,0x3ec3ef15,0x3e47c5c2,0x3f226799,0x3ee63375,0x3e78cfcc,0x3cc90ab0,0x3f0e39da,0x3eac7cd4,0x3dc8bd36,0xbe164083,0x3ef15aea,0x3e605c13,0xbd48fb30,0xbea09ae5,
    0x3ec3ef15,0x3dc8bd36,0xbe47c5c2,0xbef15aea,0x3e94a031,0xbcc90ab0,0xbeac7cd4,0xbf1d7fd1,0x3e47c5c2,0xbe164083,0xbef15aea,0xbf3daef9,0x3dc8bd36,0xbe888e93,0xbf187fc0,0xbf584853,
    0x3f800000,0x3f800000,0x3f800000,0x3f800000,0x3f7b14be,0x3f79c79d,0x3f7853f8,0x3f76ba07,0x3f6c835e,0x3f676bd8,0x3f61c598,0x3f5b941a,0x3f54db31,0x3f49d112,0x3f3daef9,0x3f3085bb,
    0x3f3504f3,0x3f226799,0x3f0e39da,0x3ef15aea,0x3f0e39da,0x3ee63375,0x3eac7cd4,0x3e605c13,0x3ec3ef15,0x3e78cfcc,0x3dc8bd36,0xbd48fb30,0x3e47c5c2,0x3cc90ab0,0xbe164083,0xbea09ae5,
    0x248d3132,0xbe47c5c2,0xbec3ef15,0xbf0e39da,0xbe47c5c2,0xbecf7bca,0xbf187fc0,0xbf41d870,0xbec3ef15,0xbf187fc0,0xbf45e403,0xbf676bd8,0xbf0e39da,0xbf41d870,0xbf676bd8,0xbf7c3b28,
    0xbf3504f3,0xbf61c598,0xbf7b14be,0xbf7ec46d,0xbf54db31,0xbf76ba07,0xbf7fb10f,0xbf6ed89e,0xbf6c835e,0xbf7fb10f,0xbf74fa0b,0xbf4d9f02,0xbf7b14be,0xbf7c3b28,0xbf5b941a,0xbf1d7fd1,
    0x3f800000,0x3f800000,0x3f800000,0x3f800000,0x3f74fa0b,0x3f731447,0x3f710908,0x3f6ed89e,0x3f54db31,0x3f4d9f02,0x3f45e403,0x3f3daef9,0x3f226799,0x3f13682a,0x3f039c3d,0x3ee63375,
    0x3ec3ef15,0x3e94a031,0x3e47c5c2,0x3dc8bd36,0x3dc8bd36,0xbcc90ab0,0xbe164083,0xbe888e93,0xbe47c5c2,0xbeac7cd4,0xbef15aea,0xbf187fc0,0xbef15aea,0xbf1d7fd1,0xbf3daef9,0xbf584853,
    0xbf3504f3,0xbf54db31,0xbf6c835e,0xbf7b14be,0xbf61c598,0xbf76ba07,0xbf7fb10f,0xbf7c3b28,0xbf7b14be,0xbf7fb10f,0xbf74fa0b,0xbf5b941a,0xbf7ec46d,0xbf6ed89e,0xbf4d9f02,0xbf1d7fd1,
    0xbf6c835e,0xbf45e403,0xbf0e39da,0xbe94a031,0xbf45e403,0xbf08f59b,0xbe78cfcc,0x3d96a905,0xbf0e39da,0xbe78cfcc,0x3dc8bd36,0x3edae880,0xbe94a031,0x3d96a905,0x3edae880,0x3f396842,
    0x0,0x0,0x0,0x0,0x0,0x3cc90ab0,0x3d48fb30,0x3d96a905,0x0,0x3d48fb30,0x3dc8bd36,0x3e164083,0x0,0x3d96a905,0x3e164083,0x3e605c13,
    0x0,0x3dc8bd36,0x3e47c5c2,0x3e94a031,0x0,0x3dfab273,0x3e78cfcc,0x3eb8442a,0x0,0x3e164083,0x3e94a031,0x3edae880,0x0,0x3e2f10a2,0x3eac7cd4,0x3efc5d27,
    0x0,0x3e47c5c2,0x3ec3ef15,0x3f0e39da,0x0,0x3e605c13,0x3edae880,0x3f1d7fd1,0x0,0x3e78cfcc,0x3ef15aea,0x3f2beb4a,0x0,0x3e888e93,0x3f039c3d,0x3f396842,
    0x0,0x3e94a031,0x3f0e39da,0x3f45e403,0x0,0x3ea09ae5,0x3f187fc0,0x3f514d3d,0x0,0x3eac7cd4,0x3f226799,0x3f5b941a,0x0,0x3eb8442a,0x3f2beb4a,0x3f64aa59,
    0x0,0x0,0x0,0x0,0x3dc8bd36,0x3dfab273,0x3e164083,0x3e2f10a2,0x3e47c5c2,0x3e78cfcc,0x3e94a031,0x3eac7cd4,0x3e94a031,0x3eb8442a,0x3edae880,0x3efc5d27,
    0x3ec3ef15,0x3ef15aea,0x3f0e39da,0x3f226799,0x3ef15aea,0x3f13682a,0x3f2beb4a,0x3f41d870,0x3f0e39da,0x3f2beb4a,0x3f45e403,0x3f5b941a,0x3f226799,0x3f41d870,0x3f5b941a,0x3f6ed89e,
    0x3f3504f3,0x3f54db31,0x3f6c835e,0x3f7b14be,0x3f45e403,0x3f64aa59,0x3f7853f8,0x3f7fec43,0x3f54db31,0x3f710908,0x3f7ec46d,0x3f7d3aac,0x3f61c598,0x3f79c79d,0x3f7fb10f,0x3f731447,
    0x3f6c835e,0x3f7ec46d,0x3f7b14be,0x3f61c598,0x3f74fa0b,0x3f7fec43,0x3f710908,0x3f49d112,0x3f7b14be,0x3f7d3aac,0x3f61c598,0x3f2beb4a,0x3f7ec46d,0x3f76ba07,0x3f4d9f02,0x3f08f59b,
    0x0,0x0,0x0,0x0,0x3e47c5c2,0x3e605c13,0x3e78cfcc,0x3e888e93,0x3ec3ef15,0x3edae880,0x3ef15aea,0x3f039c3d,0x3f0e39da,0x3f1d7fd1,0x3f2beb4a,0x3f396842,
    0x3f3504f3,0x3f45e403,0x3f54db31,0x3f61c598,0x3f54db31,0x3f64aa59,0x3f710908,0x3f79c79d,0x3f6c835e,0x3f7853f8,0x3f7ec46d,0x3f7fb10f,0x3f7b14be,0x3f7fec43,0x3f7d3aac,0x3f731447,
    0x3f800000,0x3f7b14be,0x3f6c835e,0x3f54db31,0x3f7b14be,0x3f6a09a7,0x3f4d9f02,0x3f273656,0x3f6c835e,0x3f4d9f02,0x3f226799,0x3edae880,0x3f54db31,0x3f273656,0x3edae880,0x3e2f10a2,
    0x3f3504f3,0x3ef15aea,0x3e47c5c2,0xbdc8bd36,0x3f0e39da,0x3e888e93,0xbd48fb30,0xbeb8442a,0x3ec3ef15,0x3d48fb30,0xbe94a031,0xbf187fc0,0x3e47c5c2,0xbe2f10a2,0xbf039c3d,0xbf49d112,
    0x0,0x0,0x0,0x0,0x3e94a031,0x3ea09ae5,0x3eac7cd4,0x3eb8442a,0x3f0e39da,0x3f187fc0,0x3f226799,0x3f2beb4a,0x3f45e403,0x3f514d3d,0x3f5b941a,0x3f64aa59,
    0x3f6c835e,0x3f74fa0b,0x3f7b14be,0x3f7ec46d,0x3f7ec46d,0x3f7fec43,0x3f7d3aac,0x3f76ba07,0x3f7b14be,0x3f710908,0x3f61c598,0x3f4d9f02,0x3f61c598,0x3f49d112,0x3f2beb4a,0x3f08f59b,
    0x3f3504f3,0x3f0e39da,0x3ec3ef15,0x3e47c5c2,0x3ef15aea,0x3e888e93,0x3d48fb30,0xbe2f10a2,0x3e47c5c2,0xbd48fb30,0xbe94a031,0xbf039c3d,0xbdc8bd36,0xbeb8442a,0xbf187fc0,0xbf49d112,
    0xbec3ef15,0xbf226799,0xbf54db31,0xbf74fa0b,0xbf226799,0xbf584853,0xbf7853f8,0xbf7f4e6d,0xbf54db31,0xbf7853f8,0xbf7ec46d,0xbf676bd8,0xbf74fa0b,0xbf7f4e6d,0xbf676bd8,0xbf3085bb,
    0x3f80,0x3e483f7b,0x3ec43f6d,0x3f0e3f55,0x3f80,0x3f0e3f55,0x3f6d3ec4,0x3f7bbe48,0x3f80,0x3f553f0e,0x3f6dbec4,0x3e48bf7b,0x3f80,0x3f7b3e48,0x3ec4bf6d,0xbf55bf0e,
    0x3f80,0x3f7bbe48,0xbec4bf6d,0xbf553f0e,0x3f80,0x3f55bf0e,0xbf6dbec4,0x3e483f7b,0x3f80,0x3f0ebf55,0xbf6d3ec4,0x3f7b3e48,0x3f80,0x3e48bf7b,0xbec43f6d,0x3f0ebf55,
    0x3f80,0xbe48bf7b,0x3ec43f6d,0xbf0ebf55,0x3f80,0xbf0ebf55,0x3f6d3ec4,0xbf7b3e48,0x3f80,0xbf55bf0e,0x3f6dbec4,0xbe483f7b,0x3f80,0xbf7bbe48,0x3ec4bf6d,0x3f553f0e,
    0x3f80,0xbf7b3e48,0xbec4bf6d,0x3f55bf0e,0x3f80,0xbf553f0e,0xbf6dbec4,0xbe48bf7b,0x3f80,0xbf0e3f55,0xbf6d3ec4,0xbf7bbe48,0x3f80,0xbe483f7b,0xbec43f6d,0xbf0e3f55,
    0x3f353f35,0x3f553f0e,0x3f6d3ec4,0x3f7b3e48,0x3f35bf35,0x3e48bf7b,0xbec4bf6d,0xbf55bf0e,0xbf35bf35,0xbf7b3e48,0xbec43f6d,0x3f0e3f55,0xbf353f35,0x3f0e3f55,0x3f6dbec4,0xbe48bf7b,
    0x3f353f35,0x3f0ebf55,0xbf6dbec4,0xbe483f7b,0x3f35bf35,0xbf7bbe48,0x3ec43f6d,0x3f0ebf55,0xbf35bf35,0x3e483f7b,0x3ec4bf6d,0xbf553f0e,0xbf353f35,0x3f55bf0e,0xbf6d3ec4,0x3f7bbe48,
    0x3f353f35,0xbf55bf0e,0x3f6d3ec4,0xbf7bbe48,0x3f35bf35,0xbe483f7b,0xbec4bf6d,0x3f553f0e,0xbf35bf35,0x3f7bbe48,0xbec43f6d,0xbf0ebf55,0xbf353f35,0xbf0ebf55,0x3f6dbec4,0x3e483f7b,
    0x3f353f35,0xbf0e3f55,0xbf6dbec4,0x3e48bf7b,0x3f35bf35,0x3f7b3e48,0x3ec43f6d,0xbf0e3f55,0xbf35bf35,0xbe48bf7b,0x3ec4bf6d,0x3f55bf0e,0xbf353f35,0xbf553f0e,0xbf6d3ec4,0xbf7b3e48,
    0x3f80248d,0x3f7bbe48,0x3f6dbec4,0x3f55bf0e,0xbf80a554,0xbf553f0e,0xbec43f6d,0x3e483f7b,0x3f80248d,0x3f0ebf55,0xbec4bf6d,0xbf7bbe48,0xbf80a554,0xbe483f7b,0x3f6d3ec4,0x3f0ebf55,
    0x3f80248d,0xbe48bf7b,0xbf6d3ec4,0x3f0e3f55,0xbf80a554,0x3f0e3f55,0x3ec4bf6d,0xbf7b3e48,0x3f80248d,0xbf55bf0e,0x3ec43f6d,0x3e48bf7b,0xbf80a554,0x3f7b3e48,0xbf6dbec4,0x3f553f0e,
    0x3f80248d,0xbf7b3e48,0x3f6dbec4,0xbf553f0e,0xbf80a554,0x3f55bf0e,0xbec43f6d,0xbe48bf7b,0x3f80248d,0xbf0e3f55,0xbec4bf6d,0x3f7b3e48,0xbf80a554,0x3e48bf7b,0x3f6d3ec4,0xbf0e3f55,
    0x3f80248d,0x3e483f7b,0xbf6d3ec4,0xbf0ebf55,0xbf80a554,0xbf0ebf55,0x3ec4bf6d,0x3f7bbe48,0x3f80248d,0x3f553f0e,0x3ec43f6d,0xbe483f7b,0xbf80a554,0xbf7bbe48,0xbf6dbec4,0xbf55bf0e,
    0x3f35bf35,0x3f0ebf55,0x3ec4bf6d,0x3e48bf7b,0x3f353f35,0x3f7b3e48,0x3f6dbec4,0x3f0ebf55,0xbf353f35,0x3e483f7b,0x3f6d3ec4,0x3f55bf0e,0xbf35bf35,0xbf553f0e,0x3ec43f6d,0x3f7bbe48,
    0x3f35bf35,0xbf55bf0e,0xbec43f6d,0x3f7b3e48,0x3f353f35,0x3e48bf7b,0xbf6d3ec4,0x3f553f0e,0xbf353f35,0x3f7bbe48,0xbf6dbec4,0x3f0e3f55,0xbf35bf35,0x3f0e3f55,0xbec4bf6d,0x3e483f7b,
    0x3f35bf35,0xbf0e3f55,0x3ec4bf6d,0xbe483f7b,0x3f353f35,0xbf7bbe48,0x3f6dbec4,0xbf0e3f55,0xbf353f35,0xbe48bf7b,0x3f6d3ec4,0xbf553f0e,0xbf35bf35,0x3f55bf0e,0x3ec43f6d,0xbf7b3e48,
    0x3f35bf35,0x3f553f0e,0xbec43f6d,0xbf7bbe48,0x3f353f35,0xbe483f7b,0xbf6d3ec4,0xbf55bf0e,0xbf353f35,0xbf7b3e48,0xbf6dbec4,0xbf0ebf55,0xbf35bf35,0xbf0ebf55,0xbec4bf6d,0xbe48bf7b,
    0x3f808000,0x3f7bbe48,0x3f6dbec4,0x3f55bf0e,0x3f808000,0x3f55bf0e,0x3ec4bf6d,0xbe48bf7b,0x3f808000,0x3f0ebf55,0xbec4bf6d,0xbf7bbe48,0x3f808000,0x3e48bf7b,0xbf6dbec4,0xbf0e3f55,
    0x3f808000,0xbe48bf7b,0xbf6d3ec4,0x3f0e3f55,0x3f808000,0xbf0ebf55,0xbec43f6d,0x3f7bbe48,0x3f808000,0xbf55bf0e,0x3ec43f6d,0x3e48bf7b,0x3f808000,0xbf7bbe48,0x3f6d3ec4,0xbf55bf0e,
    0x3f808000,0xbf7b3e48,0x3f6dbec4,0xbf553f0e,0x3f808000,0xbf553f0e,0x3ec4bf6d,0x3e483f7b,0x3f808000,0xbf0e3f55,0xbec4bf6d,0x3f7b3e48,0x3f808000,0xbe483f7b,0xbf6dbec4,0x3f0ebf55,
    0x3f808000,0x3e483f7b,0xbf6d3ec4,0xbf0ebf55,0x3f808000,0x3f0e3f55,0xbec43f6d,0xbf7b3e48,0x3f808000,0x3f553f0e,0x3ec43f6d,0xbe483f7b,0x3f808000,0x3f7b3e48,0x3f6d3ec4,0x3f553f0e,
    0x3f35bf35,0x3f0ebf55,0x3ec4bf6d,0x3e48bf7b,0xbf35bf35,0xbf7bbe48,0xbf6d3ec4,0xbf0e3f55,0xbf353f35,0x3e483f7b,0x3f6d3ec4,0x3f55bf0e,0x3f353f35,0x3f55bf0e,0xbec4bf6d,0xbf7b3e48,
    0x3f35bf35,0xbf55bf0e,0xbec43f6d,0x3f7b3e48,0xbf35bf35,0xbe483f7b,0x3f6dbec4,0xbf55bf0e,0xbf353f35,0x3f7bbe48,0xbf6dbec4,0x3f0e3f55,0x3f353f35,0xbf0ebf55,0x3ec43f6d,0xbe48bf7b,
    0x3f35bf35,0xbf0e3f55,0x3ec4bf6d,0xbe483f7b,0xbf35bf35,0x3f7b3e48,0xbf6d3ec4,0x3f0ebf55,0xbf353f35,0xbe48bf7b,0x3f6d3ec4,0xbf553f0e,0x3f353f35,0xbf553f0e,0xbec4bf6d,0x3f7bbe48,
    0x3f35bf35,0x3f553f0e,0xbec43f6d,0xbf7bbe48,0xbf35bf35,0x3e48bf7b,0x3f6dbec4,0x3f553f0e,0xbf353f35,0xbf7b3e48,0xbf6dbec4,0xbf0ebf55,0x3f353f35,0x3f0e3f55,0x3ec43f6d,0x3e483f7b,
    0x248dbf80,0xbe48bf7b,0xbec4bf6d,0xbf0ebf55,0xa5543f80,0x3f0e3f55,0x3f6d3ec4,0x3f7bbe48,0x248dbf80,0xbf55bf0e,0xbf6d3ec4,0xbe483f7b,0xa5543f80,0x3f7b3e48,0x3ec4bf6d,0xbf55bf0e,
    0x248dbf80,0xbf7b3e48,0x3ec43f6d,0x3f55bf0e,0xa5543f80,0x3f55bf0e,0xbf6dbec4,0x3e483f7b,0x248dbf80,0xbf0e3f55,0x3f6dbec4,0xbf7bbe48,0xa5543f80,0x3e48bf7b,0xbec43f6d,0x3f0ebf55,
    0x248dbf80,0x3e483f7b,0xbec4bf6d,0x3f0e3f55,0xa5543f80,0xbf0ebf55,0x3f6d3ec4,0xbf7b3e48,0x248dbf80,0x3f553f0e,0xbf6d3ec4,0x3e48bf7b,0xa5543f80,0xbf7bbe48,0x3ec4bf6d,0x3f553f0e,
    0x248dbf80,0x3f7bbe48,0x3ec43f6d,0xbf553f0e,0xa5543f80,0xbf553f0e,0xbf6dbec4,0xbe48bf7b,0x248dbf80,0x3f0ebf55,0x3f6dbec4,0x3f7b3e48,0xa5543f80,0xbe483f7b,0xbec43f6d,0xbf0e3f55,
    0xbf35bf35,0xbf55bf0e,0xbf6dbec4,0xbf7bbe48,0x3f35bf35,0x3e48bf7b,0xbec4bf6d,0xbf55bf0e,0x3f353f35,0x3f7bbe48,0x3ec4bf6d,0xbf0ebf55,0xbf353f35,0x3f0e3f55,0x3f6dbec4,0xbe48bf7b,
    0xbf35bf35,0xbf0e3f55,0x3f6d3ec4,0x3e48bf7b,0x3f35bf35,0xbf7bbe48,0x3ec43f6d,0x3f0ebf55,0x3f353f35,0xbe48bf7b,0xbec43f6d,0x3f55bf0e,0xbf353f35,0x3f55bf0e,0xbf6d3ec4,0x3f7bbe48,
    0xbf35bf35,0x3f553f0e,0xbf6dbec4,0x3f7b3e48,0x3f35bf35,0xbe483f7b,0xbec4bf6d,0x3f553f0e,0x3f353f35,0xbf7b3e48,0x3ec4bf6d,0x3f0e3f55,0xbf353f35,0xbf0ebf55,0x3f6dbec4,0x3e483f7b,
    0xbf35bf35,0x3f0ebf55,0x3f6d3ec4,0xbe483f7b,0x3f35bf35,0x3f7b3e48,0x3ec43f6d,0xbf0e3f55,0x3f353f35,0x3e483f7b,0xbec43f6d,0xbf553f0e,0xbf353f35,0xbf553f0e,0xbf6d3ec4,0xbf7b3e48,
    0x3f008000,0x3f808000,0x3f808000,0x3f808000,0x3f008000,0x3e9ebf73,0xbf4fbf16,0xbf4f3f16,0x3f008000,0xbf4fbf16,0x3e9e3f73,0x3e9ebf73,0x3f008000,0xbf4f3f16,0x3e9ebf73,0x3e9e3f73,
    0x3f008000,0x3f73be9e,0x3f4fbf16,0x3f16bf4f,0x3f008000,0x248dbf80,0xbf80a50d,0xa5543f80,0x3f008000,0xbf73be9e,0x3f4f3f16,0xbf16bf4f,0x3f008000,0xbf163f4f,0xbe9ebf73,0x3f733e9e,
    0x3f008000,0x3f4fbf16,0x3e9ebf73,0xbe9ebf73,0x3f008000,0xbe9ebf73,0xbf4f3f16,0x3f4f3f16,0x3f008000,0xbf80a50d,0x3f808000,0xbf80a50d,0x3f008000,0xbe9e3f73,0xbf4fbf16,0x3f4fbf16,
    0x3f008000,0x3f16bf4f,0xbe9ebf73,0xbf73be9e,0x3f008000,0xbf16bf4f,0xbe9e3f73,0x3f73be9e,0x3f008000,0xbf733e9e,0x3f4fbf16,0xbf163f4f,0x3f008000,0xa5543f80,0xbf80a50d,0x248dbf80,
    0x3f808000,0x3f808000,0x3f808000,0x3f808000,0x3e9e3f73,0x3f808000,0x3e9ebf73,0xbf4fbf16,0xbf4f3f16,0x3f808000,0xbf4fbf16,0x3e9e3f73,0xbf4fbf16,0x3f808000,0xbf4f3f16,0x3e9ebf73,
    0x3e9ebf73,0x248dbf80,0xbe9ebf73,0xbf16bf4f,0x3f808000,0x248dbf80,0xbf80a50d,0xa5543f80,0x3e9e3f73,0x248dbf80,0xbe9e3f73,0x3f16bf4f,0xbf4f3f16,0x248dbf80,0x3f4f3f16,0xbf733e9e,
    0xbf4fbf16,0xbf80a50d,0xbf4f3f16,0xbe9e3f73,0x3e9ebf73,0xbf80a50d,0x3e9e3f73,0x3f4fbf16,0x3f808000,0xbf80a50d,0x3f808000,0xbf80a50d,0x3e9e3f73,0xbf80a50d,0x3e9ebf73,0x3f4f3f16,
    0xbf4f3f16,0xa5543f80,0x3f4f3f16,0x3f73be9e,0xbf4fbf16,0xa5543f80,0x3f4fbf16,0xbf73be9e,0x3e9ebf73,0xa5543f80,0xbe9ebf73,0x3f163f4f,0x3f808000,0xa5543f80,0xbf80a50d,0x248dbf80,
    0x3f808000,0x3f808000,0x3f008000,0x8000,0xbf4f3f16,0x3e9e3f73,0x3f008000,0x8000,0x3e9ebf73,0xbf4f3f16,0x3f008000,0x80008000,0x3e9e3f73,0xbf4fbf16,0x3f008000,0x80000000,
    0xbf4fbf16,0xbf73be9e,0xbf00a48d,0x80000000,0x3f808000,0x248dbf80,0xbf00a48d,0x80000000,0xbf4f3f16,0x3f73be9e,0xbf00a48d,0x0,0x3e9ebf73,0x3f163f4f,0xbf00a48d,0x8000,
    0x3e9e3f73,0x3f4f3f16,0x3f008000,0x8000,0xbf4fbf16,0xbe9e3f73,0x3f008000,0x80008000,0x3f808000,0xbf80a50d,0x3f008000,0x80008000,0xbf4f3f16,0xbe9ebf73,0x3f008000,0x80000000,
    0x3e9ebf73,0xbf16bf4f,0xbf00a48d,0x80000000,0x3e9e3f73,0x3f16bf4f,0xbf00a48d,0x0,0xbf4fbf16,0x3f733e9e,0xbf00a48d,0x8000,0x3f808000,0xa5543f80,0xbf00a48d,0x8000,
    0x8000,0x8000,0x8000,0x8000,0x80008000,0x80000000,0x0,0x8000,0x0,0x8000,0x80000000,0x8000,0x8000,0x0,0x80008000,0x8000,
    0x80000000,0x80000000,0x80000000,0x80000000,0x8000,0x8000,0x80008000,0x80000000,0x80008000,0x0,0x80008000,0x80000000,0x0,0x80008000,0x8000,0x80000000,
    0x8000,0x80008000,0x80008000,0x80008000,0x80000000,0x0,0x8000,0x80008000,0x8000,0x80008000,0x8000,0x80008000,0x80008000,0x8000,0x0,0x80008000,
    0x0,0x0,0x8000,0x8000,0x8000,0x80000000,0x0,0x8000,0x80000000,0x8000,0x80000000,0x8000,0x8000,0x80000000,0x80008000,0x8000,
    0x3f008000,0x3e9e3f73,0xbf4f3f16,0xbf4fbf16,0x3f008000,0x3e9e3f73,0xbf4f3f16,0xbf4fbf16,0x3f008000,0x3e9e3f73,0xbf4f3f16,0xbf4fbf16,0x3f008000,0x3e9e3f73,0xbf4f3f16,0xbf4fbf16,
    0x3f008000,0x3f163f4f,0xbe9e3f73,0xbf733e9e,0x3f008000,0x3f163f4f,0xbe9e3f73,0xbf733e9e,0x3f008000,0x3f163f4f,0xbe9e3f73,0xbf733e9e,0x3f008000,0x3f163f4f,0xbe9e3f73,0xbf733e9e,
    0x3f008000,0x3f4f3f16,0x3e9e3f73,0xbe9e3f73,0x3f008000,0x3f4f3f16,0x3e9e3f73,0xbe9e3f73,0x3f008000,0x3f4f3f16,0x3e9e3f73,0xbe9e3f73,0x3f008000,0x3f4f3f16,0x3e9e3f73,0xbe9e3f73,
    0x3f008000,0x3f733e9e,0x3f4f3f16,0x3f163f4f,0x3f008000,0x3f733e9e,0x3f4f3f16,0x3f163f4f,0x3f008000,0x3f733e9e,0x3f4f3f16,0x3f163f4f,0x3f008000,0x3f733e9e,0x3f4f3f16,0x3f163f4f,
    0x3e9ebf73,0x3f808000,0x3e9e3f73,0xbf4f3f16,0x3e9ebf73,0x3f808000,0x3e9e3f73,0xbf4f3f16,0x3e9ebf73,0x3f808000,0x3e9e3f73,0xbf4f3f16,0x3e9ebf73,0x3f808000,0x3e9e3f73,0xbf4f3f16,
    0xbf4fbf16,0x248dbf80,0x3f4fbf16,0x3f733e9e,0xbf4fbf16,0x248dbf80,0x3f4fbf16,0x3f733e9e,0xbf4fbf16,0x248dbf80,0x3f4fbf16,0x3f733e9e,0xbf4fbf16,0x248dbf80,0x3f4fbf16,0x3f733e9e,
    0xbf4f3f16,0xbf80a50d,0xbf4fbf16,0xbe9ebf73,0xbf4f3f16,0xbf80a50d,0xbf4fbf16,0xbe9ebf73,0xbf4f3f16,0xbf80a50d,0xbf4fbf16,0xbe9ebf73,0xbf4f3f16,0xbf80a50d,0xbf4fbf16,0xbe9ebf73,
    0x3e9e3f73,0xa5543f80,0xbe9e3f73,0xbf163f4f,0x3e9e3f73,0xa5543f80,0xbe9e3f73,0xbf163f4f,0x3e9e3f73,0xa5543f80,0xbe9e3f73,0xbf163f4f,0x3e9e3f73,0xa5543f80,0xbe9e3f73,0xbf163f4f,
    0xbf4fbf16,0x3e9ebf73,0x3f008000,0x0,0xbf4fbf16,0x3e9ebf73,0x3f008000,0x0,0xbf4fbf16,0x3e9ebf73,0x3f008000,0x0,0xbf4fbf16,0x3e9ebf73,0x3f008000,0x0,
    0x3e9e3f73,0xbf163f4f,0xbf00a48d,0x80008000,0x3e9e3f73,0xbf163f4f,0xbf00a48d,0x80008000,0x3e9e3f73,0xbf163f4f,0xbf00a48d,0x80008000,0x3e9e3f73,0xbf163f4f,0xbf00a48d,0x80008000,
    0x3e9ebf73,0x3f4fbf16,0x3f008000,0x0,0x3e9ebf73,0x3f4fbf16,0x3f008000,0x0,0x3e9ebf73,0x3f4fbf16,0x3f008000,0x0,0x3e9ebf73,0x3f4fbf16,0x3f008000,0x0,
    0xbf4f3f16,0xbf733e9e,0xbf00a48d,0x80008000,0xbf4f3f16,0xbf733e9e,0xbf00a48d,0x80008000,0xbf4f3f16,0xbf733e9e,0xbf00a48d,0x80008000,0xbf4f3f16,0xbf733e9e,0xbf00a48d,0x80008000,
    0x80000000,0x80008000,0x8000,0x8000,0x80000000,0x80008000,0x8000,0x8000,0x80000000,0x80008000,0x8000,0x8000,0x80000000,0x80008000,0x8000,0x8000,
    0x8000,0x8000,0x0,0x80000000,0x8000,0x8000,0x0,0x80000000,0x8000,0x8000,0x0,0x80000000,0x8000,0x8000,0x0,0x80000000,
    0x0,0x80000000,0x80000000,0x80008000,0x0,0x80000000,0x80000000,0x80008000,0x0,0x80000000,0x80000000,0x80008000,0x0,0x80000000,0x80000000,0x80008000,
    0x80008000,0x80008000,0x80008000,0x8000,0x80008000,0x80008000,0x80008000,0x8000,0x80008000,0x80008000,0x80008000,0x8000,0x80008000,0x80008000,0x80008000,0x8000,
    0x3f800000,0x3f800000,0x3f800000,0x3f800000,0x3f800000,0x3f7ff35e,0x3f7fcd79,0x3f7f8e56,0x3f800000,0x3f7fcd79,0x3f7f35f9,0x3f7e39bc,0x3f800000,0x3f7f8e56,0x3f7e39bc,0x3f7c0361,
    0x3f800000,0x3f7f35f9,0x3f7cd925,0x3f78ed3c,0x3f800000,0x3f7ec46d,0x3f7b14be,0x3f74fa0b,0x3f800000,0x3f7e39bc,0x3f78ed3c,0x3f702d4f,0x3f800000,0x3f7d95f4,0x3f766377,0x3f6a8b4b,
    0x3f800000,0x3f7cd925,0x3f737871,0x3f641901,0x3f800000,0x3f7c0361,0x3f702d4f,0x3f5cdc29,0x3f800000,0x3f7b14be,0x3f6c835e,0x3f54db31,0x3f800000,0x3f7a0d54,0x3f687c12,0x3f4c1d35,
    0x3f800000,0x3f78ed3c,0x3f641901,0x3f42a9f7,0x3f800000,0x3f77b493,0x3f5f5be6,0x3f3889dc,0x3f800000,0x3f766377,0x3f5a46a0,0x3f2dc5e3,0x3f800000,0x3f74fa0b,0x3f54db31,0x3f226799,
    0x3f800000,0x3f800000,0x3f800000,0x3f800000,0x3f7f35f9,0x3f7ec46d,0x3f7e39bc,0x3f7d95f4,0x3f7cd925,0x3f7b14be,0x3f78ed3c,0x3f766377,0x3f78ed3c,0x3f74fa0b,0x3f702d4f,0x3f6a8b4b,
    0x3f737871,0x3f6c835e,0x3f641901,0x3f5a46a0,0x3f6c835e,0x3f61c598,0x3f54db31,0x3f45e403,0x3f641901,0x3f54db31,0x3f42a9f7,0x3f2dc5e3,0x3f5a46a0,0x3f45e403,0x3f2dc5e3,0x3f1260b2,
    0x3f4f1bbd,0x3f3504f3,0x3f167918,0x3ee87171,0x3f42a9f7,0x3f226799,0x3efa2c90,0x3ea7bf25,0x3f3504f3,0x3f0e39da,0x3ec3ef15,0x3e47c5c2,0x3f26423a,0x3ef15aea,0x3e8afa3e,0x3d712283,
    0x3f167918,0x3ec3ef15,0x3e20305b,0xbda0af2a,0x3f05c277,0x3e94a031,0x3d20cee7,0xbe5b73dc,0x3ee87171,0x3e47c5c2,0xbda0af2a,0xbeb13641,0x3ec3ef15,0x3dc8bd36,0xbe47c5c2,0xbef15aea,
    0x3f800000,0x3f800000,0x3f800000,0x3f800000,0x3f7cd925,0x3f7c0361,0x3f7b14be,0x3f7a0d54,0x3f737871,0x3f702d4f,0x3f6c835e,0x3f687c12,0x3f641901,0x3f5cdc29,0x3f54db31,0x3f4c1d35,
    0x3f4f1bbd,0x3f42a9f7,0x3f3504f3,0x3f26423a,0x3f3504f3,0x3f226799,0x3f0e39da,0x3ef15aea,0x3f167918,0x3efa2c90,0x3ec3ef15,0x3e8afa3e,0x3ee87171,0x3ea7bf25,0x3e47c5c2,0x3d712283,
    0x3e9e377a,0x3e20305b,0x248d3132,0xbe20305b,0x3e20305b,0xbca0d6d6,0xbe47c5c2,0xbeba9be1,0x248d3132,0xbe47c5c2,0xbec3ef15,0xbf0e39da,0xbe20305b,0xbeba9be1,0xbf0e39da,0xbf3889dc,
    0xbe9e377a,0xbf05c277,0xbf3504f3,0xbf5a46a0,0xbee87171,0xbf2a0c73,0xbf54db31,0xbf71decf,0xbf167918,0xbf490a88,0xbf6c835e,0xbf7e39bc,0xbf3504f3,0xbf61c598,0xbf7b14be,0xbf7ec46d,
    0x3f800000,0x3f800000,0x3f800000,0x3f800000,0x3f78ed3c,0x3f77b493,0x3f766377,0x3f74fa0b,0x3f641901,0x3f5f5be6,0x3f5a46a0,0x3f54db31,0x3f42a9f7,0x3f3889dc,0x3f2dc5e3,0x3f226799,
    0x3f167918,0x3f05c277,0x3ee87171,0x3ec3ef15,0x3ec3ef15,0x3e94a031,0x3e47c5c2,0x3dc8bd36,0x3e20305b,0x3d20cee7,0xbda0af2a,0xbe47c5c2,0xbda0af2a,0xbe5b73dc,0xbeb13641,0xbef15aea,
    0xbe9e377a,0xbee87171,0xbf167918,0xbf3504f3,0xbf05c277,0xbf2a0c73,0xbf490a88,0xbf61c598,0xbf3504f3,0xbf54db31,0xbf6c835e,0xbf7b14be,0xbf5a46a0,0xbf71decf,0xbf7e39bc,0xbf7ec46d,
    0xbf737871,0xbf7f35f9,0xbf7cd925,0xbf6c835e,0xbf7f35f9,0xbf7c0361,0xbf687c12,0xbf45e403,0xbf7cd925,0xbf687c12,0xbf42a9f7,0xbf0e39da,0xbf6c835e,0xbf45e403,0xbf0e39da,0xbe94a031,
    0x3f800000,0x3f7ffcd7,0x3f7ff35e,0x3f7fe394,0x3f800000,0x3f7fe394,0x3f7f8e56,0x3f7f0059,0x3f800000,0x3f7fb10f,0x3f7ec46d,0x3f7d3aac,0x3f800000,0x3f7f654e,0x3f7d95f4,0x3f7a9421,
    0x3f800000,0x3f7f0059,0x3f7c0361,0x3f770f11,0x3f800000,0x3f7e8238,0x3f7a0d54,0x3f72ae9e,0x3f800000,0x3f7deafa,0x3f77b493,0x3f6d76aa,0x3f800000,0x3f7d3aac,0x3f74fa0b,0x3f676bd8,
    0x3f800000,0x3f7c7160,0x3f71decf,0x3f609384,0x3f800000,0x3f7b8f2a,0x3f6e641a,0x3f58f3c4,0x3f800000,0x3f7a9421,0x3f6a8b4b,0x3f50935b,0x3f800000,0x3f79805c,0x3f6655e7,0x3f4779bc,
    0x3f800000,0x3f7853f8,0x3f61c598,0x3f3daef9,0x3f800000,0x3f770f11,0x3f5cdc29,0x3f333bc6,0x3f800000,0x3f75b1c9,0x3f579b8c,0x3f28296a,0x3f800000,0x3f743c41,0x3f5205d4,0x3f1c81b9,
    0x3f7fcd79,0x3f7fb10f,0x3f7f8e56,0x3f7f654e,0x3f7e39bc,0x3f7d3aac,0x3f7c0361,0x3f7a9421,0x3f7b14be,0x3f7853f8,0x3f74fa0b,0x3f710908,0x3f766377,0x3f710908,0x3f6a8b4b,0x3f62f219,
    0x3f702d4f,0x3f676bd8,0x3f5cdc29,0x3f50935b,0x3f687c12,0x3f5b941a,0x3f4c1d35,0x3f3a4582,0x3f5f5be6,0x3f4d9f02,0x3f3889dc,0x3f207440,0x3f54db31,0x3f3daef9,0x3f226799,0x3f039c3d,
    0x3f490a88,0x3f2beb4a,0x3f0a04f8,0x3ec8917e,0x3f3bfc8f,0x3f187fc0,0x3edf7108,0x3e862210,0x3f2dc5e3,0x3f039c3d,0x3ea7bf25,0x3e0255f4,0x3f1e7cf1,0x3edae880,0x3e5b73dc,0xbc20d8d2,
    0x3f0e39da,0x3eac7cd4,0x3dc8bd36,0xbe164083,0x3efa2c90,0x3e78cfcc,0xbca0d6d6,0xbe8fcefe,0x3ed65a92,0x3e164083,0xbe0c4cf7,0xbed1c759,0x3eb13641,0x3d48fb30,0xbe814693,0xbf07e565,
    0x3f7f35f9,0x3f7f0059,0x3f7ec46d,0x3f7e8238,0x3f78ed3c,0x3f770f11,0x3f74fa0b,0x3f72ae9e,0x3f6c835e,0x3f676bd8,0x3f61c598,0x3f5b941a,0x3f5a46a0,0x3f50935b,0x3f45e403,0x3f3a4582,
    0x3f42a9f7,0x3f333bc6,0x3f226799,0x3f104f0e,0x3f26423a,0x3f104f0e,0x3ef15aea,0x3ebf47d7,0x3f05c277,0x3ed1c759,0x3e94a031,0x3e2a1c3f,0x3ec3ef15,0x3e78cfcc,0x3dc8bd36,0xbd48fb30,
    0x3e6f0c4d,0x3d8ca1f2,0xbdc8bd36,0xbe862210,0x3da0af2a,0xbddcbd0d,0xbe94a031,0xbeece91a,0xbda0af2a,0xbe8fcefe,0xbef15aea,0xbf2456f1,0xbe6f0c4d,0xbee3f40d,0xbf226799,0xbf4a965e,
    0xbec3ef15,0xbf187fc0,0xbf45e403,0xbf676bd8,0xbf05c277,0xbf3a4582,0xbf61c598,0xbf79805c,0xbf26423a,0xbf563e03,0xbf74fa0b,0xbf7ffcd7,0xbf42a9f7,0xbf6b8a3d,0xbf7ec46d,0xbf7a9421,
    0x3f7e39bc,0x3f7deafa,0x3f7d95f4,0x3f7d3aac,0x3f702d4f,0x3f6d76aa,0x3f6a8b4b,0x3f676bd8,0x3f54db31,0x3f4d9f02,0x3f45e403,0x3f3daef9,0x3f2dc5e3,0x3f207440,0x3f1260b2,0x3f039c3d,
    0x3efa2c90,0x3ed1c759,0x3ea7bf25,0x3e78cfcc,0x3e8afa3e,0x3e2a1c3f,0x3d712283,0xbd48fb30,0x3d20cee7,0xbdb4b86a,0xbe5b73dc,0xbeac7cd4,0xbe47c5c2,0xbeac7cd4,0xbef15aea,0xbf187fc0,
    0xbed65a92,0xbf104f0e,0xbf316e2c,0xbf4d9f02,0xbf1e7cf1,0xbf4105b8,0xbf5cdc29,0xbf710908,0xbf490a88,0xbf653a48,0xbf77b493,0xbf7fb10f,0xbf687c12,0xbf7a9421,0xbf7ff35e,0xbf7853f8,
    0xbf7b14be,0xbf7fb10f,0xbf74fa0b,0xbf5b941a,0xbf7fcd79,0xbf743c41,0xbf579b8c,0xbf2beb4a,0xbf766377,0xbf58f3c4,0xbf2a0c73,0xbedae880,0xbf5f5be6,0xbf2f9c32,0xbedf7108,0xbe164083,
    0x0,0x0,0x0,0x0,0x0,0x3ca0d6d6,0x3d20cee7,0x3d712283,0x0,0x3d20cee7,0x3da0af2a,0x3df0b772,0x0,0x3d712283,0x3df0b772,0x3e3403f1,
    0x0,0x3da0af2a,0x3e20305b,0x3e6f0c4d,0x0,0x3dc8bd36,0x3e47c5c2,0x3e94a031,0x0,0x3df0b772,0x3e6f0c4d,0x3eb13641,0x0,0x3e0c4cf7,0x3e8afa3e,0x3ecd2ef3,
    0x0,0x3e20305b,0x3e9e377a,0x3ee87171,0x0,0x3e3403f1,0x3eb13641,0x3f0172c3,0x0,0x3e47c5c2,0x3ec3ef15,0x3f0e39da,0x0,0x3e5b73dc,0x3ed65a92,0x3f1a82a4,
    0x0,0x3e6f0c4d,0x3ee87171,0x3f26423a,0x0,0x3e814693,0x3efa2c90,0x3f316e2c,0x0,0x3e8afa3e,0x3f05c277,0x3f3bfc8f,0x0,0x3e94a031,0x3f0e39da,0x3f45e403,
    0x0,0x0,0x0,0x0,0x3da0af2a,0x3dc8bd36,0x3df0b772,0x3e0c4cf7,0x3e20305b,0x3e47c5c2,0x3e6f0c4d,0x3e8afa3e,0x3e6f0c4d,0x3e94a031,0x3eb13641,0x3ecd2ef3,
    0x3e9e377a,0x3ec3ef15,0x3ee87171,0x3f05c277,0x3ec3ef15,0x3ef15aea,0x3f0e39da,0x3f226799,0x3ee87171,0x3f0e39da,0x3f26423a,0x3f3bfc8f,0x3f05c277,0x3f226799,0x3f3bfc8f,0x3f5205d4,
    0x3f167918,0x3f3504f3,0x3f4f1bbd,0x3f641901,0x3f26423a,0x3f45e403,0x3f5f5be6,0x3f71decf,0x3f3504f3,0x3f54db31,0x3f6c835e,0x3f7b14be,0x3f42a9f7,0x3f61c598,0x3f766377,0x3f7f8e56,
    0x3f4f1bbd,0x3f6c835e,0x3f7cd925,0x3f7f35f9,0x3f5a46a0,0x3f74fa0b,0x3f7fcd79,0x3f7a0d54,0x3f641901,0x3f7b14be,0x3f7f35f9,0x3f702d4f,0x3f6c835e,0x3f7ec46d,0x3f7b14be,0x3f61c598,
    0x0,0x0,0x0,0x0,0x3e20305b,0x3e3403f1,0x3e47c5c2,0x3e5b73dc,0x3e9e377a,0x3eb13641,0x3ec3ef15,0x3ed65a92,0x3ee87171,0x3f0172c3,0x3f0e39da,0x3f1a82a4,
    0x3f167918,0x3f26423a,0x3f3504f3,0x3f42a9f7,0x3f3504f3,0x3f45e403,0x3f54db31,0x3f61c598,0x3f4f1bbd,0x3f5f5be6,0x3f6c835e,0x3f766377,0x3f641901,0x3f71decf,0x3f7b14be,0x3f7f8e56,
    0x3f737871,0x3f7cd925,0x3f800000,0x3f7cd925,0x3f7cd925,0x3f7ff35e,0x3f7b14be,0x3f6e641a,0x3f800000,0x3f7b14be,0x3f6c835e,0x3f54db31,0x3f7cd925,0x3f6e641a,0x3f54db31,0x3f316e2c,
    0x3f737871,0x3f5a46a0,0x3f3504f3,0x3f05c277,0x3f641901,0x3f3f5cb5,0x3f0e39da,0x3ea7bf25,0x3f4f1bbd,0x3f1e7cf1,0x3ec3ef15,0x3df0b772,0x3f3504f3,0x3ef15aea,0x3e47c5c2,0xbdc8bd36,
    0x0,0x0,0x0,0x0,0x3e6f0c4d,0x3e814693,0x3e8afa3e,0x3e94a031,0x3ee87171,0x3efa2c90,0x3f05c277,0x3f0e39da,0x3f26423a,0x3f316e2c,0x3f3bfc8f,0x3f45e403,
    0x3f4f1bbd,0x3f5a46a0,0x3f641901,0x3f6c835e,0x3f6c835e,0x3f74fa0b,0x3f7b14be,0x3f7ec46d,0x3f7cd925,0x3f7fcd79,0x3f7f35f9,0x3f7b14be,0x3f7f35f9,0x3f7a0d54,0x3f702d4f,0x3f61c598,
    0x3f737871,0x3f641901,0x3f4f1bbd,0x3f3504f3,0x3f5a46a0,0x3f3f5cb5,0x3f1e7cf1,0x3ef15aea,0x3f3504f3,0x3f0e39da,0x3ec3ef15,0x3e47c5c2,0x3f05c277,0x3ea7bf25,0x3df0b772,0xbdc8bd36,
    0x3e9e377a,0x3da0af2a,0xbe20305b,0xbec3ef15,0x3da0af2a,0xbe3403f1,0xbed65a92,0xbf226799,0xbe20305b,0xbed65a92,0xbf26423a,0xbf54db31,0xbec3ef15,0xbf226799,0xbf54db31,0xbf74fa0b,
    0x0,0x3c20d8d2,0x3ca0d6d6,0x3cf13d4c,0x0,0x3cf13d4c,0x3d712283,0x3db4b86a,0x0,0x3d48fb30,0x3dc8bd36,0x3e164083,0x0,0x3d8ca1f2,0x3e0c4cf7,0x3e519f65,
    0x0,0x3db4b86a,0x3e3403f1,0x3e862210,0x0,0x3ddcbd0d,0x3e5b73dc,0x3ea2fd52,0x0,0x3e0255f4,0x3e814693,0x3ebf47d7,0x0,0x3e164083,0x3e94a031,0x3edae880,
    0x0,0x3e2a1c3f,0x3ea7bf25,0x3ef5c6c5,0x0,0x3e3de731,0x3eba9be1,0x3f07e565,0x0,0x3e519f65,0x3ecd2ef3,0x3f146eba,0x0,0x3e6542e9,0x3edf7108,0x3f207440,
    0x0,0x3e78cfcc,0x3ef15aea,0x3f2beb4a,0x0,0x3e862210,0x3f0172c3,0x3f36c9a9,0x0,0x3e8fcefe,0x3f0a04f8,0x3f4105b8,0x0,0x3e996dba,0x3f1260b2,0x3f4a965e,
    0x3d20cee7,0x3d48fb30,0x3d712283,0x3d8ca1f2,0x3df0b772,0x3e164083,0x3e3403f1,0x3e519f65,0x3e47c5c2,0x3e78cfcc,0x3e94a031,0x3eac7cd4,0x3e8afa3e,0x3eac7cd4,0x3ecd2ef3,0x3eece91a,
    0x3eb13641,0x3edae880,0x3f0172c3,0x3f146eba,0x3ed65a92,0x3f039c3d,0x3f1a82a4,0x3f2f9c32,0x3efa2c90,0x3f187fc0,0x3f316e2c,0x3f4779bc,0x3f0e39da,0x3f2beb4a,0x3f45e403,0x3f5b941a,
    0x3f1e7cf1,0x3f3daef9,0x3f579b8c,0x3f6b8a3d,0x3f2dc5e3,0x3f4d9f02,0x3f6655e7,0x3f770f11,0x3f3bfc8f,0x3f5b941a,0x3f71decf,0x3f7deafa,0x3f490a88,0x3f676bd8,0x3f7a0d54,0x3f7ffcd7,
    0x3f54db31,0x3f710908,0x3f7ec46d,0x3f7d3aac,0x3f5f5be6,0x3f7853f8,0x3f7ff35e,0x3f75b1c9,0x3f687c12,0x3f7d3aac,0x3f7d95f4,0x3f698690,0x3f702d4f,0x3f7fb10f,0x3f77b493,0x3f58f3c4,
    0x3da0af2a,0x3db4b86a,0x3dc8bd36,0x3ddcbd0d,0x3e6f0c4d,0x3e862210,0x3e94a031,0x3ea2fd52,0x3ec3ef15,0x3edae880,0x3ef15aea,0x3f039c3d,0x3f05c277,0x3f146eba,0x3f226799,0x3f2f9c32,
    0x3f26423a,0x3f36c9a9,0x3f45e403,0x3f53731f,0x3f42a9f7,0x3f53731f,0x3f61c598,0x3f6d76aa,0x3f5a46a0,0x3f698690,0x3f74fa0b,0x3f7c7160,0x3f6c835e,0x3f7853f8,0x3f7ec46d,0x3f7fb10f,
    0x3f78ed3c,0x3f7f654e,0x3f7ec46d,0x3f770f11,0x3f7f35f9,0x3f7e8238,0x3f74fa0b,0x3f62f219,0x3f7f35f9,0x3f75b1c9,0x3f61c598,0x3f444969,0x3f78ed3c,0x3f653a48,0x3f45e403,0x3f1c81b9,
    0x3f6c835e,0x3f4d9f02,0x3f226799,0x3edae880,0x3f5a46a0,0x3f2f9c32,0x3ef15aea,0x3e6542e9,0x3f42a9f7,0x3f0c2124,0x3e94a031,0x3c20d8d2,0x3f26423a,0x3ec8917e,0x3dc8bd36,0xbe519f65,
    0x3df0b772,0x3e0255f4,0x3e0c4cf7,0x3e164083,0x3eb13641,0x3ebf47d7,0x3ecd2ef3,0x3edae880,0x3f0e39da,0x3f187fc0,0x3f226799,0x3f2beb4a,0x3f3bfc8f,0x3f4779bc,0x3f5205d4,0x3f5b941a,
    0x3f5f5be6,0x3f698690,0x3f71decf,0x3f7853f8,0x3f766377,0x3f7c7160,0x3f7f8e56,0x3f7fb10f,0x3f7fcd79,0x3f7f0059,0x3f7a0d54,0x3f710908,0x3f7b14be,0x3f710908,0x3f61c598,0x3f4d9f02,
    0x3f687c12,0x3f53731f,0x3f3889dc,0x3f187fc0,0x3f490a88,0x3f28296a,0x3f0172c3,0x3eac7cd4,0x3f1e7cf1,0x3ee3f40d,0x3e814693,0x3d48fb30,0x3ed65a92,0x3e519f65,0xbca0d6d6,0xbe78cfcc,
    0x3e47c5c2,0xbd48fb30,0xbe94a031,0xbf039c3d,0xbd20cee7,0xbe996dba,0xbf0a04f8,0xbf3daef9,0xbe8afa3e,0xbf07e565,0xbf3f5cb5,0xbf676bd8,0xbefa2c90,0xbf3a4582,0xbf6655e7,0xbf7d3aac};

__global__ __launch_bounds__(512, 2) void k_fused(const void* x, u32 x2, u32 x3, void* ws, u32 ws2, u32 ws3, const void* wr,
                                                  const void* wi, const void* w2, u32 w22, u32 w23, const void* w1, u32 w12,
                                                  u32 w13, void* out, u32 o2, u32 o3) {
#if defined(__HIP_DEVICE_COMPILE__)
    asm volatile(
        ".Lkstart:\n"
        "s_load_dwordx16 s[8:23], s[0:1], 0\n"
        "s_load_dwordx8 s[24:31], s[0:1], 64\n"
        "s_getpc_b64 s[64:65]\n"
        ".Lkpc:\n"
        "v_readfirstlane_b32 s33, v0\n"
        "v_and_b32_e32 v1, 63, v0\n"
        "s_mov_b32 s6, 0x5000\n"
        "s_mov_b32 s7, 0x20000\n"
        "s_lshr_b32 s33, s33, 6\n"
        "v_lshlrev_b32_e32 v3, 4, v1\n"
        "s_lshl_b32 s42, s33, 10\n"
        "s_add_u32 m0, s42, 0xe000\n"
        "v_lshlrev_b32_e32 v2, 2, v1\n"
        "s_and_b32 s34, s2, 7\n"
        "s_lshr_b32 s35, s2, 3\n"
        "s_lshl3_add_u32 s43, s35, s33\n"
        "s_lshl_b32 s43, s43, 8\n"
        "v_add_u32_e32 v26, s43, v2\n"
        "v_min_u32_e32 v26, .Lkend-.Lkpc-4, v26\n"
        "global_load_dword v26, v26, s[64:65]\n"
        "buffer_load_dwordx4 v3, s[4:7], s42 offen lds\n"
        "s_add_u32 s42, s42, 0x2000\n"
        "s_add_u32 m0, s42, 0xe000\n"
        "s_nop 0\n"
        "buffer_load_dwordx4 v3, s[4:7], s42 offen lds\n"
        "s_cmp_lt_u32 s33, 4\n"
        "s_cbranch_scc0 .Lk1\n"
        "s_add_u32 s42, s42, 0x2000\n"
        "s_add_u32 m0, s42, 0xe000\n"
        "s_nop 0\n"
        "buffer_load_dwordx4 v3, s[4:7], s42 offen lds\n"
        ".Lk1:\n"
        "s_lshl_b32 s36, s34, 5\n"
        "s_add_u32 s36, s36, s35\n"
        "s_lshl_b32 s37, s33, 8\n"
        "s_add_u32 s37, s37, s36\n"
        "s_mul_i32 s37, s37, 0x500\n"
        "v_lshrrev_b32_e32 v83, 5, v1\n"
        "v_and_b32_e32 v82, 7, v1\n"
        "v_cvt_f32_u32_e32 v83, v83\n"
        "v_cvt_f32_u32_e32 v82, v82\n"
        "v_cvt_f32_u32_e32 v84, s36\n"
        "v_mul_f32_e32 v83, 4.0, v83\n"
        "v_mul_f32_e32 v82, 0x3e000000, v82\n"
        "v_fmamk_f32 v83, v84, 0x3b800000, v83\n"
        "v_mul_f32_e32 v83, v83, v82\n"
        "v_fmamk_f32 v88, v82, 1.0, v83\n"
        "v_fmamk_f32 v91, v82, 2.0, v83\n"
        "v_fmamk_f32 v94, v82, 0x40400000, v83\n"
        "v_lshrrev_b32_e32 v5, 4, v1\n"
        "v_lshrrev_b32_e32 v73, 3, v1\n"
        "v_cos_f32_e32 v86, v83\n"
        "v_sin_f32_e32 v87, v83\n"
        "v_cos_f32_e32 v89, v88\n"
        "v_sin_f32_e32 v90, v88\n"
        "v_cos_f32_e32 v92, v91\n"
        "v_sin_f32_e32 v93, v91\n"
        "v_cos_f32_e32 v95, v94\n"
        "v_sin_f32_e32 v96, v94\n"
        "s_mul_i32 s44, s36, 0x2800\n"
        "v_xor_b32_e32 v73, v73, v5\n"
        "s_add_u32 s44, s44, 0x60000\n"
        "s_lshl_b32 s42, s33, 10\n"
        "v_lshrrev_b32_e32 v72, 2, v1\n"
        "v_and_b32_e32 v73, 1, v73\n"
        "s_add_u32 s44, s44, s42\n"
        "v_lshlrev_b32_e32 v72, 14, v72\n"
        "s_lshl_b32 s45, s33, 12\n"
        "v_add_u32_e32 v73, -1, v73\n"
        "v_cvt_pk_bf16_f32 v74, v86, v87\n"
        "v_cvt_pk_bf16_f32 v78, -v87, v86\n"
        "v_cvt_pk_bf16_f32 v75, v89, v90\n"
        "v_cvt_pk_bf16_f32 v79, -v90, v89\n"
        "v_cvt_pk_bf16_f32 v76, v92, v93\n"
        "v_cvt_pk_bf16_f32 v80, -v93, v92\n"
        "v_cvt_pk_bf16_f32 v77, v95, v96\n"
        "v_cvt_pk_bf16_f32 v81, -v96, v95\n"
        "s_add_u32 s45, s45, s36\n"
        "v_and_b32_e32 v4, 15, v1\n"
        "v_mov_b32_e32 v14, -1\n"
        "v_mov_b32_e32 v15, -1\n"
        "v_mov_b32_e32 v16, -1\n"
        "v_mov_b32_e32 v17, -1\n"
        "v_and_or_b32 v45, v3, 48, v72\n"
        "s_lshl_b32 s45, s45, 6\n"
        "v_and_b32_e32 v74, v73, v74\n"
        "v_and_b32_e32 v78, v73, v78\n"
        "v_and_b32_e32 v75, v73, v75\n"
        "v_and_b32_e32 v79, v73, v79\n"
        "v_and_b32_e32 v76, v73, v76\n"
        "v_and_b32_e32 v80, v73, v80\n"
        "v_and_b32_e32 v77, v73, v77\n"
        "v_and_b32_e32 v81, v73, v81\n"
        "s_add_u32 s45, s45, 0x2e0000\n"
        "s_waitcnt lgkmcnt(0)\n"
        "buffer_load_dword v7, v2, s[8:11], s37 offen\n"
        "buffer_load_dword v8, v2, s[8:11], s37 offen offset:256\n"
        "buffer_load_dword v9, v2, s[8:11], s37 offen offset:512\n"
        "buffer_load_dword v10, v2, s[8:11], s37 offen offset:768\n"
        "buffer_load_dword v11, v2, s[8:11], s37 offen offset:1024\n"
        "buffer_load_dword v12, off, s[12:15], 0 sc1\n"
        "s_barrier\n"
        "buffer_store_dwordx4 v[14:17], v3, s[12:15], s44 offen sc1\n"
        "s_cmp_ge_u32 s33, 6\n"
        "s_cbranch_scc0 .Lk2\n"
        "s_add_u32 s44, s44, 0x800\n"
        "buffer_store_dwordx4 v[14:17], v3, s[12:15], s44 offen sc1\n"
        ".Lk2:\n"
        "v_mul_u32_u24_e32 v40, 0x50, v5\n"
        "v_add_lshl_u32 v40, v40, v4, 2\n"
        "s_mul_i32 s42, s33, 0x600\n"
        "s_add_u32 s42, s42, 0x2300c\n"
        "v_add_u32_e32 v97, s42, v40\n"
        "s_add_u32 s43, s42, 0x410\n"
        "v_lshrrev_b32_e32 v101, 5, v1\n"
        "v_sub_u32_e32 v98, s43, v40\n"
        "v_mul_u32_u24_e32 v101, 0x1800, v101\n"
        "v_and_b32_e32 v99, 0x7c, v2\n"
        "s_lshl_b32 s42, s33, 7\n"
        "s_add_u32 s43, s42, 0x23010\n"
        "s_mul_i32 s46, s36, 0x2800\n"
        "v_add_u32_e32 v99, v99, v101\n"
        "s_add_u32 s46, s46, 0x60000\n"
        "s_add_u32 s46, s46, s42\n"
        "v_add_u32_e32 v99, s43, v99\n"
        "s_add_u32 s47, s46, 0x400\n"
        "s_and_b32 s42, s36, 0xc7\n"
        "s_bfe_u32 s43, s36, 0x10003\n"
        "s_lshl_b32 s43, s43, 5\n"
        "s_or_b32 s42, s42, s43\n"
        "s_bfe_u32 s43, s36, 0x20004\n"
        "s_lshl_b32 s43, s43, 3\n"
        "s_or_b32 s42, s42, s43\n"
        "s_mul_i32 s48, s42, 32\n"
        "s_add_u32 s48, s48, 0x10000\n"
        "s_lshl_b32 s42, s33, 15\n"
        "s_add_u32 s48, s48, s42\n"
        "s_lshl_b32 s49, s34, 15\n"
        "s_mul_i32 s42, s33, 0x400\n"
        "s_add_u32 s49, s49, s42\n"
        "s_add_u32 s49, s49, 0x10000\n"
        "v_bfe_u32 v110, v1, 2, 2\n"
        "s_lshr_b32 s42, s33, 1\n"
        "v_lshl_add_u32 v110, v110, 4, v5\n"
        "s_lshl_b32 s42, s42, 6\n"
        "s_and_b32 s43, s33, 1\n"
        "v_mul_u32_u24_e32 v30, 0x1410, v110\n"
        "s_lshl3_add_u32 s43, s43, s42\n"
        "s_lshl3_add_u32 s43, s43, s34\n"
        "v_and_b32_e32 v110, 3, v1\n"
        "s_mul_i32 s43, s43, 0x282\n"
        "s_add_u32 s43, s43, 2\n"
        "v_lshl_add_u32 v30, v110, 3, v30\n"
        "s_lshl_b32 s42, s35, 5\n"
        "s_add_u32 s43, s43, s42\n"
        "v_add_u32_e32 v30, s43, v30\n"
        "v_min_u32_e32 v112, 41, v1\n"
        "v_and_b32_e32 v101, 7, v1\n"
        "v_mul_u32_u24_e32 v111, 43, v112\n"
        "s_and_b32 s43, s33, 1\n"
        "s_mul_i32 s43, s43, 6\n"
        "s_mul_i32 s42, s35, 13\n"
        "v_mul_u32_u24_e32 v100, 0x500, v101\n"
        "v_lshrrev_b32_e32 v111, 8, v111\n"
        "s_add_u32 s42, s42, s43\n"
        "s_min_u32 s42, s42, 0xf9\n"
        "v_and_b32_e32 v101, 8, v1\n"
        "v_mul_u32_u24_e32 v111, 0x1110, v111\n"
        "s_lshl_b32 s42, s42, 3\n"
        "s_add_u32 s42, s42, s34\n"
        "v_lshl_add_u32 v100, v101, 3, v100\n"
        "v_and_b32_e32 v42, 31, v1\n"
        "v_lshl_add_u32 v111, v112, 7, v111\n"
        "s_mul_i32 s42, s42, 0x282\n"
        "s_and_b32 s42, s42, -4\n"
        "v_mov_b32_e32 v13, 0x3acccccd\n"
        "v_lshl_add_u32 v100, v5, 4, v100\n"
        "v_mov_b32_e32 v105, 0\n"
        "v_mov_b32_e32 v109, 0\n"
        "v_add_u32_e32 v41, 0xe000, v3\n"
        "v_mul_u32_u24_e32 v42, 32, v42\n"
        "v_add_u32_e32 v31, 0x5040, v30\n"
        "v_add_u32_e32 v111, s42, v111\n"
        "v_mul_u32_u24_e32 v28, 0x88, v1\n"
        "s_mov_b32 s38, 0x8000\n"
        "s_mov_b32 s40, 0x3f803f80\n"
        "buffer_store_dwordx4 v[14:17], v45, s[12:15], s45 offen sc1\n"
        "s_cmp_ge_u32 s33, 6\n"
        "s_cbranch_scc0 .Lk3\n"
        "s_add_u32 s45, s45, 0x80000\n"
        "buffer_store_dwordx4 v[14:17], v45, s[12:15], s45 offen sc1\n"
        ".Lk3:\n"
        "s_and_b32 s42, s34, 1\n"
        "s_lshl_b32 s42, s42, 3\n"
        "s_and_b32 s41, s35, 7\n"
        "s_or_b32 s41, s41, s42\n"
        "s_max_u32 s42, s41, 10\n"
        "s_min_u32 s42, s42, 14\n"
        "s_mul_i32 s50, s41, 7\n"
        "s_sub_u32 s50, s50, s42\n"
        "s_add_u32 s50, s50, 10\n"
        "s_lshl_b32 s50, s50, 5\n"
        "s_mul_i32 s42, s33, 0x1b000\n"
        "s_add_u32 s50, s50, s42\n"
        "s_mul_i32 s51, s33, 0x1c00\n"
        "s_cmp_lt_u32 s35, 20\n"
        "s_cbranch_scc1 .Lk4\n"
        "s_mul_i32 s52, s34, 12\n"
        "s_add_u32 s52, s52, s35\n"
        "s_sub_u32 s52, s52, 20\n"
        "s_min_u32 s58, s33, 5\n"
        "s_mul_i32 s42, s52, 6\n"
        "s_add_u32 s42, s42, s58\n"
        "s_min_u32 s42, s42, 0x22f\n"
        "s_mul_i32 s43, s42, 0x199a\n"
        "s_lshr_b32 s43, s43, 16\n"
        "s_mul_i32 s55, s43, 10\n"
        "s_sub_u32 s55, s42, s55\n"
        "s_mul_i32 s53, s43, 0x2493\n"
        "s_lshr_b32 s53, s53, 16\n"
        "s_mul_i32 s54, s53, 7\n"
        "s_sub_u32 s54, s43, s54\n"
        "s_lshl_b32 s42, s53, 1\n"
        "s_max_u32 s43, s42, 10\n"
        "s_min_u32 s43, s43, 14\n"
        "s_mul_i32 s56, s42, 7\n"
        "s_sub_u32 s56, s56, s43\n"
        "s_add_u32 s56, s56, 10\n"
        "s_add_u32 s42, s42, 1\n"
        "s_max_u32 s43, s42, 10\n"
        "s_min_u32 s43, s43, 14\n"
        "s_mul_i32 s57, s42, 7\n"
        "s_sub_u32 s57, s57, s43\n"
        "s_add_u32 s57, s57, 10\n"
        "s_sub_u32 s42, s57, 6\n"
        "s_cmp_lt_u32 s54, 3\n"
        "s_cselect_b32 s42, s56, s42\n"
        "s_lshl1_add_u32 s42, s54, s42\n"
        "s_add_u32 s56, s56, 6\n"
        "s_add_u32 s57, s57, 6\n"
        "s_add_u32 s43, s42, 1\n"
        "s_cmp_lt_u32 s54, 6\n"
        "s_cselect_b32 s56, s42, s56\n"
        "s_cselect_b32 s57, s43, s57\n"
        "s_lshl_b32 s42, s55, 7\n"
        "s_mul_i32 s56, s56, 0x5000\n"
        "s_add_u32 s56, s56, s42\n"
        "s_mul_i32 s57, s57, 0x5000\n"
        "s_add_u32 s57, s57, s42\n"
        "v_lshrrev_b32_e32 v113, 3, v1\n"
        "v_mul_u32_u24_e32 v113, 0x500, v113\n"
        "v_and_b32_e32 v114, 0x70, v3\n"
        "v_add_u32_e32 v113, v113, v114\n"
        "s_lshl_b32 s58, s58, 12\n"
        "s_add_u32 s58, s58, 0x1c000\n"
        ".Lk4:\n"
        "s_cmp_lt_u32 s35, 20\n"
        "s_cbranch_scc1 .Lk5\n"
        "s_cmp_ge_u32 s33, 5\n"
        "s_cbranch_scc0 .Lk6\n"
        "s_mul_i32 s42, s52, 3\n"
        "s_add_u32 s42, s42, s33\n"
        "s_sub_u32 s42, s42, 5\n"
        "s_mul_i32 s42, s42, 0x1e00\n"
        "v_lshlrev_b32_e32 v28, 3, v3\n"
        "buffer_load_dword v29, v28, s[24:27], s42 offen\n"
        ".Lk6:\n"
        ".Lk5:\n"
        "v_lshlrev_b32_e32 v43, 1, v2\n"
        "s_cmp_lt_u32 s35, 20\n"
        "s_cbranch_scc0 .Lk7\n"
        "s_cmp_lt_u32 s33, 4\n"
        "s_cbranch_scc0 .Lk8\n"
        "s_waitcnt vmcnt(1)\n"
        "s_branch .Lk9\n"
        ".Lk8:\n"
        "s_cmp_lt_u32 s33, 6\n"
        "s_cbranch_scc0 .Lk10\n"
        "s_cmp_lt_u32 s33, 5\n"
        "s_cbranch_scc0 .Lk11\n"
        "s_waitcnt vmcnt(1)\n"
        "s_branch .Lk12\n"
        ".Lk11:\n"
        "s_waitcnt vmcnt(1)\n"
        ".Lk12:\n"
        "s_branch .Lk13\n"
        ".Lk10:\n"
        "s_waitcnt vmcnt(2)\n"
        ".Lk13:\n"
        ".Lk9:\n"
        "s_branch .Lk14\n"
        ".Lk7:\n"
        "s_cmp_lt_u32 s33, 4\n"
        "s_cbranch_scc0 .Lk15\n"
        "s_waitcnt vmcnt(1)\n"
        "s_branch .Lk16\n"
        ".Lk15:\n"
        "s_cmp_lt_u32 s33, 6\n"
        "s_cbranch_scc0 .Lk17\n"
        "s_cmp_lt_u32 s33, 5\n"
        "s_cbranch_scc0 .Lk18\n"
        "s_waitcnt vmcnt(1)\n"
        "s_branch .Lk19\n"
        ".Lk18:\n"
        "s_waitcnt vmcnt(2)\n"
        ".Lk19:\n"
        "s_branch .Lk20\n"
        ".Lk17:\n"
        "s_waitcnt vmcnt(3)\n"
        ".Lk20:\n"
        ".Lk16:\n"
        ".Lk14:\n"
        "v_readfirstlane_b32 s39, v12\n"
        "s_barrier\n"
        "s_memrealtime s[66:67]\n"
        "s_add_u32 s39, s39, 1\n"
        "v_mov_b32_e32 v46, s39\n"
        "s_waitcnt lgkmcnt(0)\n"
        "v_mov_b32_e32 v47, s66\n"
        "s_mov_b64 exec, 15\n"
        "buffer_store_dwordx2 v[46:47], v43, s[12:15], s48 offen sc1\n"
        "s_mov_b64 exec, -1\n"
        "s_cmp_lt_u32 s35, 20\n"
        "s_cbranch_scc0 .Lk21\n"
        "s_cmp_ge_u32 s33, 4\n"
        "s_cbranch_scc0 .Lk22\n"
        "s_cmp_lt_u32 s33, 6\n"
        "s_cbranch_scc0 .Lk23\n"
        "global_load_dword v6, v111, s[16:17]\n"
        "s_branch .Lk24\n"
        ".Lk23:\n"
        "global_load_dword v6, v111, s[18:19]\n"
        ".Lk24:\n"
        ".Lk22:\n"
        ".Lk21:\n"
        "ds_read_b128 v[116:119], v41\n"
        "v_dot2_f32_bf16 v157, v7, s40, 0\n"
        "v_dot2_f32_bf16 v158, v7, v7, 0\n"
        "v_dot2_f32_bf16 v157, v8, s40, v157\n"
        "ds_read_b128 v[120:123], v41 offset:1024\n"
        "v_dot2_f32_bf16 v158, v8, v8, v158\n"
        "v_dot2_f32_bf16 v157, v9, s40, v157\n"
        "v_dot2_f32_bf16 v158, v9, v9, v158\n"
        "ds_read_b128 v[132:135], v41 offset:2048\n"
        "v_dot2_f32_bf16 v157, v10, s40, v157\n"
        "v_dot2_f32_bf16 v158, v10, v10, v158\n"
        "v_dot2_f32_bf16 v157, v11, s40, v157\n"
        "ds_read_b128 v[140:143], v41 offset:3072\n"
        "v_dot2_f32_bf16 v158, v11, v11, v158\n"
        "s_nop 0\n"
        "v_add_f32_dpp v157, v157, v157 quad_perm:[1,0,3,2] row_mask:0xf bank_mask:0xf\n"
        "v_lshlrev_b32_e32 v115, 16, v7\n"
        "ds_read_b128 v[136:139], v41 offset:4096\n"
        "v_add_f32_dpp v158, v158, v158 quad_perm:[1,0,3,2] row_mask:0xf bank_mask:0xf\n"
        "v_add_f32_dpp v157, v157, v157 quad_perm:[2,3,0,1] row_mask:0xf bank_mask:0xf\n"
        "v_and_b32_e32 v152, 0xffff0000, v7\n"
        "ds_read_b128 v[144:147], v41 offset:5120\n"
        "v_add_f32_dpp v158, v158, v158 quad_perm:[2,3,0,1] row_mask:0xf bank_mask:0xf\n"
        "v_add_f32_dpp v157, v157, v157 row_ror:4 row_mask:0xf bank_mask:0xf\n"
        "v_lshlrev_b32_e32 v148, 16, v8\n"
        "ds_read_b128 v[48:51], v41 offset:6144\n"
        "v_add_f32_dpp v158, v158, v158 row_ror:4 row_mask:0xf bank_mask:0xf\n"
        "v_add_f32_dpp v157, v157, v157 row_ror:8 row_mask:0xf bank_mask:0xf\n"
        "v_and_b32_e32 v153, 0xffff0000, v8\n"
        "ds_read_b128 v[52:55], v41 offset:7168\n"
        "v_add_f32_dpp v158, v158, v158 row_ror:8 row_mask:0xf bank_mask:0xf\n"
        "v_add_f32_dpp v157, v157, v157 row_bcast:15 row_mask:0xa bank_mask:0xf\n"
        "v_lshlrev_b32_e32 v149, 16, v9\n"
        "ds_read_b128 v[124:127], v41 offset:8192\n"
        "v_add_f32_dpp v158, v158, v158 row_bcast:15 row_mask:0xa bank_mask:0xf\n"
        "v_add_f32_dpp v157, v157, v157 row_bcast:31 row_mask:0xc bank_mask:0xf\n"
        "v_and_b32_e32 v154, 0xffff0000, v9\n"
        "ds_read_b128 v[128:131], v41 offset:9216\n"
        "v_add_f32_dpp v158, v158, v158 row_bcast:31 row_mask:0xc bank_mask:0xf\n"
        "v_readlane_b32 s59, v157, 63\n"
        "v_readlane_b32 s60, v158, 63\n"
        "v_lshlrev_b32_e32 v150, 16, v10\n"
        "v_mul_f32_e32 v157, s59, v13\n"
        "v_mul_f32_e32 v158, s60, v13\n"
        "v_fma_f32 v158, -v157, v157, v158\n"
        "v_max_f32_e32 v158, 0, v158\n"
        "v_add_f32_e32 v158, 0x3727c5ac, v158\n"
        "v_rsq_f32_e32 v158, v158\n"
        "v_and_b32_e32 v155, 0xffff0000, v10\n"
        "v_mul_f32_e64 v157, -v157, v158\n"
        "v_lshlrev_b32_e32 v151, 16, v11\n"
        "v_and_b32_e32 v156, 0xffff0000, v11\n"
        "v_fma_f32 v115, v115, v158, v157\n"
        "v_fma_f32 v148, v148, v158, v157\n"
        "v_fma_f32 v149, v149, v158, v157\n"
        "v_fma_f32 v150, v150, v158, v157\n"
        "v_fma_f32 v151, v151, v158, v157\n"
        "v_fma_f32 v152, v152, v158, v157\n"
        "v_fma_f32 v153, v153, v158, v157\n"
        "v_fma_f32 v154, v154, v158, v157\n"
        "v_fma_f32 v155, v155, v158, v157\n"
        "v_fma_f32 v156, v156, v158, v157\n"
        "v_cvt_pk_bf16_f32 v102, v115, v148\n"
        "v_cvt_pk_bf16_f32 v103, v149, v150\n"
        "v_cvt_pk_bf16_f32 v104, v151, 0\n"
        "v_cvt_pk_bf16_f32 v106, v152, v153\n"
        "v_cvt_pk_bf16_f32 v107, v154, v155\n"
        "v_cvt_pk_bf16_f32 v108, v156, 0\n"
        "s_waitcnt lgkmcnt(9)\n"
        "v_mfma_f32_16x16x32_bf16 v[160:163], v[102:105], v[116:119], 0\n"
        "s_waitcnt lgkmcnt(8)\n"
        "v_mfma_f32_16x16x32_bf16 v[164:167], v[102:105], v[120:123], 0\n"
        "v_mfma_f32_16x16x32_bf16 v[168:171], v[106:109], v[116:119], 0\n"
        "v_mfma_f32_16x16x32_bf16 v[172:175], v[106:109], v[120:123], 0\n"
        "s_waitcnt lgkmcnt(7)\n"
        "v_mul_f32_e32 v159, v160, v132\n"
        "s_nop 1\n"
        "v_mul_f32_e32 v188, v164, v132\n"
        "v_mul_f32_e32 v189, v161, v133\n"
        "v_mul_f32_e32 v190, v165, v133\n"
        "v_mul_f32_e32 v191, v162, v134\n"
        "v_mul_f32_e32 v192, v166, v134\n"
        "v_mul_f32_e32 v193, v163, v135\n"
        "v_mul_f32_e32 v194, v167, v135\n"
        "s_waitcnt lgkmcnt(6)\n"
        "v_fmac_f32_e32 v159, v164, v140\n"
        "v_fma_f32 v188, -v160, v140, v188\n"
        "v_fmac_f32_e32 v189, v165, v141\n"
        "v_fma_f32 v190, -v161, v141, v190\n"
        "v_fmac_f32_e32 v191, v166, v142\n"
        "v_fma_f32 v192, -v162, v142, v192\n"
        "v_fmac_f32_e32 v193, v167, v143\n"
        "v_fma_f32 v194, -v163, v143, v194\n"
        "v_cvt_pk_bf16_f32 v176, v159, v188\n"
        "v_cvt_pk_bf16_f32 v177, v189, v190\n"
        "v_cvt_pk_bf16_f32 v178, v191, v192\n"
        "v_cvt_pk_bf16_f32 v179, v193, v194\n"
        "s_waitcnt lgkmcnt(5)\n"
        "v_mul_f32_e32 v195, v168, v136\n"
        "v_mul_f32_e32 v196, v172, v136\n"
        "v_mul_f32_e32 v197, v169, v137\n"
        "v_mul_f32_e32 v198, v173, v137\n"
        "v_mul_f32_e32 v199, v170, v138\n"
        "v_mul_f32_e32 v200, v174, v138\n"
        "v_mul_f32_e32 v201, v171, v139\n"
        "v_mul_f32_e32 v202, v175, v139\n"
        "s_waitcnt lgkmcnt(4)\n"
        "v_fmac_f32_e32 v195, v172, v144\n"
        "v_fma_f32 v196, -v168, v144, v196\n"
        "v_fmac_f32_e32 v197, v173, v145\n"
        "v_fma_f32 v198, -v169, v145, v198\n"
        "v_fmac_f32_e32 v199, v174, v146\n"
        "v_fma_f32 v200, -v170, v146, v200\n"
        "v_fmac_f32_e32 v201, v175, v147\n"
        "v_fma_f32 v202, -v171, v147, v202\n"
        "s_waitcnt lgkmcnt(3)\n"
        "v_mfma_f32_16x16x32_bf16 v[204:207], v[48:51], v[176:179], 0\n"
        "v_cvt_pk_bf16_f32 v180, v195, v196\n"
        "v_cvt_pk_bf16_f32 v181, v197, v198\n"
        "s_waitcnt lgkmcnt(2)\n"
        "v_mfma_f32_16x16x32_bf16 v[208:211], v[52:55], v[176:179], 0\n"
        "v_cvt_pk_bf16_f32 v182, v199, v200\n"
        "v_cvt_pk_bf16_f32 v183, v201, v202\n"
        "v_xor_b32_e32 v184, 0x80008000, v180\n"
        "v_xor_b32_e32 v185, 0x80008000, v181\n"
        "v_xor_b32_e32 v186, 0x80008000, v182\n"
        "v_xor_b32_e32 v187, 0x80008000, v183\n"
        "s_waitcnt lgkmcnt(1)\n"
        "v_mfma_f32_16x16x32_bf16 v[212:215], v[124:127], v[180:183], v[204:207]\n"
        "s_waitcnt lgkmcnt(0)\n"
        "v_mfma_f32_16x16x32_bf16 v[216:219], v[128:131], v[180:183], v[208:211]\n"
        "v_mfma_f32_16x16x32_bf16 v[220:223], v[124:127], v[184:187], v[204:207]\n"
        "v_mfma_f32_16x16x32_bf16 v[86:89], v[128:131], v[184:187], v[208:211]\n"
        "s_nop 3\n"
        "v_cvt_pk_bf16_f32 v203, v212, v216\n"
        "v_cvt_pk_bf16_f32 v72, v213, v217\n"
        "v_cvt_pk_bf16_f32 v85, v214, v218\n"
        "v_cvt_pk_bf16_f32 v90, v215, v219\n"
        "ds_write2_b32 v97, v203, v72 offset1:20\n"
        "ds_write2_b32 v97, v85, v90 offset0:40 offset1:60\n"
        "v_cvt_pk_bf16_f32 v91, v220, -v86\n"
        "v_cvt_pk_bf16_f32 v92, v221, -v87\n"
        "v_cvt_pk_bf16_f32 v93, v222, -v88\n"
        "v_cvt_pk_bf16_f32 v94, v223, -v89\n"
        "ds_write2_b32 v98, v91, v92 offset0:60 offset1:40\n"
        "ds_write2_b32 v98, v93, v94 offset0:20\n"
        "s_cmp_lt_u32 s35, 20\n"
        "s_cbranch_scc0 .Lk25\n"
        "s_cmp_lt_u32 s33, 4\n"
        "s_cbranch_scc0 .Lk26\n"
        "s_waitcnt vmcnt(1)\n"
        "s_branch .Lk27\n"
        ".Lk26:\n"
        "s_cmp_lt_u32 s33, 6\n"
        "s_cbranch_scc0 .Lk28\n"
        "s_cmp_lt_u32 s33, 5\n"
        "s_cbranch_scc0 .Lk29\n"
        "s_waitcnt vmcnt(2)\n"
        "s_branch .Lk30\n"
        ".Lk29:\n"
        "s_waitcnt vmcnt(2)\n"
        ".Lk30:\n"
        "s_branch .Lk31\n"
        ".Lk28:\n"
        "s_waitcnt vmcnt(2)\n"
        ".Lk31:\n"
        ".Lk27:\n"
        "s_branch .Lk32\n"
        ".Lk25:\n"
        "s_cmp_lt_u32 s33, 4\n"
        "s_cbranch_scc0 .Lk33\n"
        "s_waitcnt vmcnt(1)\n"
        "s_branch .Lk34\n"
        ".Lk33:\n"
        "s_cmp_lt_u32 s33, 6\n"
        "s_cbranch_scc0 .Lk35\n"
        "s_cmp_lt_u32 s33, 5\n"
        "s_cbranch_scc0 .Lk36\n"
        "s_waitcnt vmcnt(1)\n"
        "s_branch .Lk37\n"
        ".Lk36:\n"
        "s_waitcnt vmcnt(2)\n"
        ".Lk37:\n"
        "s_branch .Lk38\n"
        ".Lk35:\n"
        "s_waitcnt vmcnt(2)\n"
        ".Lk38:\n"
        ".Lk34:\n"
        ".Lk32:\n"
        "s_waitcnt lgkmcnt(0)\n"
        "s_barrier\n"
        "s_cmp_lt_u32 s33, 2\n"
        "s_cbranch_scc0 .Lk39\n"
        "ds_read2st64_b32 v[82:83], v99 offset1:6\n"
        "v_add_u32_e32 v95, 0x400, v99\n"
        "ds_read2st64_b32 v[84:85], v99 offset0:12 offset1:18\n"
        "ds_read2st64_b32 v[156:157], v95 offset1:6\n"
        "ds_read2st64_b32 v[158:159], v95 offset0:12 offset1:18\n"
        "s_waitcnt lgkmcnt(2)\n"
        "v_mfma_f32_16x16x32_bf16 v[114:117], v[82:85], v[74:77], 0\n"
        "v_mfma_f32_16x16x32_bf16 v[148:151], v[82:85], v[78:81], 0\n"
        "s_waitcnt lgkmcnt(0)\n"
        "v_mfma_f32_16x16x32_bf16 v[188:191], v[156:159], v[74:77], 0\n"
        "v_mfma_f32_16x16x32_bf16 v[192:195], v[156:159], v[78:81], 0\n"
        "s_nop 2\n"
        "v_cvt_pk_bf16_f32 v152, v114, v148\n"
        "v_cvt_pk_bf16_f32 v153, v115, v149\n"
        "v_cvt_pk_bf16_f32 v154, v116, v150\n"
        "v_cvt_pk_bf16_f32 v155, v117, v151\n"
        "buffer_store_dwordx4 v[152:155], v100, s[12:15], s46 offen sc1\n"
        "v_cvt_pk_bf16_f32 v196, v188, v192\n"
        "v_cvt_pk_bf16_f32 v197, v189, v193\n"
        "v_cvt_pk_bf16_f32 v198, v190, v194\n"
        "v_cvt_pk_bf16_f32 v199, v191, v195\n"
        "buffer_store_dwordx4 v[196:199], v100, s[12:15], s47 offen sc1\n"
        "s_branch .Lk40\n"
        ".Lk39:\n"
        "ds_read2st64_b32 v[200:201], v99 offset1:6\n"
        "ds_read2st64_b32 v[202:203], v99 offset0:12 offset1:18\n"
        "s_waitcnt lgkmcnt(0)\n"
        "v_mfma_f32_16x16x32_bf16 v[160:163], v[200:203], v[74:77], 0\n"
        "v_mfma_f32_16x16x32_bf16 v[164:167], v[200:203], v[78:81], 0\n"
        "s_nop 7\n"
        "v_cvt_pk_bf16_f32 v168, v160, v164\n"
        "v_cvt_pk_bf16_f32 v169, v161, v165\n"
        "v_cvt_pk_bf16_f32 v170, v162, v166\n"
        "v_cvt_pk_bf16_f32 v171, v163, v167\n"
        "buffer_store_dwordx4 v[168:171], v100, s[12:15], s46 offen sc1\n"
        ".Lk40:\n"
        "s_waitcnt lgkmcnt(0)\n"
        "s_barrier\n"
        "s_cmp_lt_u32 s35, 20\n"
        "s_cbranch_scc1 .Lk41\n"
        "s_cmp_lt_u32 s33, 6\n"
        "s_cbranch_scc0 .Lk42\n"
        "s_mov_b32 m0, s58\n"
        "s_add_u32 s43, s56, 0\n"
        "buffer_load_dwordx4 v113, s[20:23], s43 offen lds\n"
        "s_add_u32 s43, s56, 0x2400\n"
        "buffer_load_dwordx4 v113, s[20:23], s43 offen offset:1024 lds\n"
        "s_add_u32 s43, s57, 0xfffff800\n"
        "buffer_load_dwordx4 v113, s[20:23], s43 offen offset:2048 lds\n"
        "s_add_u32 s43, s57, 0x1c00\n"
        "buffer_load_dwordx4 v113, s[20:23], s43 offen offset:3072 lds\n"
        ".Lk42:\n"
        ".Lk41:\n"
        "s_cmp_lt_u32 s35, 20\n"
        "s_cbranch_scc0 .Lk43\n"
        "v_and_b32_e32 v90, 3, v1\n"
        "s_lshr_b32 s42, s33, 1\n"
        "v_mul_u32_u24_e32 v85, 0x408, v90\n"
        "s_lshl_b32 s42, s42, 6\n"
        "v_bfe_u32 v89, v1, 2, 2\n"
        "s_and_b32 s43, s33, 1\n"
        "v_lshl_add_u32 v85, v5, 2, v85\n"
        "s_lshl3_add_u32 s43, s43, s42\n"
        "v_add_lshl_u32 v85, v85, v89, 2\n"
        "s_lshl_b32 s60, s33, 7\n"
        "s_add_u32 s60, s60, 0x23000\n"
        "v_add_u32_e32 v85, s60, v85\n"
        "s_lshr_b32 s60, s33, 1\n"
        "s_lshl_b32 s60, s60, 5\n"
        "v_lshl_add_u32 v86, v89, 4, v5\n"
        "s_lshl_b32 s42, s33, 11\n"
        "s_add_u32 s60, s60, s42\n"
        "v_add_u32_e32 v86, s43, v86\n"
        "s_add_u32 s60, s60, 0x1c000\n"
        "s_mul_i32 s46, s34, 0x500\n"
        "s_add_u32 s46, s46, 0x60000\n"
        "v_lshlrev_b32_e32 v87, 6, v86\n"
        "s_lshl_b32 s42, s35, 6\n"
        "s_add_u32 s46, s46, s42\n"
        "v_mul_u32_u24_e32 v86, 0x2800, v86\n"
        "s_add_u32 s47, s46, 0xa000\n"
        "s_mul_i32 s56, s34, 20\n"
        "s_add_u32 s56, s56, s35\n"
        "v_lshl_add_u32 v87, v90, 4, v87\n"
        "v_lshl_add_u32 v86, v90, 4, v86\n"
        "v_add_u32_e32 v91, 64, v85\n"
        "v_add_u32_e32 v88, s60, v3\n"
        "s_lshl_b32 s56, s56, 14\n"
        "s_add_u32 s56, s56, 0x2e0000\n"
        "s_sleep 8\n"
        "buffer_load_dwordx2 v[46:47], v42, s[12:15], s49 offen sc1\n"
        "global_load_dwordx2 v[18:19], v30, s[16:17]\n"
        "global_load_dwordx2 v[20:21], v30, s[18:19]\n"
        "ds_read_b128 v[56:59], v41 offset:10240\n"
        "ds_read_b128 v[60:63], v41 offset:11264\n"
        "ds_read_b128 v[224:227], v41 offset:12288\n"
        "ds_read_b128 v[228:231], v41 offset:13312\n"
        "ds_read_b128 v[232:235], v41 offset:14336\n"
        "ds_read_b128 v[236:239], v41 offset:15360\n"
        "ds_read_b128 v[240:243], v41 offset:16384\n"
        "ds_read_b128 v[244:247], v41 offset:17408\n"
        "ds_read_b128 v[248:251], v41 offset:18432\n"
        "ds_read_b128 v[252:255], v41 offset:19456\n"
        "v_mul_u32_u24_e32 v92, 0x124a, v1\n"
        "v_lshrrev_b32_e32 v32, 16, v92\n"
        "v_add_u32_e32 v33, 0x49280, v92\n"
        "v_add_u32_e32 v34, 0x92500, v92\n"
        "v_add_u32_e32 v35, 0xdb780, v92\n"
        "v_add_u32_e32 v36, 0x124a00, v92\n"
        "v_add_u32_e32 v37, 0x16dc80, v92\n"
        "v_add_u32_e32 v38, 0x1b6f00, v92\n"
        "v_lshrrev_b32_e32 v92, 2, v1\n"
        "v_lshl_add_u32 v92, v5, 2, v92\n"
        "v_lshrrev_b32_e32 v36, 16, v36\n"
        "v_lshrrev_b32_e32 v37, 16, v37\n"
        "v_lshrrev_b32_e32 v38, 16, v38\n"
        "v_mul_u32_u24_e32 v39, 0xe0, v92\n"
        "s_movk_i32 s57, 0xca0\n"
        "v_lshrrev_b32_e32 v33, 16, v33\n"
        "v_lshrrev_b32_e32 v34, 16, v34\n"
        "v_lshrrev_b32_e32 v35, 16, v35\n"
        "v_mad_u32_u24 v36, v36, s57, v3\n"
        "v_mad_u32_u24 v37, v37, s57, v3\n"
        "v_mad_u32_u24 v38, v38, s57, v3\n"
        "v_and_b32_e32 v92, 3, v1\n"
        "v_mad_u32_u24 v32, v32, s57, v3\n"
        "v_mad_u32_u24 v33, v33, s57, v3\n"
        "v_mad_u32_u24 v34, v34, s57, v3\n"
        "v_mad_u32_u24 v35, v35, s57, v3\n"
        "v_add_u32_e32 v36, 0x1000, v36\n"
        "v_add_u32_e32 v37, 0x1000, v37\n"
        "v_add_u32_e32 v38, 0x1000, v38\n"
        "v_lshl_add_u32 v39, v92, 3, v39\n"
        "s_and_b32 s42, s2, 25\n"
        "s_lshl_b32 s67, s42, 5\n"
        "s_or_b32 s42, s42, s67\n"
        "s_bfe_u32 s42, s42, 0x30003\n"
        "s_lshl3_add_u32 s42, s42, s33\n"
        "s_mul_i32 s61, s42, 0x8c00\n"
        "s_add_u32 s61, s61, 0xf88000\n"
        "v_lshrrev_b32_e32 v93, 5, v1\n"
        "v_mul_u32_u24_e32 v12, 0x140000, v93\n"
        "v_and_b32_e32 v93, 16, v1\n"
        "s_lshl_b32 s63, s33, 9\n"
        "v_lshl_add_u32 v12, v93, 10, v12\n"
        "s_add_u32 s63, s63, s36\n"
        "s_lshl_b32 s63, s63, 6\n"
        "v_lshl_add_u32 v12, v4, 2, v12\n"
        "s_add_u32 s63, s63, 0x2e0000\n"
        "s_add_u32 s3, s63, 0x50000\n"
        "v_add_u32_e32 v45, 0x40000, v12\n"
        "s_add_u32 s44, s63, 0xa0000\n"
        "s_add_u32 s45, s63, 0xf0000\n"
        "s_cmp_lt_u32 s33, 2\n"
        "s_cbranch_scc0 .Lk44\n"
        "s_waitcnt vmcnt(2)\n"
        "s_branch .Lk45\n"
        ".Lk44:\n"
        "s_waitcnt vmcnt(2)\n"
        ".Lk45:\n"
        "v_xor_b32_e32 v6, s39, v46\n"
        "v_cmp_ne_u32_e32 vcc, 0, v6\n"
        "s_cbranch_vccnz .Lk46\n"
        ".Lk47:\n"
        "v_subrev_u32_e32 v6, s66, v47\n"
        "s_nop 1\n"
        "v_max_i32_dpp v6, v6, v6 quad_perm:[1,0,3,2] row_mask:0xf bank_mask:0xf\n"
        "s_nop 1\n"
        "v_max_i32_dpp v6, v6, v6 quad_perm:[2,3,0,1] row_mask:0xf bank_mask:0xf\n"
        "s_nop 1\n"
        "v_max_i32_dpp v6, v6, v6 row_ror:4 row_mask:0xf bank_mask:0xf\n"
        "s_nop 1\n"
        "v_max_i32_dpp v6, v6, v6 row_ror:8 row_mask:0xf bank_mask:0xf\n"
        "s_nop 1\n"
        "v_max_i32_dpp v6, v6, v6 row_bcast:15 row_mask:0xa bank_mask:0xf\n"
        "s_nop 1\n"
        "v_max_i32_dpp v6, v6, v6 row_bcast:31 row_mask:0xc bank_mask:0xf\n"
        "s_nop 0\n"
        "v_readlane_b32 s58, v6, 63\n"
        "s_nop 0\n"
        "s_add_u32 s58, s58, s66\n"
        "s_add_u32 s58, s58, 0x6e\n"
        "s_movk_i32 s60, 0x64\n"
        ".Lk48:\n"
        "s_memrealtime s[66:67]\n"
        "s_waitcnt lgkmcnt(0)\n"
        "s_sub_u32 s42, s66, s58\n"
        "s_cmp_ge_i32 s42, 0\n"
        "s_cbranch_scc1 .Lk49\n"
        "s_sub_u32 s60, s60, 1\n"
        "s_cbranch_scc0 .Lk48\n"
        ".Lk49:\n"
        "buffer_load_dwordx4 v[72:75], v86, s[12:15], s46 offen sc1\n"
        "buffer_load_dwordx4 v[76:79], v86, s[12:15], s47 offen sc1\n"
        "s_waitcnt lgkmcnt(0)\n"
        "s_barrier\n"
        "global_load_dwordx2 v[22:23], v31, s[16:17]\n"
        "global_load_dwordx2 v[24:25], v31, s[18:19]\n"
        "v_mul_f32_e32 v64, 0x35cccccd, v56\n"
        "v_mul_f32_e32 v68, 0x35cccccd, v60\n"
        "v_mul_f32_e32 v65, 0x35cccccd, v57\n"
        "v_mul_f32_e32 v69, 0x35cccccd, v61\n"
        "v_mul_f32_e32 v66, 0x35cccccd, v58\n"
        "v_mul_f32_e32 v70, 0x35cccccd, v62\n"
        "v_mul_f32_e32 v67, 0x35cccccd, v59\n"
        "v_mul_f32_e32 v71, 0x35cccccd, v63\n"
        "s_mov_b32 s42, 0x5040100\n"
        "s_mov_b32 s43, 0x7060302\n"
        "v_add_u32_e32 v30, 0xffff9000, v85\n"
        "s_waitcnt vmcnt(4)\n"
        "v_perm_b32 v94, v20, v18, s42\n"
        "v_perm_b32 v95, v20, v18, s43\n"
        "ds_write2st64_b32 v30, v94, v95 offset0:0 offset1:4\n"
        "v_perm_b32 v96, v21, v19, s42\n"
        "v_perm_b32 v97, v21, v19, s43\n"
        "ds_write2st64_b32 v30, v96, v97 offset0:8 offset1:12\n"
        "v_and_b32_e32 v43, 7, v1\n"
        "v_mul_u32_u24_e32 v83, 0x590, v43\n"
        "v_and_b32_e32 v43, 8, v1\n"
        "v_and_b32_e32 v99, 7, v1\n"
        "v_lshl_add_u32 v83, v43, 3, v83\n"
        "v_lshrrev_b32_e32 v100, 5, v1\n"
        "v_cvt_f32_u32_e32 v99, v99\n"
        "v_lshl_add_u32 v83, v5, 4, v83\n"
        "s_lshl_b32 s42, s33, 7\n"
        "s_add_u32 s42, s42, 0x23010\n"
        "v_cvt_f32_u32_e32 v100, v100\n"
        "v_cvt_f32_u32_e32 v101, s36\n"
        "v_mul_f32_e32 v99, 0x3e000000, v99\n"
        "v_add_u32_e32 v83, s42, v83\n"
        "v_mul_f32_e32 v100, 4.0, v100\n"
        "v_fmamk_f32 v99, v101, 0x3a000000, v99\n"
        "s_mul_i32 s42, s33, 0x590\n"
        "s_add_u32 s42, s42, 0x2300c\n"
        "v_mul_f32_e32 v100, v100, v99\n"
        "v_add_u32_e32 v84, s42, v40\n"
        "s_add_u32 s43, s42, 0x410\n"
        "v_fmamk_f32 v105, v99, 1.0, v100\n"
        "v_fmamk_f32 v108, v99, 2.0, v100\n"
        "v_fmamk_f32 v111, v99, 0x40400000, v100\n"
        "v_sub_u32_e32 v41, s43, v40\n"
        "s_mul_i32 s42, s33, 0x590\n"
        "s_add_u32 s42, s42, 0x23000\n"
        "v_lshrrev_b32_e32 v98, 3, v1\n"
        "v_cos_f32_e32 v103, v100\n"
        "v_sin_f32_e32 v104, v100\n"
        "v_cos_f32_e32 v106, v105\n"
        "v_sin_f32_e32 v107, v105\n"
        "v_cos_f32_e32 v109, v108\n"
        "v_sin_f32_e32 v110, v108\n"
        "v_cos_f32_e32 v112, v111\n"
        "v_sin_f32_e32 v113, v111\n"
        "v_add_u32_e32 v42, s42, v2\n"
        "v_and_b32_e32 v43, 7, v1\n"
        "s_lshl_b32 s43, s33, 7\n"
        "s_add_u32 s43, s43, 0x23000\n"
        "v_xor_b32_e32 v98, v98, v5\n"
        "v_mul_u32_u24_e32 v46, 0x590, v43\n"
        "v_lshlrev_b32_e32 v47, 4, v43\n"
        "s_lshr_b32 s42, s36, 1\n"
        "s_mul_i32 s49, s42, 0x5500\n"
        "v_and_b32_e32 v98, 1, v98\n"
        "v_lshrrev_b32_e32 v43, 3, v1\n"
        "s_add_u32 s49, s49, 0x560000\n"
        "s_and_b32 s42, s36, 1\n"
        "v_add_u32_e32 v98, -1, v98\n"
        "v_cvt_pk_bf16_f32 v216, v103, v104\n"
        "v_cvt_pk_bf16_f32 v220, -v104, v103\n"
        "v_cvt_pk_bf16_f32 v217, v106, v107\n"
        "v_cvt_pk_bf16_f32 v221, -v107, v106\n"
        "v_cvt_pk_bf16_f32 v218, v109, v110\n"
        "v_cvt_pk_bf16_f32 v222, -v110, v109\n"
        "v_cvt_pk_bf16_f32 v219, v112, v113\n"
        "v_cvt_pk_bf16_f32 v223, -v113, v112\n"
        "v_lshl_add_u32 v46, v43, 4, v46\n"
        "v_lshl_add_u32 v47, v43, 8, v47\n"
        "s_lshl4_add_u32 s42, s33, s42\n"
        "s_lshl_b32 s42, s42, 7\n"
        "v_and_b32_e32 v216, v98, v216\n"
        "v_and_b32_e32 v220, v98, v220\n"
        "v_and_b32_e32 v217, v98, v217\n"
        "v_and_b32_e32 v221, v98, v221\n"
        "v_and_b32_e32 v218, v98, v218\n"
        "v_and_b32_e32 v222, v98, v222\n"
        "v_and_b32_e32 v219, v98, v219\n"
        "v_and_b32_e32 v223, v98, v223\n"
        "v_add_u32_e32 v46, s43, v46\n"
        "s_add_u32 s49, s49, s42\n"
        "v_mov_b32_e32 v43, 0\n"
        "s_add_u32 s59, s49, 0x4000\n"
        "s_waitcnt vmcnt(3)\n"
        "v_max3_u32 v6, v72, v73, v74\n"
        "s_waitcnt vmcnt(2)\n"
        "v_max3_u32 v6, v6, v75, v76\n"
        "v_max3_u32 v6, v6, v77, v78\n"
        "v_max_u32_e32 v6, v6, v79\n"
        "v_cmp_eq_u32_e32 vcc, -1, v6\n"
        "s_cbranch_vccnz .Lk50\n"
        ".Lk51:\n"
        "ds_write2st64_b32 v85, v72, v73 offset0:0 offset1:4\n"
        "ds_write2st64_b32 v85, v74, v75 offset0:8 offset1:12\n"
        "ds_write2st64_b32 v91, v76, v77 offset0:0 offset1:4\n"
        "ds_write2st64_b32 v91, v78, v79 offset0:8 offset1:12\n"
        "s_waitcnt lgkmcnt(0)\n"
        "s_barrier\n"
        "s_cmp_eq_u32 s33, 6\n"
        "s_cbranch_scc0 .Lk52\n"
        "s_memrealtime s[66:67]\n"
        "v_mov_b32_e32 v140, s39\n"
        "s_mul_i32 s42, s34, 20\n"
        "s_add_u32 s42, s42, s35\n"
        "s_lshl4_add_u32 s42, s42, 0x58000\n"
        "s_waitcnt lgkmcnt(0)\n"
        "v_mov_b32_e32 v141, s66\n"
        "v_lshlrev_b32_e32 v6, 12, v1\n"
        "s_mov_b64 exec, 0xff\n"
        "buffer_store_dwordx4 v[140:143], v6, s[12:15], s42 offen sc1\n"
        "s_mov_b64 exec, -1\n"
        ".Lk52:\n"
        "s_or_b32 s42, s2, s33\n"
        "s_cmp_eq_u32 s42, 0\n"
        "s_cbranch_scc0 .Lk53\n"
        "v_mov_b32_e32 v114, s39\n"
        "s_mov_b64 exec, 1\n"
        "buffer_store_dword v114, off, s[12:15], 0 sc1\n"
        "s_mov_b64 exec, -1\n"
        ".Lk53:\n"
        "s_add_u32 m0, s51, 0\n"
        "ds_read_b128 v[72:75], v88 offset:28672\n"
        "ds_read_b128 v[76:79], v88 offset:29696\n"
        "buffer_load_dwordx4 v32, s[24:27], s50 offen lds\n"
        "buffer_load_dwordx4 v33, s[24:27], s50 offen offset:1024 lds\n"
        "s_waitcnt lgkmcnt(1)\n"
        "v_mfma_f32_16x16x32_bf16 v[116:119], v[72:75], v[48:51], 0\n"
        "v_mfma_f32_16x16x32_bf16 v[14:17], v[72:75], v[52:55], 0\n"
        "s_waitcnt lgkmcnt(0)\n"
        "v_mfma_f32_16x16x32_bf16 v[120:123], v[76:79], v[48:51], 0\n"
        "s_nop 2\n"
        "v_mul_f32_e32 v115, v116, v56\n"
        "v_mul_f32_e32 v90, v117, v57\n"
        "v_mfma_f32_16x16x32_bf16 v[26:29], v[76:79], v[52:55], 0\n"
        "v_mul_f32_e32 v89, v14, v56\n"
        "v_mul_f32_e32 v92, v15, v57\n"
        "v_mul_f32_e32 v93, v118, v58\n"
        "v_mul_f32_e32 v94, v16, v58\n"
        "v_mul_f32_e32 v95, v119, v59\n"
        "buffer_load_dwordx4 v34, s[24:27], s50 offen offset:2048 lds\n"
        "v_mul_f32_e32 v96, v17, v59\n"
        "v_fmac_f32_e32 v115, v14, v60\n"
        "v_fma_f32 v89, -v116, v60, v89\n"
        "v_fmac_f32_e32 v90, v15, v61\n"
        "v_fma_f32 v92, -v117, v61, v92\n"
        "v_fmac_f32_e32 v93, v16, v62\n"
        "v_fma_f32 v94, -v118, v62, v94\n"
        "v_fmac_f32_e32 v95, v17, v63\n"
        "v_fma_f32 v96, -v119, v63, v96\n"
        "v_cvt_pk_bf16_f32 v72, v115, v89\n"
        "v_cvt_pk_bf16_f32 v73, v90, v92\n"
        "v_cvt_pk_bf16_f32 v74, v93, v94\n"
        "v_cvt_pk_bf16_f32 v75, v95, v96\n"
        "v_mul_f32_e32 v115, v120, v56\n"
        "v_mul_f32_e32 v89, v26, v56\n"
        "v_mul_f32_e32 v90, v121, v57\n"
        "v_mul_f32_e32 v92, v27, v57\n"
        "v_mul_f32_e32 v93, v122, v58\n"
        "v_mul_f32_e32 v94, v28, v58\n"
        "v_mul_f32_e32 v95, v123, v59\n"
        "v_mul_f32_e32 v96, v29, v59\n"
        "v_fmac_f32_e32 v115, v26, v60\n"
        "buffer_load_dwordx4 v35, s[24:27], s50 offen offset:3072 lds\n"
        "v_fma_f32 v89, -v120, v60, v89\n"
        "v_fmac_f32_e32 v90, v27, v61\n"
        "v_fma_f32 v92, -v121, v61, v92\n"
        "v_fmac_f32_e32 v93, v28, v62\n"
        "v_fma_f32 v94, -v122, v62, v94\n"
        "v_fmac_f32_e32 v95, v29, v63\n"
        "v_fma_f32 v96, -v123, v63, v96\n"
        "v_cvt_pk_bf16_f32 v76, v115, v89\n"
        "v_cvt_pk_bf16_f32 v77, v90, v92\n"
        "v_cvt_pk_bf16_f32 v78, v93, v94\n"
        "v_cvt_pk_bf16_f32 v79, v95, v96\n"
        "s_mov_b32 s42, 0x5040100\n"
        "s_mov_b32 s43, 0x7060302\n"
        "v_add_u32_e32 v31, 0xffff9040, v85\n"
        "s_waitcnt vmcnt(4)\n"
        "v_perm_b32 v97, v24, v22, s42\n"
        "v_perm_b32 v102, v24, v22, s43\n"
        "ds_write2st64_b32 v31, v97, v102 offset0:0 offset1:4\n"
        "v_perm_b32 v103, v25, v23, s42\n"
        "v_perm_b32 v104, v25, v23, s43\n"
        "ds_write2st64_b32 v31, v103, v104 offset0:8 offset1:12\n"
        "s_waitcnt lgkmcnt(0)\n"
        "s_barrier\n"
        "s_add_u32 m0, s51, 0x1000\n"
        "ds_read_b128 v[132:135], v88\n"
        "ds_read_b128 v[136:139], v88 offset:1024\n"
        "v_mfma_f32_16x16x32_bf16 v[116:119], v[48:51], v[72:75], 0\n"
        "buffer_load_dwordx4 v36, s[24:27], s50 offen lds\n"
        "v_mfma_f32_16x16x32_bf16 v[14:17], v[52:55], v[72:75], 0\n"
        "v_mfma_f32_16x16x32_bf16 v[120:123], v[48:51], v[76:79], 0\n"
        "v_mfma_f32_16x16x32_bf16 v[26:29], v[52:55], v[76:79], 0\n"
        "buffer_load_dwordx4 v37, s[24:27], s50 offen offset:1024 lds\n"
        "s_waitcnt lgkmcnt(1)\n"
        "v_lshlrev_b32_e32 v124, 16, v132\n"
        "v_lshlrev_b32_e32 v125, 16, v133\n"
        "v_lshlrev_b32_e32 v126, 16, v134\n"
        "v_lshlrev_b32_e32 v127, 16, v135\n"
        "v_and_b32_e32 v132, 0xffff0000, v132\n"
        "v_and_b32_e32 v133, 0xffff0000, v133\n"
        "v_and_b32_e32 v134, 0xffff0000, v134\n"
        "v_and_b32_e32 v135, 0xffff0000, v135\n"
        "v_mul_f32_e32 v115, v116, v124\n"
        "v_mul_f32_e32 v89, v14, v124\n"
        "v_mul_f32_e32 v90, v117, v125\n"
        "v_mul_f32_e32 v92, v15, v125\n"
        "v_mul_f32_e32 v93, v118, v126\n"
        "v_mul_f32_e32 v94, v16, v126\n"
        "v_mul_f32_e32 v95, v119, v127\n"
        "v_mul_f32_e32 v96, v17, v127\n"
        "v_fma_f32 v115, -v14, v132, v115\n"
        "v_fmac_f32_e32 v89, v116, v132\n"
        "v_fma_f32 v90, -v15, v133, v90\n"
        "v_fmac_f32_e32 v92, v117, v133\n"
        "v_fma_f32 v93, -v16, v134, v93\n"
        "v_fmac_f32_e32 v94, v118, v134\n"
        "buffer_load_dwordx4 v38, s[24:27], s50 offen offset:2048 lds\n"
        "s_add_u32 s50, s50, 0xd8000\n"
        "s_add_u32 s51, s51, 0xe000\n"
        "s_add_u32 m0, s51, 0\n"
        "v_fma_f32 v95, -v17, v135, v95\n"
        "v_fmac_f32_e32 v96, v119, v135\n"
        "s_waitcnt lgkmcnt(0)\n"
        "v_lshlrev_b32_e32 v128, 16, v136\n"
        "v_lshlrev_b32_e32 v129, 16, v137\n"
        "v_lshlrev_b32_e32 v130, 16, v138\n"
        "v_lshlrev_b32_e32 v131, 16, v139\n"
        "v_cvt_pk_bf16_f32 v72, v89, v115\n"
        "v_cvt_pk_bf16_f32 v73, v92, v90\n"
        "v_cvt_pk_bf16_f32 v74, v94, v93\n"
        "v_cvt_pk_bf16_f32 v75, v96, v95\n"
        "v_and_b32_e32 v136, 0xffff0000, v136\n"
        "v_and_b32_e32 v137, 0xffff0000, v137\n"
        "v_and_b32_e32 v138, 0xffff0000, v138\n"
        "v_and_b32_e32 v139, 0xffff0000, v139\n"
        "v_mul_f32_e32 v115, v120, v128\n"
        "v_mul_f32_e32 v89, v26, v128\n"
        "v_mul_f32_e32 v90, v121, v129\n"
        "v_mul_f32_e32 v92, v27, v129\n"
        "v_mul_f32_e32 v93, v122, v130\n"
        "v_mul_f32_e32 v94, v28, v130\n"
        "buffer_load_dwordx4 v32, s[24:27], s50 offen lds\n"
        "v_mul_f32_e32 v95, v123, v131\n"
        "v_mul_f32_e32 v96, v29, v131\n"
        "v_fma_f32 v115, -v26, v136, v115\n"
        "v_fmac_f32_e32 v89, v120, v136\n"
        "v_fma_f32 v90, -v27, v137, v90\n"
        "v_fmac_f32_e32 v92, v121, v137\n"
        "v_fma_f32 v93, -v28, v138, v93\n"
        "v_fmac_f32_e32 v94, v122, v138\n"
        "v_fma_f32 v95, -v29, v139, v95\n"
        "v_fmac_f32_e32 v96, v123, v139\n"
        "v_cvt_pk_bf16_f32 v76, v89, v115\n"
        "v_cvt_pk_bf16_f32 v77, v92, v90\n"
        "v_cvt_pk_bf16_f32 v78, v94, v93\n"
        "v_cvt_pk_bf16_f32 v79, v96, v95\n"
        "v_mfma_f32_16x16x32_bf16 v[116:119], v[72:75], v[48:51], 0\n"
        "v_mfma_f32_16x16x32_bf16 v[14:17], v[72:75], v[52:55], 0\n"
        "v_mfma_f32_16x16x32_bf16 v[120:123], v[76:79], v[48:51], 0\n"
        "s_nop 3\n"
        "v_mul_f32_e32 v115, v116, v64\n"
        "v_mul_f32_e32 v90, v117, v65\n"
        "v_mfma_f32_16x16x32_bf16 v[26:29], v[76:79], v[52:55], 0\n"
        "v_mul_f32_e32 v89, v14, v64\n"
        "v_mul_f32_e32 v92, v15, v65\n"
        "v_mul_f32_e32 v93, v118, v66\n"
        "v_mul_f32_e32 v94, v16, v66\n"
        "v_mul_f32_e32 v95, v119, v67\n"
        "v_mul_f32_e32 v96, v17, v67\n"
        "v_fmac_f32_e32 v115, v14, v68\n"
        "v_fma_f32 v89, -v116, v68, v89\n"
        "v_fmac_f32_e32 v90, v15, v69\n"
        "v_fma_f32 v92, -v117, v69, v92\n"
        "v_fmac_f32_e32 v93, v16, v70\n"
        "v_fma_f32 v94, -v118, v70, v94\n"
        "v_fmac_f32_e32 v95, v17, v71\n"
        "v_fma_f32 v96, -v119, v71, v96\n"
        "v_cvt_pk_bf16_f32 v72, v115, v89\n"
        "v_cvt_pk_bf16_f32 v73, v90, v92\n"
        "v_cvt_pk_bf16_f32 v74, v93, v94\n"
        "v_cvt_pk_bf16_f32 v75, v95, v96\n"
        "v_mul_f32_e32 v115, v120, v64\n"
        "v_mul_f32_e32 v89, v26, v64\n"
        "v_mul_f32_e32 v90, v121, v65\n"
        "v_mul_f32_e32 v92, v27, v65\n"
        "v_mul_f32_e32 v93, v122, v66\n"
        "v_mul_f32_e32 v94, v28, v66\n"
        "v_mul_f32_e32 v95, v123, v67\n"
        "v_mul_f32_e32 v96, v29, v67\n"
        "v_fmac_f32_e32 v115, v26, v68\n"
        "v_fma_f32 v89, -v120, v68, v89\n"
        "v_fmac_f32_e32 v90, v27, v69\n"
        "v_fma_f32 v92, -v121, v69, v92\n"
        "v_fmac_f32_e32 v93, v28, v70\n"
        "v_fma_f32 v94, -v122, v70, v94\n"
        "v_fmac_f32_e32 v95, v29, v71\n"
        "v_fma_f32 v96, -v123, v71, v96\n"
        "v_cvt_pk_bf16_f32 v76, v115, v89\n"
        "v_cvt_pk_bf16_f32 v77, v90, v92\n"
        "v_cvt_pk_bf16_f32 v78, v93, v94\n"
        "v_cvt_pk_bf16_f32 v79, v95, v96\n"
        "v_mfma_f32_16x16x32_bf16 v[116:119], v[48:51], v[72:75], 0\n"
        "v_mfma_f32_16x16x32_bf16 v[14:17], v[52:55], v[72:75], 0\n"
        "v_mfma_f32_16x16x32_bf16 v[120:123], v[48:51], v[76:79], 0\n"
        "v_mfma_f32_16x16x32_bf16 v[26:29], v[52:55], v[76:79], 0\n"
        "s_nop 3\n"
        "v_cvt_pk_bf16_f32 v72, v116, v14\n"
        "v_cvt_pk_bf16_f32 v73, v117, v15\n"
        "v_cvt_pk_bf16_f32 v74, v118, v16\n"
        "v_cvt_pk_bf16_f32 v75, v119, v17\n"
        "ds_write_b128 v88, v[72:75] offset:28672\n"
        "v_cvt_pk_bf16_f32 v76, v120, v26\n"
        "v_cvt_pk_bf16_f32 v77, v121, v27\n"
        "v_cvt_pk_bf16_f32 v78, v122, v28\n"
        "v_cvt_pk_bf16_f32 v79, v123, v29\n"
        "ds_write_b128 v88, v[76:79] offset:29696\n"
        "s_waitcnt lgkmcnt(0)\n"
        "s_barrier\n"
        "ds_read2st64_b32 v[116:117], v85 offset0:0 offset1:4\n"
        "ds_read2st64_b32 v[118:119], v85 offset0:8 offset1:12\n"
        "ds_read2st64_b32 v[120:121], v91 offset0:0 offset1:4\n"
        "ds_read2st64_b32 v[122:123], v91 offset0:8 offset1:12\n"
        "s_cmp_eq_u32 s33, 3\n"
        "s_cbranch_scc0 .Lk54\n"
        "s_lshl_b32 s48, s34, 12\n"
        "s_add_u32 s48, s48, 0x58000\n"
        "v_min_u32_e32 v82, 0x350, v3\n"
        "buffer_load_dwordx2 v[18:19], v82, s[12:15], s48 offen sc1\n"
        "buffer_load_dwordx2 v[20:21], v82, s[12:15], s48 offen offset:848 sc1\n"
        "buffer_load_dwordx2 v[22:23], v82, s[12:15], s48 offen offset:1696 sc1\n"
        ".Lk54:\n"
        "s_waitcnt lgkmcnt(2)\n"
        "buffer_store_dwordx4 v[116:119], v87, s[12:15], s56 offen sc1\n"
        "s_waitcnt lgkmcnt(0)\n"
        "buffer_store_dwordx4 v[120:123], v87, s[12:15], s56 offen offset:256 sc1\n"
        "s_barrier\n"
        "buffer_load_dwordx4 v33, s[24:27], s50 offen offset:1024 lds\n"
        "buffer_load_dwordx4 v34, s[24:27], s50 offen offset:2048 lds\n"
        "buffer_load_dwordx4 v35, s[24:27], s50 offen offset:3072 lds\n"
        "s_add_u32 m0, s51, 0x1000\n"
        "s_nop 0\n"
        "buffer_load_dwordx4 v36, s[24:27], s50 offen lds\n"
        "buffer_load_dwordx4 v37, s[24:27], s50 offen offset:1024 lds\n"
        "buffer_load_dwordx4 v38, s[24:27], s50 offen offset:2048 lds\n"
        "s_waitcnt vmcnt(9)\n"
        "s_mul_i32 s42, s33, 0x1c00\n"
        "v_add_u32_e32 v105, s42, v39\n"
        "v_add_u32_e32 v106, s42, v3\n"
        "ds_read_b64_tr_b16 v[140:141], v105\n"
        "ds_read_b64_tr_b16 v[142:143], v105 offset:896\n"
        "ds_read_b64_tr_b16 v[144:145], v105 offset:32\n"
        "ds_read_b64_tr_b16 v[146:147], v105 offset:928\n"
        "ds_read_b64_tr_b16 v[148:149], v105 offset:64\n"
        "ds_read_b64_tr_b16 v[150:151], v105 offset:960\n"
        "ds_read_b64_tr_b16 v[152:153], v105 offset:96\n"
        "ds_read_b64_tr_b16 v[154:155], v105 offset:992\n"
        "ds_read_b64_tr_b16 v[156:157], v105 offset:128\n"
        "ds_read_b64_tr_b16 v[158:159], v105 offset:1024\n"
        "ds_read_b64_tr_b16 v[160:161], v105 offset:160\n"
        "ds_read_b64_tr_b16 v[162:163], v105 offset:1056\n"
        "ds_read_b64_tr_b16 v[164:165], v105 offset:192\n"
        "ds_read_b64_tr_b16 v[166:167], v105 offset:1088\n"
        "s_waitcnt lgkmcnt(12)\n"
        "ds_write_b128 v106, v[140:143]\n"
        "s_waitcnt lgkmcnt(11)\n"
        "ds_write_b128 v106, v[144:147] offset:1024\n"
        "s_waitcnt lgkmcnt(10)\n"
        "ds_write_b128 v106, v[148:151] offset:2048\n"
        "s_waitcnt lgkmcnt(9)\n"
        "ds_write_b128 v106, v[152:155] offset:3072\n"
        "s_waitcnt lgkmcnt(8)\n"
        "ds_write_b128 v106, v[156:159] offset:4096\n"
        "s_waitcnt lgkmcnt(7)\n"
        "ds_write_b128 v106, v[160:163] offset:5120\n"
        "s_waitcnt lgkmcnt(6)\n"
        "ds_write_b128 v106, v[164:167] offset:6144\n"
        "s_cmp_eq_u32 s33, 3\n"
        "s_cbranch_scc0 .Lk55\n"
        "v_xor_b32_e32 v6, s39, v18\n"
        "v_bitop3_b32 v6, s39, v20, v6 bitop3:0xbe\n"
        "s_waitcnt vmcnt(8)\n"
        "v_bitop3_b32 v6, s39, v22, v6 bitop3:0xbe\n"
        "v_cmp_ne_u32_e32 vcc, 0, v6\n"
        "s_cbranch_vccnz .Lk56\n"
        ".Lk57:\n"
        "s_memrealtime s[66:67]\n"
        "s_waitcnt lgkmcnt(0)\n"
        "v_subrev_u32_e32 v19, s66, v19\n"
        "v_subrev_u32_e32 v21, s66, v21\n"
        "v_subrev_u32_e32 v23, s66, v23\n"
        "v_max3_i32 v6, v19, v21, v23\n"
        "s_nop 1\n"
        "v_max_i32_dpp v6, v6, v6 quad_perm:[1,0,3,2] row_mask:0xf bank_mask:0xf\n"
        "s_nop 1\n"
        "v_max_i32_dpp v6, v6, v6 quad_perm:[2,3,0,1] row_mask:0xf bank_mask:0xf\n"
        "s_nop 1\n"
        "v_max_i32_dpp v6, v6, v6 row_ror:4 row_mask:0xf bank_mask:0xf\n"
        "s_nop 1\n"
        "v_max_i32_dpp v6, v6, v6 row_ror:8 row_mask:0xf bank_mask:0xf\n"
        "s_nop 1\n"
        "v_max_i32_dpp v6, v6, v6 row_bcast:15 row_mask:0xa bank_mask:0xf\n"
        "s_nop 1\n"
        "v_max_i32_dpp v6, v6, v6 row_bcast:31 row_mask:0xc bank_mask:0xf\n"
        "s_nop 0\n"
        "v_readlane_b32 s57, v6, 63\n"
        "s_nop 0\n"
        "s_add_u32 s57, s57, s66\n"
        "s_add_u32 s57, s57, 0x82\n"
        "s_movk_i32 s60, 0x64\n"
        ".Lk58:\n"
        "s_memrealtime s[66:67]\n"
        "s_waitcnt lgkmcnt(0)\n"
        "s_sub_u32 s42, s66, s57\n"
        "s_cmp_ge_i32 s42, 0\n"
        "s_cbranch_scc1 .Lk59\n"
        "s_sub_u32 s60, s60, 1\n"
        "s_cbranch_scc0 .Lk58\n"
        ".Lk59:\n"
        ".Lk55:\n"
        "s_barrier\n"
        "buffer_load_dword v72, v12, s[12:15], s63 offen sc1\n"
        "buffer_load_dword v73, v12, s[12:15], s3 offen sc1\n"
        "buffer_load_dword v74, v12, s[12:15], s44 offen sc1\n"
        "buffer_load_dword v75, v12, s[12:15], s45 offen sc1\n"
        "s_cmp_lt_u32 s33, 2\n"
        "s_cbranch_scc0 .Lk60\n"
        "buffer_load_dword v76, v45, s[12:15], s63 offen sc1\n"
        "buffer_load_dword v77, v45, s[12:15], s3 offen sc1\n"
        "buffer_load_dword v78, v45, s[12:15], s44 offen sc1\n"
        "buffer_load_dword v79, v45, s[12:15], s45 offen sc1\n"
        ".Lk60:\n"
        "s_waitcnt lgkmcnt(0)\n"
        "s_barrier\n"
        "s_add_u32 s62, s33, 1\n"
        "s_lshr_b32 s62, s62, 1\n"
        "s_sub_u32 s62, 8, s62\n"
        "s_mul_i32 s42, s62, 0x1b000\n"
        "s_add_u32 s50, s50, s42\n"
        "s_mul_i32 s42, s62, 0x1c00\n"
        "s_add_u32 s51, s51, s42\n"
        "s_and_b32 s42, s33, 1\n"
        "s_mul_i32 s62, s42, 0xc00\n"
        "s_add_u32 s51, s51, s62\n"
        "s_sub_u32 s42, 0, s42\n"
        "v_add_u32_e32 v44, 0xc00, v35\n"
        "v_bfi_b32 v32, s42, v44, v32\n"
        "v_add_u32_e32 v44, 0xfffffc00, v36\n"
        "v_bfi_b32 v33, s42, v44, v33\n"
        "v_add_u32_e32 v44, 0xfffffc00, v37\n"
        "v_bfi_b32 v34, s42, v44, v34\n"
        "v_add_u32_e32 v44, 0xfffffc00, v38\n"
        "v_bfi_b32 v35, s42, v44, v35\n"
        "s_add_u32 m0, s51, 0\n"
        "s_nop 0\n"
        "buffer_load_dwordx4 v32, s[24:27], s50 offen lds\n"
        "buffer_load_dwordx4 v33, s[24:27], s50 offen offset:1024 lds\n"
        "buffer_load_dwordx4 v34, s[24:27], s50 offen offset:2048 lds\n"
        "s_branch .Lk61\n"
        ".Lk43:\n"
        "ds_read_b128 v[56:59], v41 offset:10240\n"
        "ds_read_b128 v[60:63], v41 offset:11264\n"
        "ds_read_b128 v[224:227], v41 offset:12288\n"
        "ds_read_b128 v[228:231], v41 offset:13312\n"
        "ds_read_b128 v[232:235], v41 offset:14336\n"
        "ds_read_b128 v[236:239], v41 offset:15360\n"
        "ds_read_b128 v[240:243], v41 offset:16384\n"
        "ds_read_b128 v[244:247], v41 offset:17408\n"
        "ds_read_b128 v[248:251], v41 offset:18432\n"
        "ds_read_b128 v[252:255], v41 offset:19456\n"
        "v_mul_u32_u24_e32 v107, 0x124a, v1\n"
        "v_lshrrev_b32_e32 v32, 16, v107\n"
        "v_add_u32_e32 v33, 0x49280, v107\n"
        "v_add_u32_e32 v34, 0x92500, v107\n"
        "v_add_u32_e32 v35, 0xdb780, v107\n"
        "v_add_u32_e32 v36, 0x124a00, v107\n"
        "v_add_u32_e32 v37, 0x16dc80, v107\n"
        "v_add_u32_e32 v38, 0x1b6f00, v107\n"
        "v_lshrrev_b32_e32 v107, 2, v1\n"
        "v_lshl_add_u32 v107, v5, 2, v107\n"
        "v_lshrrev_b32_e32 v36, 16, v36\n"
        "v_lshrrev_b32_e32 v37, 16, v37\n"
        "v_lshrrev_b32_e32 v38, 16, v38\n"
        "v_mul_u32_u24_e32 v39, 0xe0, v107\n"
        "s_movk_i32 s58, 0xca0\n"
        "v_lshrrev_b32_e32 v33, 16, v33\n"
        "v_lshrrev_b32_e32 v34, 16, v34\n"
        "v_lshrrev_b32_e32 v35, 16, v35\n"
        "v_mad_u32_u24 v36, v36, s58, v3\n"
        "v_mad_u32_u24 v37, v37, s58, v3\n"
        "v_mad_u32_u24 v38, v38, s58, v3\n"
        "v_and_b32_e32 v107, 3, v1\n"
        "v_mad_u32_u24 v32, v32, s58, v3\n"
        "v_mad_u32_u24 v33, v33, s58, v3\n"
        "v_mad_u32_u24 v34, v34, s58, v3\n"
        "v_mad_u32_u24 v35, v35, s58, v3\n"
        "v_add_u32_e32 v36, 0x1000, v36\n"
        "v_add_u32_e32 v37, 0x1000, v37\n"
        "v_add_u32_e32 v38, 0x1000, v38\n"
        "v_lshl_add_u32 v39, v107, 3, v39\n"
        "s_and_b32 s42, s2, 25\n"
        "s_lshl_b32 s67, s42, 5\n"
        "s_or_b32 s42, s42, s67\n"
        "s_bfe_u32 s42, s42, 0x30003\n"
        "s_lshl3_add_u32 s42, s42, s33\n"
        "s_mul_i32 s61, s42, 0x8c00\n"
        "s_add_u32 s61, s61, 0xf88000\n"
        "v_lshrrev_b32_e32 v108, 5, v1\n"
        "v_mul_u32_u24_e32 v12, 0x140000, v108\n"
        "v_and_b32_e32 v108, 16, v1\n"
        "s_lshl_b32 s63, s33, 9\n"
        "v_lshl_add_u32 v12, v108, 10, v12\n"
        "s_add_u32 s63, s63, s36\n"
        "s_lshl_b32 s63, s63, 6\n"
        "v_lshl_add_u32 v12, v4, 2, v12\n"
        "s_add_u32 s63, s63, 0x2e0000\n"
        "s_add_u32 s3, s63, 0x50000\n"
        "v_add_u32_e32 v45, 0x40000, v12\n"
        "s_add_u32 s44, s63, 0xa0000\n"
        "s_add_u32 s45, s63, 0xf0000\n"
        "s_waitcnt lgkmcnt(0)\n"
        "v_and_b32_e32 v43, 7, v1\n"
        "v_mul_u32_u24_e32 v83, 0x590, v43\n"
        "v_and_b32_e32 v43, 8, v1\n"
        "v_and_b32_e32 v110, 7, v1\n"
        "v_lshl_add_u32 v83, v43, 3, v83\n"
        "v_lshrrev_b32_e32 v111, 5, v1\n"
        "v_cvt_f32_u32_e32 v110, v110\n"
        "v_lshl_add_u32 v83, v5, 4, v83\n"
        "s_lshl_b32 s42, s33, 7\n"
        "s_add_u32 s42, s42, 0x23010\n"
        "v_cvt_f32_u32_e32 v111, v111\n"
        "v_cvt_f32_u32_e32 v112, s36\n"
        "v_mul_f32_e32 v110, 0x3e000000, v110\n"
        "v_add_u32_e32 v83, s42, v83\n"
        "v_mul_f32_e32 v111, 4.0, v111\n"
        "v_fmamk_f32 v110, v112, 0x3a000000, v110\n"
        "s_mul_i32 s42, s33, 0x590\n"
        "s_add_u32 s42, s42, 0x2300c\n"
        "v_mul_f32_e32 v111, v111, v110\n"
        "v_add_u32_e32 v84, s42, v40\n"
        "s_add_u32 s43, s42, 0x410\n"
        "v_fmamk_f32 v100, v110, 1.0, v111\n"
        "v_fmamk_f32 v97, v110, 2.0, v111\n"
        "v_fmamk_f32 v104, v110, 0x40400000, v111\n"
        "v_sub_u32_e32 v41, s43, v40\n"
        "s_mul_i32 s42, s33, 0x590\n"
        "s_add_u32 s42, s42, 0x23000\n"
        "v_lshrrev_b32_e32 v109, 3, v1\n"
        "v_cos_f32_e32 v98, v111\n"
        "v_sin_f32_e32 v99, v111\n"
        "v_cos_f32_e32 v101, v100\n"
        "v_sin_f32_e32 v114, v100\n"
        "v_cos_f32_e32 v102, v97\n"
        "v_sin_f32_e32 v103, v97\n"
        "v_cos_f32_e32 v115, v104\n"
        "v_sin_f32_e32 v89, v104\n"
        "v_add_u32_e32 v42, s42, v2\n"
        "v_and_b32_e32 v43, 7, v1\n"
        "s_lshl_b32 s43, s33, 7\n"
        "s_add_u32 s43, s43, 0x23000\n"
        "v_xor_b32_e32 v109, v109, v5\n"
        "v_mul_u32_u24_e32 v46, 0x590, v43\n"
        "v_lshlrev_b32_e32 v47, 4, v43\n"
        "s_lshr_b32 s42, s36, 1\n"
        "s_mul_i32 s49, s42, 0x5500\n"
        "v_and_b32_e32 v109, 1, v109\n"
        "v_lshrrev_b32_e32 v43, 3, v1\n"
        "s_add_u32 s49, s49, 0x560000\n"
        "s_and_b32 s42, s36, 1\n"
        "v_add_u32_e32 v109, -1, v109\n"
        "v_cvt_pk_bf16_f32 v216, v98, v99\n"
        "v_cvt_pk_bf16_f32 v220, -v99, v98\n"
        "v_cvt_pk_bf16_f32 v217, v101, v114\n"
        "v_cvt_pk_bf16_f32 v221, -v114, v101\n"
        "v_cvt_pk_bf16_f32 v218, v102, v103\n"
        "v_cvt_pk_bf16_f32 v222, -v103, v102\n"
        "v_cvt_pk_bf16_f32 v219, v115, v89\n"
        "v_cvt_pk_bf16_f32 v223, -v89, v115\n"
        "v_lshl_add_u32 v46, v43, 4, v46\n"
        "v_lshl_add_u32 v47, v43, 8, v47\n"
        "s_lshl4_add_u32 s42, s33, s42\n"
        "s_lshl_b32 s42, s42, 7\n"
        "v_and_b32_e32 v216, v109, v216\n"
        "v_and_b32_e32 v220, v109, v220\n"
        "v_and_b32_e32 v217, v109, v217\n"
        "v_and_b32_e32 v221, v109, v221\n"
        "v_and_b32_e32 v218, v109, v218\n"
        "v_and_b32_e32 v222, v109, v222\n"
        "v_and_b32_e32 v219, v109, v219\n"
        "v_and_b32_e32 v223, v109, v223\n"
        "v_add_u32_e32 v46, s43, v46\n"
        "s_add_u32 s49, s49, s42\n"
        "v_mov_b32_e32 v43, 0\n"
        "s_add_u32 s59, s49, 0x4000\n"
        "s_sleep 24\n"
        "s_cmp_lt_u32 s33, 4\n"
        "s_cbranch_scc0 .Lk62\n"
        "s_waitcnt vmcnt(0)\n"
        "s_branch .Lk63\n"
        ".Lk62:\n"
        "s_cmp_lt_u32 s33, 6\n"
        "s_cbranch_scc0 .Lk64\n"
        "s_cmp_lt_u32 s33, 5\n"
        "s_cbranch_scc0 .Lk65\n"
        "s_waitcnt vmcnt(0)\n"
        "s_branch .Lk66\n"
        ".Lk65:\n"
        "s_waitcnt vmcnt(0)\n"
        ".Lk66:\n"
        "s_branch .Lk67\n"
        ".Lk64:\n"
        "s_waitcnt vmcnt(0)\n"
        ".Lk67:\n"
        ".Lk63:\n"
        "s_cmp_lt_u32 s33, 6\n"
        "s_cbranch_scc0 .Lk68\n"
        "v_lshrrev_b32_e32 v92, 2, v1\n"
        "v_lshlrev_b32_e32 v92, 7, v92\n"
        "s_lshl_b32 s42, s33, 12\n"
        "s_add_u32 s42, s42, 0x1c000\n"
        "v_add_u32_e32 v92, s42, v92\n"
        "s_lshl1_add_u32 s56, s55, 0\n"
        "s_mul_i32 s60, s56, 52\n"
        "s_lshr_b32 s60, s60, 8\n"
        "s_mul_i32 s42, s60, 5\n"
        "s_sub_u32 s56, s56, s42\n"
        "s_cmp_gt_u32 s56, 2\n"
        "s_cselect_b32 s42, 1, 0\n"
        "s_cselect_b32 s43, 3, 0\n"
        "s_lshl1_add_u32 s42, s60, s42\n"
        "s_lshl3_add_u32 s42, s42, s53\n"
        "s_mul_i32 s48, s42, 35\n"
        "s_sub_u32 s43, s56, s43\n"
        "s_lshl1_add_u32 s48, s43, s48\n"
        "s_mul_i32 s42, s54, 5\n"
        "s_add_u32 s48, s48, s42\n"
        "s_lshl_b32 s48, s48, 10\n"
        "s_add_u32 s48, s48, 0xf88000\n"
        "s_cmp_eq_u32 s56, 2\n"
        "s_cselect_b32 s62, 0x118, 1\n"
        "s_cselect_b32 s42, 3, 4\n"
        "s_cselect_b32 s43, 32, 8\n"
        "s_lshl_b32 s62, s62, 10\n"
        "v_and_b32_e32 v90, 3, v1\n"
        "v_lshlrev_b32_e32 v90, s42, v90\n"
        "v_add_u32_e32 v90, v90, v92\n"
        "v_add_u32_e32 v93, s43, v90\n"
        "ds_read_b64_tr_b16 v[94:95], v90\n"
        "ds_read_b64_tr_b16 v[96:97], v90 offset:2048\n"
        "ds_read_b64_tr_b16 v[86:87], v93\n"
        "ds_read_b64_tr_b16 v[88:89], v93 offset:2048\n"
        "s_waitcnt lgkmcnt(2)\n"
        "buffer_store_dwordx4 v[94:97], v3, s[12:15], s48 offen sc1\n"
        "s_add_u32 s48, s48, s62\n"
        "s_waitcnt lgkmcnt(0)\n"
        "buffer_store_dwordx4 v[86:89], v3, s[12:15], s48 offen sc1\n"
        "s_lshl1_add_u32 s56, s55, 1\n"
        "s_mul_i32 s60, s56, 52\n"
        "s_lshr_b32 s60, s60, 8\n"
        "s_mul_i32 s42, s60, 5\n"
        "s_sub_u32 s56, s56, s42\n"
        "s_cmp_gt_u32 s56, 2\n"
        "s_cselect_b32 s42, 1, 0\n"
        "s_cselect_b32 s43, 3, 0\n"
        "s_lshl1_add_u32 s42, s60, s42\n"
        "s_lshl3_add_u32 s42, s42, s53\n"
        "s_mul_i32 s48, s42, 35\n"
        "s_sub_u32 s43, s56, s43\n"
        "s_lshl1_add_u32 s48, s43, s48\n"
        "s_mul_i32 s42, s54, 5\n"
        "s_add_u32 s48, s48, s42\n"
        "s_lshl_b32 s48, s48, 10\n"
        "s_add_u32 s48, s48, 0xf88000\n"
        "s_cmp_eq_u32 s56, 2\n"
        "s_cselect_b32 s62, 0x118, 1\n"
        "s_cselect_b32 s42, 3, 4\n"
        "s_cselect_b32 s43, 32, 8\n"
        "s_lshl_b32 s62, s62, 10\n"
        "v_and_b32_e32 v90, 3, v1\n"
        "v_lshlrev_b32_e32 v90, s42, v90\n"
        "v_add_u32_e32 v90, v90, v92\n"
        "v_add_u32_e32 v85, s43, v90\n"
        "ds_read_b64_tr_b16 v[106:107], v90 offset:64\n"
        "ds_read_b64_tr_b16 v[108:109], v90 offset:2112\n"
        "ds_read_b64_tr_b16 v[98:99], v85 offset:64\n"
        "ds_read_b64_tr_b16 v[100:101], v85 offset:2112\n"
        "s_waitcnt lgkmcnt(2)\n"
        "buffer_store_dwordx4 v[106:109], v3, s[12:15], s48 offen sc1\n"
        "s_add_u32 s48, s48, s62\n"
        "s_waitcnt lgkmcnt(0)\n"
        "buffer_store_dwordx4 v[98:101], v3, s[12:15], s48 offen sc1\n"
        ".Lk68:\n"
        "s_mul_i32 s47, s52, 64\n"
        "s_add_u32 s47, s47, 0x4000\n"
        "s_waitcnt lgkmcnt(0)\n"
        "s_barrier\n"
        "s_barrier\n"
        "s_add_u32 m0, s51, 0\n"
        "s_nop 0\n"
        "buffer_load_dwordx4 v32, s[24:27], s50 offen lds\n"
        "buffer_load_dwordx4 v33, s[24:27], s50 offen offset:1024 lds\n"
        "buffer_load_dwordx4 v34, s[24:27], s50 offen offset:2048 lds\n"
        "buffer_load_dwordx4 v35, s[24:27], s50 offen offset:3072 lds\n"
        "s_waitcnt vmcnt(4)\n"
        "s_barrier\n"
        "s_cmp_eq_u32 s33, 0\n"
        "s_cbranch_scc0 .Lk69\n"
        "v_mov_b32_e32 v91, s39\n"
        "s_mov_b64 exec, 0xffff\n"
        "buffer_store_dword v91, v2, s[12:15], s47 offen sc1\n"
        "s_mov_b64 exec, -1\n"
        ".Lk69:\n"
        "s_add_u32 m0, s51, 0x1000\n"
        "s_nop 0\n"
        "buffer_load_dwordx4 v36, s[24:27], s50 offen lds\n"
        "buffer_load_dwordx4 v37, s[24:27], s50 offen offset:1024 lds\n"
        "buffer_load_dwordx4 v38, s[24:27], s50 offen offset:2048 lds\n"
        "s_add_u32 s50, s50, 0xd8000\n"
        "s_add_u32 s51, s51, 0xe000\n"
        "s_add_u32 m0, s51, 0\n"
        "s_nop 0\n"
        "buffer_load_dwordx4 v32, s[24:27], s50 offen lds\n"
        "buffer_load_dwordx4 v33, s[24:27], s50 offen offset:1024 lds\n"
        "buffer_load_dwordx4 v34, s[24:27], s50 offen offset:2048 lds\n"
        "buffer_load_dwordx4 v35, s[24:27], s50 offen offset:3072 lds\n"
        "s_add_u32 m0, s51, 0x1000\n"
        "s_nop 0\n"
        "buffer_load_dwordx4 v36, s[24:27], s50 offen lds\n"
        "s_waitcnt vmcnt(5)\n"
        "s_mul_i32 s42, s33, 0x1c00\n"
        "v_add_u32_e32 v105, s42, v39\n"
        "v_add_u32_e32 v113, s42, v3\n"
        "ds_read_b64_tr_b16 v[140:141], v105\n"
        "ds_read_b64_tr_b16 v[142:143], v105 offset:896\n"
        "ds_read_b64_tr_b16 v[144:145], v105 offset:32\n"
        "ds_read_b64_tr_b16 v[146:147], v105 offset:928\n"
        "ds_read_b64_tr_b16 v[148:149], v105 offset:64\n"
        "ds_read_b64_tr_b16 v[150:151], v105 offset:960\n"
        "ds_read_b64_tr_b16 v[152:153], v105 offset:96\n"
        "ds_read_b64_tr_b16 v[154:155], v105 offset:992\n"
        "ds_read_b64_tr_b16 v[156:157], v105 offset:128\n"
        "ds_read_b64_tr_b16 v[158:159], v105 offset:1024\n"
        "ds_read_b64_tr_b16 v[160:161], v105 offset:160\n"
        "ds_read_b64_tr_b16 v[162:163], v105 offset:1056\n"
        "ds_read_b64_tr_b16 v[164:165], v105 offset:192\n"
        "ds_read_b64_tr_b16 v[166:167], v105 offset:1088\n"
        "s_waitcnt lgkmcnt(12)\n"
        "ds_write_b128 v113, v[140:143]\n"
        "s_waitcnt lgkmcnt(11)\n"
        "ds_write_b128 v113, v[144:147] offset:1024\n"
        "s_waitcnt lgkmcnt(10)\n"
        "ds_write_b128 v113, v[148:151] offset:2048\n"
        "s_waitcnt lgkmcnt(9)\n"
        "ds_write_b128 v113, v[152:155] offset:3072\n"
        "s_waitcnt lgkmcnt(8)\n"
        "ds_write_b128 v113, v[156:159] offset:4096\n"
        "s_waitcnt lgkmcnt(7)\n"
        "ds_write_b128 v113, v[160:163] offset:5120\n"
        "s_waitcnt lgkmcnt(6)\n"
        "ds_write_b128 v113, v[164:167] offset:6144\n"
        "s_cmp_eq_u32 s33, 3\n"
        "s_cbranch_scc0 .Lk70\n"
        "s_lshl_b32 s47, s34, 8\n"
        "s_add_u32 s47, s47, s52\n"
        "s_lshl4_add_u32 s47, s47, 0x58000\n"
        "buffer_load_dwordx2 v[18:19], off, s[12:15], s47 sc1\n"
        "s_waitcnt vmcnt(0)\n"
        "v_xor_b32_e32 v6, s39, v18\n"
        "v_cmp_ne_u32_e32 vcc, 0, v6\n"
        "s_cbranch_vccnz .Lk71\n"
        ".Lk72:\n"
        "v_readfirstlane_b32 s47, v19\n"
        "s_nop 0\n"
        "s_add_u32 s47, s47, 30\n"
        "s_movk_i32 s57, 0x64\n"
        ".Lk73:\n"
        "s_memrealtime s[66:67]\n"
        "s_waitcnt lgkmcnt(0)\n"
        "s_sub_u32 s42, s66, s47\n"
        "s_cmp_ge_i32 s42, 0\n"
        "s_cbranch_scc1 .Lk74\n"
        "s_sub_u32 s57, s57, 1\n"
        "s_cbranch_scc0 .Lk73\n"
        ".Lk74:\n"
        "s_lshl_b32 s48, s34, 12\n"
        "s_add_u32 s48, s48, 0x58000\n"
        "v_min_u32_e32 v82, 0x350, v3\n"
        "buffer_load_dwordx2 v[18:19], v82, s[12:15], s48 offen sc1\n"
        "buffer_load_dwordx2 v[20:21], v82, s[12:15], s48 offen offset:848 sc1\n"
        "buffer_load_dwordx2 v[22:23], v82, s[12:15], s48 offen offset:1696 sc1\n"
        "s_waitcnt vmcnt(2)\n"
        "v_xor_b32_e32 v6, s39, v18\n"
        "s_waitcnt vmcnt(1)\n"
        "v_bitop3_b32 v6, s39, v20, v6 bitop3:0xbe\n"
        "s_waitcnt vmcnt(0)\n"
        "v_bitop3_b32 v6, s39, v22, v6 bitop3:0xbe\n"
        "v_cmp_ne_u32_e32 vcc, 0, v6\n"
        "s_cbranch_vccnz .Lk75\n"
        ".Lk76:\n"
        "s_memrealtime s[66:67]\n"
        "s_waitcnt lgkmcnt(0)\n"
        "v_subrev_u32_e32 v19, s66, v19\n"
        "v_subrev_u32_e32 v21, s66, v21\n"
        "v_subrev_u32_e32 v23, s66, v23\n"
        "v_max3_i32 v6, v19, v21, v23\n"
        "s_nop 1\n"
        "v_max_i32_dpp v6, v6, v6 quad_perm:[1,0,3,2] row_mask:0xf bank_mask:0xf\n"
        "s_nop 1\n"
        "v_max_i32_dpp v6, v6, v6 quad_perm:[2,3,0,1] row_mask:0xf bank_mask:0xf\n"
        "s_nop 1\n"
        "v_max_i32_dpp v6, v6, v6 row_ror:4 row_mask:0xf bank_mask:0xf\n"
        "s_nop 1\n"
        "v_max_i32_dpp v6, v6, v6 row_ror:8 row_mask:0xf bank_mask:0xf\n"
        "s_nop 1\n"
        "v_max_i32_dpp v6, v6, v6 row_bcast:15 row_mask:0xa bank_mask:0xf\n"
        "s_nop 1\n"
        "v_max_i32_dpp v6, v6, v6 row_bcast:31 row_mask:0xc bank_mask:0xf\n"
        "s_nop 0\n"
        "v_readlane_b32 s47, v6, 63\n"
        "s_nop 0\n"
        "s_add_u32 s47, s47, s66\n"
        "s_add_u32 s47, s47, 0x82\n"
        "s_movk_i32 s58, 0x64\n"
        ".Lk77:\n"
        "s_memrealtime s[66:67]\n"
        "s_waitcnt lgkmcnt(0)\n"
        "s_sub_u32 s42, s66, s47\n"
        "s_cmp_ge_i32 s42, 0\n"
        "s_cbranch_scc1 .Lk78\n"
        "s_sub_u32 s58, s58, 1\n"
        "s_cbranch_scc0 .Lk77\n"
        ".Lk78:\n"
        ".Lk70:\n"
        "s_waitcnt lgkmcnt(0)\n"
        "s_barrier\n"
        "buffer_load_dword v72, v12, s[12:15], s63 offen sc1\n"
        "buffer_load_dword v73, v12, s[12:15], s3 offen sc1\n"
        "buffer_load_dword v74, v12, s[12:15], s44 offen sc1\n"
        "buffer_load_dword v75, v12, s[12:15], s45 offen sc1\n"
        "s_cmp_lt_u32 s33, 2\n"
        "s_cbranch_scc0 .Lk79\n"
        "buffer_load_dword v76, v45, s[12:15], s63 offen sc1\n"
        "buffer_load_dword v77, v45, s[12:15], s3 offen sc1\n"
        "buffer_load_dword v78, v45, s[12:15], s44 offen sc1\n"
        "buffer_load_dword v79, v45, s[12:15], s45 offen sc1\n"
        ".Lk79:\n"
        "s_barrier\n"
        "buffer_load_dwordx4 v37, s[24:27], s50 offen offset:1024 lds\n"
        "buffer_load_dwordx4 v38, s[24:27], s50 offen offset:2048 lds\n"
        "s_add_u32 s62, s33, 1\n"
        "s_lshr_b32 s62, s62, 1\n"
        "s_sub_u32 s62, 8, s62\n"
        "s_mul_i32 s42, s62, 0x1b000\n"
        "s_add_u32 s50, s50, s42\n"
        "s_mul_i32 s42, s62, 0x1c00\n"
        "s_add_u32 s51, s51, s42\n"
        "s_and_b32 s42, s33, 1\n"
        "s_mul_i32 s62, s42, 0xc00\n"
        "s_add_u32 s51, s51, s62\n"
        "s_sub_u32 s42, 0, s42\n"
        "v_add_u32_e32 v44, 0xc00, v35\n"
        "v_bfi_b32 v32, s42, v44, v32\n"
        "v_add_u32_e32 v44, 0xfffffc00, v36\n"
        "v_bfi_b32 v33, s42, v44, v33\n"
        "v_add_u32_e32 v44, 0xfffffc00, v37\n"
        "v_bfi_b32 v34, s42, v44, v34\n"
        "v_add_u32_e32 v44, 0xfffffc00, v38\n"
        "v_bfi_b32 v35, s42, v44, v35\n"
        "s_add_u32 m0, s51, 0\n"
        "s_nop 0\n"
        "buffer_load_dwordx4 v32, s[24:27], s50 offen lds\n"
        "buffer_load_dwordx4 v33, s[24:27], s50 offen offset:1024 lds\n"
        "buffer_load_dwordx4 v34, s[24:27], s50 offen offset:2048 lds\n"
        ".Lk61:\n"
        "v_lshlrev_b32_e32 v56, 16, v7\n"
        "v_and_b32_e32 v60, 0xffff0000, v7\n"
        "v_lshlrev_b32_e32 v57, 16, v8\n"
        "v_and_b32_e32 v61, 0xffff0000, v8\n"
        "v_lshlrev_b32_e32 v58, 16, v9\n"
        "v_and_b32_e32 v62, 0xffff0000, v9\n"
        "v_lshlrev_b32_e32 v59, 16, v10\n"
        "v_and_b32_e32 v63, 0xffff0000, v10\n"
        "v_lshlrev_b32_e32 v64, 16, v11\n"
        "v_and_b32_e32 v68, 0xffff0000, v11\n"
        "s_cmp_eq_u32 s33, 2\n"
        "s_cbranch_scc0 .Lk80\n"
        "v_min_u32_e32 v82, 47, v1\n"
        "v_mul_u32_u24_e32 v82, 0x80, v82\n"
        "s_movk_i32 s48, 0x4000\n"
        "buffer_load_dword v80, v82, s[12:15], s48 offen sc1\n"
        "buffer_load_dword v81, v82, s[12:15], s48 offen offset:64 sc1\n"
        ".Lk80:\n"
        "s_mov_b64 exec, 1\n"
        "ds_write_b32 v84, v43\n"
        "s_mov_b64 exec, -1\n"
        "s_cmp_lt_u32 s35, 20\n"
        "s_cbranch_scc0 .Lk81\n"
        "s_cmp_lt_u32 s33, 4\n"
        "s_cbranch_scc0 .Lk82\n"
        "s_cmp_eq_u32 s33, 2\n"
        "s_cbranch_scc0 .Lk83\n"
        "s_waitcnt vmcnt(5)\n"
        "s_branch .Lk84\n"
        ".Lk83:\n"
        "s_waitcnt vmcnt(3)\n"
        ".Lk84:\n"
        "s_branch .Lk85\n"
        ".Lk82:\n"
        "s_waitcnt vmcnt(3)\n"
        ".Lk85:\n"
        "s_branch .Lk86\n"
        ".Lk81:\n"
        "s_cmp_lt_u32 s33, 4\n"
        "s_cbranch_scc0 .Lk87\n"
        "s_cmp_eq_u32 s33, 2\n"
        "s_cbranch_scc0 .Lk88\n"
        "s_waitcnt vmcnt(7)\n"
        "s_branch .Lk89\n"
        ".Lk88:\n"
        "s_waitcnt vmcnt(5)\n"
        ".Lk89:\n"
        "s_branch .Lk90\n"
        ".Lk87:\n"
        "s_waitcnt vmcnt(5)\n"
        ".Lk90:\n"
        ".Lk86:\n"
        "v_max3_u32 v6, v72, v73, v74\n"
        "v_max_u32_e32 v6, v6, v75\n"
        "v_cmp_eq_u32_e32 vcc, -1, v6\n"
        "s_cbranch_vccnz .Lk91\n"
        ".Lk92:\n"
        "v_mfma_f32_16x16x32_bf16 v[14:17], v[72:75], v[216:219], 0\n"
        "v_mfma_f32_16x16x32_bf16 v[18:21], v[72:75], v[220:223], 0\n"
        "s_nop 7\n"
        "v_cvt_pk_bf16_f32 v14, v14, v18\n"
        "v_cvt_pk_bf16_f32 v15, v15, v19\n"
        "v_cvt_pk_bf16_f32 v16, v16, v20\n"
        "v_cvt_pk_bf16_f32 v17, v17, v21\n"
        "ds_write_b128 v83, v[14:17]\n"
        "s_cmp_lt_u32 s33, 2\n"
        "s_cbranch_scc0 .Lk93\n"
        "v_max3_u32 v6, v76, v77, v78\n"
        "v_max_u32_e32 v6, v6, v79\n"
        "v_cmp_eq_u32_e32 vcc, -1, v6\n"
        "s_cbranch_vccnz .Lk94\n"
        ".Lk95:\n"
        "v_mfma_f32_16x16x32_bf16 v[22:25], v[76:79], v[216:219], 0\n"
        "v_mfma_f32_16x16x32_bf16 v[26:29], v[76:79], v[220:223], 0\n"
        "s_nop 7\n"
        "v_cvt_pk_bf16_f32 v22, v22, v26\n"
        "v_cvt_pk_bf16_f32 v23, v23, v27\n"
        "v_cvt_pk_bf16_f32 v24, v24, v28\n"
        "v_cvt_pk_bf16_f32 v25, v25, v29\n"
        "ds_write_b128 v83, v[22:25] offset:1024\n"
        ".Lk93:\n"
        "s_cmp_eq_u32 s33, 2\n"
        "s_cbranch_scc0 .Lk96\n"
        "s_waitcnt vmcnt(1)\n"
        "v_xor_b32_e32 v6, s39, v80\n"
        "s_waitcnt vmcnt(0)\n"
        "v_bitop3_b32 v6, s39, v81, v6 bitop3:0xbe\n"
        "v_cmp_ne_u32_e32 vcc, 0, v6\n"
        "s_cbranch_vccnz .Lk97\n"
        ".Lk98:\n"
        ".Lk96:\n"
        "s_waitcnt lgkmcnt(0)\n"
        "s_barrier\n"
        "s_mul_i32 s58, s39, 0x9e3779b1\n"
        "s_lshl_b32 s47, s34, 10\n"
        "s_add_u32 s47, s47, 0x50000\n"
        "v_and_b32_e32 v30, 12, v1\n"
        "v_add_lshl_u32 v30, v30, v5, 2\n"
        "s_mul_i32 s42, s33, 0x590\n"
        "s_add_u32 s42, s42, 0x23500\n"
        "v_add_u32_e32 v30, s42, v30\n"
        "s_cmp_lt_u32 s33, 4\n"
        "s_cbranch_scc0 .Lk99\n"
        "s_add_u32 m0, s51, 0\n"
        "s_add_u32 s62, s61, 0\n"
        "ds_read2_b32 v[72:73], v84 offset1:20\n"
        "ds_read2_b32 v[74:75], v84 offset0:40 offset1:60\n"
        "ds_read2_b32 v[76:77], v41 offset0:60 offset1:40\n"
        "ds_read2_b32 v[78:79], v41 offset0:20\n"
        "buffer_load_dwordx4 v35, s[24:27], s50 offen offset:3072 lds\n"
        "buffer_load_dwordx4 v[116:119], v3, s[12:15], s62 offen sc1\n"
        "s_waitcnt lgkmcnt(2)\n"
        "v_mfma_f32_16x16x32_bf16 v[90:93], v[72:75], v[48:51], 0\n"
        "s_waitcnt lgkmcnt(1)\n"
        "v_xor_b32_e32 v86, 0x8000, v76\n"
        "v_xor_b32_e32 v87, 0x8000, v77\n"
        "v_mfma_f32_16x16x32_bf16 v[98:101], v[72:75], v[52:55], 0\n"
        "s_waitcnt lgkmcnt(0)\n"
        "v_xor_b32_e32 v88, 0x8000, v78\n"
        "v_xor_b32_e32 v89, 0x8000, v79\n"
        "buffer_load_dwordx4 v[120:123], v3, s[12:15], s62 offen offset:1024 sc1\n"
        "v_mfma_f32_16x16x32_bf16 v[94:97], v[72:75], v[224:227], 0\n"
        "v_xor_b32_e32 v76, 0x80000000, v76\n"
        "v_xor_b32_e32 v77, 0x80000000, v77\n"
        "v_mfma_f32_16x16x32_bf16 v[102:105], v[72:75], v[228:231], 0\n"
        "v_xor_b32_e32 v78, 0x80000000, v78\n"
        "v_xor_b32_e32 v79, 0x80000000, v79\n"
        "v_mfma_f32_16x16x32_bf16 v[90:93], v[86:89], v[48:51], v[90:93]\n"
        "v_mfma_f32_16x16x32_bf16 v[98:101], v[86:89], v[52:55], v[98:101]\n"
        "buffer_load_dwordx4 v[124:127], v3, s[12:15], s62 offen offset:2048 sc1\n"
        "v_mfma_f32_16x16x32_bf16 v[94:97], v[76:79], v[224:227], v[94:97]\n"
        "s_nop 2\n"
        "v_mul_f32_e32 v31, v90, v240\n"
        "v_mul_f32_e32 v106, v91, v241\n"
        "v_mfma_f32_16x16x32_bf16 v[102:105], v[76:79], v[228:231], v[102:105]\n"
        "v_mul_f32_e32 v85, v98, v240\n"
        "v_mul_f32_e32 v107, v99, v241\n"
        "v_mul_f32_e32 v108, v92, v242\n"
        "v_mul_f32_e32 v109, v100, v242\n"
        "v_mul_f32_e32 v110, v93, v243\n"
        "v_mul_f32_e32 v111, v101, v243\n"
        "v_fmac_f32_e32 v31, v98, v248\n"
        "buffer_load_dwordx4 v[128:131], v3, s[12:15], s62 offen offset:3072 sc1\n"
        "s_add_u32 s62, s61, 0x1000\n"
        "v_fma_f32 v85, -v90, v248, v85\n"
        "v_fmac_f32_e32 v106, v99, v249\n"
        "v_fma_f32 v107, -v91, v249, v107\n"
        "v_fmac_f32_e32 v108, v100, v250\n"
        "v_fma_f32 v109, -v92, v250, v109\n"
        "v_fmac_f32_e32 v110, v101, v251\n"
        "v_fma_f32 v111, -v93, v251, v111\n"
        "v_cvt_pk_bf16_f32 v48, v31, v85\n"
        "v_cvt_pk_bf16_f32 v49, v106, v107\n"
        "v_cvt_pk_bf16_f32 v50, v108, v109\n"
        "v_cvt_pk_bf16_f32 v51, v110, v111\n"
        "v_mul_f32_e32 v31, v94, v244\n"
        "v_mul_f32_e32 v85, v102, v244\n"
        "buffer_load_dwordx4 v[132:135], v3, s[12:15], s62 offen sc1\n"
        "v_mul_f32_e32 v106, v95, v245\n"
        "v_mul_f32_e32 v107, v103, v245\n"
        "v_mul_f32_e32 v108, v96, v246\n"
        "v_mul_f32_e32 v109, v104, v246\n"
        "v_mul_f32_e32 v110, v97, v247\n"
        "v_mul_f32_e32 v111, v105, v247\n"
        "v_fmac_f32_e32 v31, v102, v252\n"
        "v_fma_f32 v85, -v94, v252, v85\n"
        "v_fmac_f32_e32 v106, v103, v253\n"
        "v_fma_f32 v107, -v95, v253, v107\n"
        "v_fmac_f32_e32 v108, v104, v254\n"
        "v_fma_f32 v109, -v96, v254, v109\n"
        "v_fmac_f32_e32 v110, v105, v255\n"
        "v_fma_f32 v111, -v97, v255, v111\n"
        "buffer_load_dwordx4 v[136:139], v3, s[12:15], s62 offen offset:1024 sc1\n"
        "v_cvt_pk_bf16_f32 v224, v31, v85\n"
        "v_cvt_pk_bf16_f32 v225, v106, v107\n"
        "v_cvt_pk_bf16_f32 v226, v108, v109\n"
        "v_cvt_pk_bf16_f32 v227, v110, v111\n"
        "v_mfma_f32_16x16x32_bf16 v[56:59], v[232:235], v[48:51], v[56:59]\n"
        "v_mfma_f32_16x16x32_bf16 v[60:63], v[232:235], v[224:227], v[60:63]\n"
        "v_mfma_f32_16x16x32_bf16 v[64:67], v[236:239], v[48:51], v[64:67]\n"
        "buffer_load_dwordx4 v[140:143], v3, s[12:15], s62 offen offset:2048 sc1\n"
        "v_mfma_f32_16x16x32_bf16 v[68:71], v[236:239], v[224:227], v[68:71]\n"
        "s_nop 2\n"
        "v_cvt_pk_bf16_f32 v7, v56, v60\n"
        "v_cvt_pk_bf16_f32 v8, v57, v61\n"
        "v_dot2_f32_bf16 v112, v7, s40, 0\n"
        "v_dot2_f32_bf16 v113, v7, v7, 0\n"
        "v_cvt_pk_bf16_f32 v9, v58, v62\n"
        "v_dot2_f32_bf16 v112, v8, s40, v112\n"
        "v_dot2_f32_bf16 v113, v8, v8, v113\n"
        "v_cvt_pk_bf16_f32 v10, v59, v63\n"
        "v_dot2_f32_bf16 v112, v9, s40, v112\n"
        "v_dot2_f32_bf16 v113, v9, v9, v113\n"
        "v_cvt_pk_bf16_f32 v11, v64, v68\n"
        "buffer_load_dwordx4 v[144:147], v3, s[12:15], s62 offen offset:3072 sc1\n"
        "s_add_u32 s62, s61, 0x2000\n"
        "v_dot2_f32_bf16 v112, v10, s40, v112\n"
        "v_dot2_f32_bf16 v113, v10, v10, v113\n"
        "v_dot2_f32_bf16 v112, v11, s40, v112\n"
        "v_dot2_f32_bf16 v113, v11, v11, v113\n"
        "s_nop 1\n"
        "v_add_f32_dpp v112, v112, v112 quad_perm:[1,0,3,2] row_mask:0xf bank_mask:0xf\n"
        "v_add_f32_dpp v113, v113, v113 quad_perm:[1,0,3,2] row_mask:0xf bank_mask:0xf\n"
        "s_nop 0\n"
        "v_add_f32_dpp v112, v112, v112 quad_perm:[2,3,0,1] row_mask:0xf bank_mask:0xf\n"
        "v_add_f32_dpp v113, v113, v113 quad_perm:[2,3,0,1] row_mask:0xf bank_mask:0xf\n"
        "s_nop 0\n"
        "v_add_f32_dpp v112, v112, v112 row_ror:4 row_mask:0xf bank_mask:0xf\n"
        "v_add_f32_dpp v113, v113, v113 row_ror:4 row_mask:0xf bank_mask:0xf\n"
        "buffer_load_dwordx4 v[148:151], v3, s[12:15], s62 offen sc1\n"
        "v_add_f32_dpp v112, v112, v112 row_ror:8 row_mask:0xf bank_mask:0xf\n"
        "v_add_f32_dpp v113, v113, v113 row_ror:8 row_mask:0xf bank_mask:0xf\n"
        "s_nop 0\n"
        "v_add_f32_dpp v112, v112, v112 row_bcast:15 row_mask:0xa bank_mask:0xf\n"
        "v_add_f32_dpp v113, v113, v113 row_bcast:15 row_mask:0xa bank_mask:0xf\n"
        "s_nop 0\n"
        "v_add_f32_dpp v112, v112, v112 row_bcast:31 row_mask:0xc bank_mask:0xf\n"
        "v_add_f32_dpp v113, v113, v113 row_bcast:31 row_mask:0xc bank_mask:0xf\n"
        "v_readlane_b32 s46, v112, 63\n"
        "v_readlane_b32 s52, v113, 63\n"
        "s_nop 0\n"
        "v_mul_f32_e32 v112, s46, v13\n"
        "v_mul_f32_e32 v113, s52, v13\n"
        "v_fma_f32 v113, -v112, v112, v113\n"
        "buffer_load_dwordx4 v[152:155], v3, s[12:15], s62 offen offset:1024 sc1\n"
        "v_max_f32_e32 v113, 0, v113\n"
        "v_add_f32_e32 v113, 0x3727c5ac, v113\n"
        "v_rsq_f32_e32 v113, v113\n"
        "v_mov_b32_e32 v103, v112\n"
        "v_mul_f32_e32 v113, 0xbfb8aa3b, v113\n"
        "v_mul_f32_e64 v112, -v112, v113\n"
        "v_fma_f32 v56, v56, v113, v112\n"
        "v_fma_f32 v60, v60, v113, v112\n"
        "v_fma_f32 v57, v57, v113, v112\n"
        "v_fma_f32 v61, v61, v113, v112\n"
        "v_fma_f32 v58, v58, v113, v112\n"
        "v_fma_f32 v62, v62, v113, v112\n"
        "v_cvt_pk_bf16_f32 v90, v56, v60\n"
        "buffer_load_dwordx4 v[156:159], v3, s[12:15], s62 offen offset:2048 sc1\n"
        "v_cvt_pk_bf16_f32 v91, v57, v61\n"
        "v_cvt_pk_bf16_f32 v92, v58, v62\n"
        "v_fma_f32 v59, v59, v113, v112\n"
        "v_fma_f32 v63, v63, v113, v112\n"
        "v_fma_f32 v64, v64, v113, v112\n"
        "v_fma_f32 v68, v68, v113, v112\n"
        "v_cvt_pk_bf16_f32 v93, v59, v63\n"
        "v_cvt_pk_bf16_f32 v94, v64, v68\n"
        "v_add3_u32 v95, v90, v91, v92\n"
        "v_add3_u32 v95, v95, v93, v94\n"
        "ds_write_b32 v42, v90\n"
        "ds_write_b32 v42, v91 offset:256\n"
        "v_add_u32_dpp v95, v95, v95 quad_perm:[1,0,3,2] row_mask:0xf bank_mask:0xf\n"
        "v_rcp_f32_e32 v102, v113\n"
        "ds_write_b32 v42, v92 offset:512\n"
        "ds_write_b32 v42, v93 offset:768\n"
        "v_add_u32_dpp v95, v95, v95 quad_perm:[2,3,0,1] row_mask:0xf bank_mask:0xf\n"
        "ds_write_b32 v42, v94 offset:1024\n"
        "v_add_u32_e32 v95, s58, v95\n"
        "v_add3_u32 v104, v102, v103, s58\n"
        "ds_write_b32 v30, v95\n"
        "s_branch .Lk100\n"
        ".Lk99:\n"
        "s_add_u32 m0, s51, 0\n"
        "s_add_u32 s62, s61, 0\n"
        "ds_read2_b32 v[72:73], v84 offset1:20\n"
        "ds_read2_b32 v[74:75], v84 offset0:40 offset1:60\n"
        "ds_read2_b32 v[76:77], v41 offset0:60 offset1:40\n"
        "ds_read2_b32 v[78:79], v41 offset0:20\n"
        "buffer_load_dwordx4 v35, s[24:27], s50 offen offset:3072 lds\n"
        "buffer_load_dwordx4 v[116:119], v3, s[12:15], s62 offen sc1\n"
        "s_waitcnt lgkmcnt(2)\n"
        "v_mfma_f32_16x16x32_bf16 v[90:93], v[72:75], v[48:51], 0\n"
        "s_waitcnt lgkmcnt(1)\n"
        "v_xor_b32_e32 v86, 0x8000, v76\n"
        "v_xor_b32_e32 v87, 0x8000, v77\n"
        "v_mfma_f32_16x16x32_bf16 v[98:101], v[72:75], v[52:55], 0\n"
        "s_waitcnt lgkmcnt(0)\n"
        "v_xor_b32_e32 v88, 0x8000, v78\n"
        "v_xor_b32_e32 v89, 0x8000, v79\n"
        "buffer_load_dwordx4 v[120:123], v3, s[12:15], s62 offen offset:1024 sc1\n"
        "v_mfma_f32_16x16x32_bf16 v[94:97], v[72:75], v[224:227], 0\n"
        "v_xor_b32_e32 v76, 0x80000000, v76\n"
        "v_xor_b32_e32 v77, 0x80000000, v77\n"
        "v_mfma_f32_16x16x32_bf16 v[102:105], v[72:75], v[228:231], 0\n"
        "v_xor_b32_e32 v78, 0x80000000, v78\n"
        "v_xor_b32_e32 v79, 0x80000000, v79\n"
        "v_mfma_f32_16x16x32_bf16 v[90:93], v[86:89], v[48:51], v[90:93]\n"
        "v_mfma_f32_16x16x32_bf16 v[98:101], v[86:89], v[52:55], v[98:101]\n"
        "buffer_load_dwordx4 v[124:127], v3, s[12:15], s62 offen offset:2048 sc1\n"
        "v_mfma_f32_16x16x32_bf16 v[94:97], v[76:79], v[224:227], v[94:97]\n"
        "s_nop 2\n"
        "v_mul_f32_e32 v31, v90, v240\n"
        "v_mul_f32_e32 v106, v91, v241\n"
        "v_mfma_f32_16x16x32_bf16 v[102:105], v[76:79], v[228:231], v[102:105]\n"
        "v_mul_f32_e32 v85, v98, v240\n"
        "v_mul_f32_e32 v107, v99, v241\n"
        "v_mul_f32_e32 v108, v92, v242\n"
        "v_mul_f32_e32 v109, v100, v242\n"
        "v_mul_f32_e32 v110, v93, v243\n"
        "v_mul_f32_e32 v111, v101, v243\n"
        "v_fmac_f32_e32 v31, v98, v248\n"
        "buffer_load_dwordx4 v[128:131], v3, s[12:15], s62 offen offset:3072 sc1\n"
        "s_add_u32 s62, s61, 0x1000\n"
        "v_fma_f32 v85, -v90, v248, v85\n"
        "v_fmac_f32_e32 v106, v99, v249\n"
        "v_fma_f32 v107, -v91, v249, v107\n"
        "v_fmac_f32_e32 v108, v100, v250\n"
        "v_fma_f32 v109, -v92, v250, v109\n"
        "v_fmac_f32_e32 v110, v101, v251\n"
        "v_fma_f32 v111, -v93, v251, v111\n"
        "v_cvt_pk_bf16_f32 v48, v31, v85\n"
        "v_cvt_pk_bf16_f32 v49, v106, v107\n"
        "v_cvt_pk_bf16_f32 v50, v108, v109\n"
        "v_cvt_pk_bf16_f32 v51, v110, v111\n"
        "v_mul_f32_e32 v31, v94, v244\n"
        "v_mul_f32_e32 v85, v102, v244\n"
        "buffer_load_dwordx4 v[132:135], v3, s[12:15], s62 offen sc1\n"
        "v_mul_f32_e32 v106, v95, v245\n"
        "v_mul_f32_e32 v107, v103, v245\n"
        "v_mul_f32_e32 v108, v96, v246\n"
        "v_mul_f32_e32 v109, v104, v246\n"
        "v_mul_f32_e32 v110, v97, v247\n"
        "v_mul_f32_e32 v111, v105, v247\n"
        "v_fmac_f32_e32 v31, v102, v252\n"
        "v_fma_f32 v85, -v94, v252, v85\n"
        "v_fmac_f32_e32 v106, v103, v253\n"
        "v_fma_f32 v107, -v95, v253, v107\n"
        "v_fmac_f32_e32 v108, v104, v254\n"
        "v_fma_f32 v109, -v96, v254, v109\n"
        "v_fmac_f32_e32 v110, v105, v255\n"
        "v_fma_f32 v111, -v97, v255, v111\n"
        "v_cvt_pk_bf16_f32 v224, v31, v85\n"
        "v_cvt_pk_bf16_f32 v225, v106, v107\n"
        "v_cvt_pk_bf16_f32 v226, v108, v109\n"
        "v_cvt_pk_bf16_f32 v227, v110, v111\n"
        "v_mfma_f32_16x16x32_bf16 v[56:59], v[232:235], v[48:51], v[56:59]\n"
        "v_mfma_f32_16x16x32_bf16 v[60:63], v[232:235], v[224:227], v[60:63]\n"
        "v_mfma_f32_16x16x32_bf16 v[64:67], v[236:239], v[48:51], v[64:67]\n"
        "v_mfma_f32_16x16x32_bf16 v[68:71], v[236:239], v[224:227], v[68:71]\n"
        "s_nop 3\n"
        "v_cvt_pk_bf16_f32 v7, v56, v60\n"
        "v_cvt_pk_bf16_f32 v8, v57, v61\n"
        "v_dot2_f32_bf16 v114, v7, s40, 0\n"
        "v_dot2_f32_bf16 v115, v7, v7, 0\n"
        "v_cvt_pk_bf16_f32 v9, v58, v62\n"
        "v_dot2_f32_bf16 v114, v8, s40, v114\n"
        "v_dot2_f32_bf16 v115, v8, v8, v115\n"
        "v_cvt_pk_bf16_f32 v10, v59, v63\n"
        "v_dot2_f32_bf16 v114, v9, s40, v114\n"
        "v_dot2_f32_bf16 v115, v9, v9, v115\n"
        "v_cvt_pk_bf16_f32 v11, v64, v68\n"
        "v_dot2_f32_bf16 v114, v10, s40, v114\n"
        "v_dot2_f32_bf16 v115, v10, v10, v115\n"
        "v_dot2_f32_bf16 v114, v11, s40, v114\n"
        "v_dot2_f32_bf16 v115, v11, v11, v115\n"
        "s_nop 1\n"
        "v_add_f32_dpp v114, v114, v114 quad_perm:[1,0,3,2] row_mask:0xf bank_mask:0xf\n"
        "v_add_f32_dpp v115, v115, v115 quad_perm:[1,0,3,2] row_mask:0xf bank_mask:0xf\n"
        "s_nop 0\n"
        "v_add_f32_dpp v114, v114, v114 quad_perm:[2,3,0,1] row_mask:0xf bank_mask:0xf\n"
        "v_add_f32_dpp v115, v115, v115 quad_perm:[2,3,0,1] row_mask:0xf bank_mask:0xf\n"
        "s_nop 0\n"
        "v_add_f32_dpp v114, v114, v114 row_ror:4 row_mask:0xf bank_mask:0xf\n"
        "v_add_f32_dpp v115, v115, v115 row_ror:4 row_mask:0xf bank_mask:0xf\n"
        "s_nop 0\n"
        "v_add_f32_dpp v114, v114, v114 row_ror:8 row_mask:0xf bank_mask:0xf\n"
        "v_add_f32_dpp v115, v115, v115 row_ror:8 row_mask:0xf bank_mask:0xf\n"
        "s_nop 0\n"
        "v_add_f32_dpp v114, v114, v114 row_bcast:15 row_mask:0xa bank_mask:0xf\n"
        "v_add_f32_dpp v115, v115, v115 row_bcast:15 row_mask:0xa bank_mask:0xf\n"
        "s_nop 0\n"
        "v_add_f32_dpp v114, v114, v114 row_bcast:31 row_mask:0xc bank_mask:0xf\n"
        "v_add_f32_dpp v115, v115, v115 row_bcast:31 row_mask:0xc bank_mask:0xf\n"
        "v_readlane_b32 s53, v114, 63\n"
        "v_readlane_b32 s54, v115, 63\n"
        "s_nop 0\n"
        "v_mul_f32_e32 v114, s53, v13\n"
        "v_mul_f32_e32 v115, s54, v13\n"
        "v_fma_f32 v115, -v114, v114, v115\n"
        "v_max_f32_e32 v115, 0, v115\n"
        "v_add_f32_e32 v115, 0x3727c5ac, v115\n"
        "v_rsq_f32_e32 v115, v115\n"
        "v_mov_b32_e32 v103, v114\n"
        "v_mul_f32_e32 v115, 0xbfb8aa3b, v115\n"
        "v_mul_f32_e64 v114, -v114, v115\n"
        "v_fma_f32 v56, v56, v115, v114\n"
        "v_fma_f32 v60, v60, v115, v114\n"
        "v_fma_f32 v57, v57, v115, v114\n"
        "v_fma_f32 v61, v61, v115, v114\n"
        "v_fma_f32 v58, v58, v115, v114\n"
        "v_fma_f32 v62, v62, v115, v114\n"
        "v_cvt_pk_bf16_f32 v90, v56, v60\n"
        "v_cvt_pk_bf16_f32 v91, v57, v61\n"
        "v_cvt_pk_bf16_f32 v92, v58, v62\n"
        "v_fma_f32 v59, v59, v115, v114\n"
        "v_fma_f32 v63, v63, v115, v114\n"
        "v_fma_f32 v64, v64, v115, v114\n"
        "v_fma_f32 v68, v68, v115, v114\n"
        "v_cvt_pk_bf16_f32 v93, v59, v63\n"
        "v_cvt_pk_bf16_f32 v94, v64, v68\n"
        "v_add3_u32 v95, v90, v91, v92\n"
        "v_add3_u32 v95, v95, v93, v94\n"
        "ds_write_b32 v42, v90\n"
        "ds_write_b32 v42, v91 offset:256\n"
        "v_add_u32_dpp v95, v95, v95 quad_perm:[1,0,3,2] row_mask:0xf bank_mask:0xf\n"
        "v_rcp_f32_e32 v102, v115\n"
        "ds_write_b32 v42, v92 offset:512\n"
        "ds_write_b32 v42, v93 offset:768\n"
        "v_add_u32_dpp v95, v95, v95 quad_perm:[2,3,0,1] row_mask:0xf bank_mask:0xf\n"
        "ds_write_b32 v42, v94 offset:1024\n"
        "v_add_u32_e32 v95, s58, v95\n"
        "v_add3_u32 v104, v102, v103, s58\n"
        "ds_write_b32 v30, v95\n"
        ".Lk100:\n"
        "s_mov_b64 exec, 1\n"
        "ds_write_b128 v42, v[102:105] offset:1344\n"
        "s_mov_b64 exec, -1\n"
        "s_waitcnt lgkmcnt(0)\n"
        "s_barrier\n"
        "ds_read_b128 v[98:101], v46\n"
        "s_cmp_lt_u32 s33, 3\n"
        "s_cbranch_scc0 .Lk101\n"
        "ds_read_b128 v[102:105], v46 offset:1024\n"
        ".Lk101:\n"
        "s_waitcnt lgkmcnt(0)\n"
        "buffer_store_dwordx4 v[98:101], v47, s[12:15], s49 offen sc1\n"
        "s_cmp_lt_u32 s33, 2\n"
        "s_cbranch_scc0 .Lk102\n"
        "buffer_store_dwordx4 v[102:105], v47, s[12:15], s59 offen sc1\n"
        ".Lk102:\n"
        "s_cmp_lt_u32 s33, 4\n"
        "s_cbranch_scc0 .Lk103\n"
        "s_cmp_lt_u32 s33, 2\n"
        "s_cbranch_scc0 .Lk104\n"
        "s_waitcnt vmcnt(13)\n"
        "s_branch .Lk105\n"
        ".Lk104:\n"
        "s_waitcnt vmcnt(12)\n"
        ".Lk105:\n"
        "s_branch .Lk106\n"
        ".Lk103:\n"
        "s_waitcnt vmcnt(6)\n"
        ".Lk106:\n"
        "s_barrier\n"
        "s_cmp_eq_u32 s33, 2\n"
        "s_cbranch_scc0 .Lk107\n"
        "s_mov_b32 s7, 0xff\n"
        "s_mov_b32 s6, -1\n"
        "s_mov_b64 exec, s[6:7]\n"
        "buffer_store_dwordx4 v[102:105], v47, s[12:15], s59 offen sc1\n"
        "s_mov_b64 exec, -1\n"
        ".Lk107:\n"
        ".Lkg:\n"
        "v_mov_b32_e32 v4, v39\n"
        "v_add_u32_e32 v5, 0xfc00, v3\n"
        "v_add_u32_e32 v1, 0x1f800, v3\n"
        "s_lshr_b32 s42, s34, 1\n"
        "s_lshr_b32 s43, s35, 3\n"
        "s_lshl2_add_u32 s55, s42, s43\n"
        "s_lshl3_add_u32 s55, s55, s33\n"
        "s_mul_i32 s49, s55, 0x5500\n"
        "s_add_u32 s49, s49, 0x560000\n"
        "s_add_u32 s59, s49, 0x1000\n"
        "s_add_u32 s56, s49, 0x2000\n"
        "s_add_u32 s60, s49, 0x3000\n"
        "s_add_u32 s57, s49, 0x4000\n"
        "s_add_u32 s63, s49, 0x5000\n"
        "s_cmp_lt_u32 s33, 4\n"
        "s_cbranch_scc0 .Lk108\n"
        "s_mul_i32 s43, s33, 0x1c00\n"
        "s_add_u32 s43, s43, 0xe000\n"
        "v_add_u32_e32 v88, s43, v4\n"
        "v_add_u32_e32 v89, s43, v3\n"
        "s_add_u32 s62, s61, 0x2000\n"
        "buffer_load_dwordx4 v[160:163], v3, s[12:15], s62 offen offset:3072 sc1\n"
        "s_add_u32 s62, s61, 0x3000\n"
        "buffer_load_dwordx4 v[164:167], v3, s[12:15], s62 offen sc1\n"
        "ds_read_b64_tr_b16 v[60:61], v88\n"
        "ds_read_b64_tr_b16 v[62:63], v88 offset:896\n"
        "ds_read_b64_tr_b16 v[64:65], v88 offset:32\n"
        "ds_read_b64_tr_b16 v[66:67], v88 offset:928\n"
        "ds_read_b64_tr_b16 v[68:69], v88 offset:64\n"
        "ds_read_b64_tr_b16 v[70:71], v88 offset:960\n"
        "ds_read_b64_tr_b16 v[72:73], v88 offset:96\n"
        "ds_read_b64_tr_b16 v[74:75], v88 offset:992\n"
        "ds_read_b64_tr_b16 v[76:77], v88 offset:128\n"
        "ds_read_b64_tr_b16 v[78:79], v88 offset:1024\n"
        "ds_read_b64_tr_b16 v[80:81], v88 offset:160\n"
        "ds_read_b64_tr_b16 v[82:83], v88 offset:1056\n"
        "ds_read_b64_tr_b16 v[84:85], v88 offset:192\n"
        "ds_read_b64_tr_b16 v[86:87], v88 offset:1088\n"
        "buffer_load_dwordx4 v[168:171], v3, s[12:15], s62 offen offset:1024 sc1\n"
        "buffer_load_dwordx4 v[172:175], v3, s[12:15], s62 offen offset:2048 sc1\n"
        "buffer_load_dwordx4 v[176:179], v3, s[12:15], s62 offen offset:3072 sc1\n"
        "s_add_u32 s62, s61, 0x4000\n"
        "buffer_load_dwordx4 v[180:183], v3, s[12:15], s62 offen sc1\n"
        "s_waitcnt lgkmcnt(12)\n"
        "ds_write_b128 v89, v[60:63]\n"
        "s_waitcnt lgkmcnt(11)\n"
        "ds_write_b128 v89, v[64:67] offset:1024\n"
        "s_waitcnt lgkmcnt(10)\n"
        "ds_write_b128 v89, v[68:71] offset:2048\n"
        "s_waitcnt lgkmcnt(9)\n"
        "ds_write_b128 v89, v[72:75] offset:3072\n"
        "s_waitcnt lgkmcnt(8)\n"
        "ds_write_b128 v89, v[76:79] offset:4096\n"
        "s_waitcnt lgkmcnt(7)\n"
        "ds_write_b128 v89, v[80:83] offset:5120\n"
        "s_waitcnt lgkmcnt(6)\n"
        "ds_write_b128 v89, v[84:87] offset:6144\n"
        "buffer_load_dwordx4 v[184:187], v3, s[12:15], s62 offen offset:1024 sc1\n"
        "buffer_load_dwordx4 v[188:191], v3, s[12:15], s62 offen offset:2048 sc1\n"
        "buffer_load_dwordx4 v[192:195], v3, s[12:15], s62 offen offset:3072 sc1\n"
        "s_add_u32 s62, s61, 0x5000\n"
        "buffer_load_dwordx4 v[196:199], v3, s[12:15], s62 offen sc1\n"
        "s_waitcnt vmcnt(10)\n"
        "s_sleep 16\n"
        "v_and_b32_e32 v108, 0xf0, v3\n"
        "buffer_load_dwordx3 v[112:114], v108, s[12:15], s63 offen offset:1024 sc1\n"
        "buffer_load_dwordx4 v[8:11], v3, s[12:15], s63 offen sc1\n"
        "buffer_load_dwordx4 v[12:15], v3, s[12:15], s57 offen sc1\n"
        "buffer_load_dwordx4 v[16:19], v3, s[12:15], s57 offen offset:1024 sc1\n"
        "buffer_load_dwordx4 v[20:23], v3, s[12:15], s57 offen offset:2048 sc1\n"
        "buffer_load_dwordx4 v[24:27], v3, s[12:15], s57 offen offset:3072 sc1\n"
        "buffer_load_dwordx4 v[28:31], v3, s[12:15], s49 offen sc1\n"
        "buffer_load_dwordx4 v[32:35], v3, s[12:15], s49 offen offset:1024 sc1\n"
        "buffer_load_dwordx4 v[36:39], v3, s[12:15], s49 offen offset:2048 sc1\n"
        "buffer_load_dwordx4 v[40:43], v3, s[12:15], s49 offen offset:3072 sc1\n"
        "s_branch .Lk109\n"
        ".Lk108:\n"
        "s_mul_i32 s43, s33, 0x1c00\n"
        "s_add_u32 s43, s43, 0xe000\n"
        "v_add_u32_e32 v88, s43, v4\n"
        "v_add_u32_e32 v89, s43, v3\n"
        "s_add_u32 s62, s61, 0x1000\n"
        "buffer_load_dwordx4 v[136:139], v3, s[12:15], s62 offen offset:1024 sc1\n"
        "buffer_load_dwordx4 v[140:143], v3, s[12:15], s62 offen offset:2048 sc1\n"
        "ds_read_b64_tr_b16 v[60:61], v88\n"
        "ds_read_b64_tr_b16 v[62:63], v88 offset:896\n"
        "ds_read_b64_tr_b16 v[64:65], v88 offset:32\n"
        "ds_read_b64_tr_b16 v[66:67], v88 offset:928\n"
        "ds_read_b64_tr_b16 v[68:69], v88 offset:64\n"
        "ds_read_b64_tr_b16 v[70:71], v88 offset:960\n"
        "ds_read_b64_tr_b16 v[72:73], v88 offset:96\n"
        "ds_read_b64_tr_b16 v[74:75], v88 offset:992\n"
        "ds_read_b64_tr_b16 v[76:77], v88 offset:128\n"
        "ds_read_b64_tr_b16 v[78:79], v88 offset:1024\n"
        "ds_read_b64_tr_b16 v[80:81], v88 offset:160\n"
        "ds_read_b64_tr_b16 v[82:83], v88 offset:1056\n"
        "ds_read_b64_tr_b16 v[84:85], v88 offset:192\n"
        "ds_read_b64_tr_b16 v[86:87], v88 offset:1088\n"
        "buffer_load_dwordx4 v[144:147], v3, s[12:15], s62 offen offset:3072 sc1\n"
        "s_add_u32 s62, s61, 0x2000\n"
        "buffer_load_dwordx4 v[148:151], v3, s[12:15], s62 offen sc1\n"
        "s_waitcnt lgkmcnt(12)\n"
        "ds_write_b128 v89, v[60:63]\n"
        "s_waitcnt lgkmcnt(11)\n"
        "ds_write_b128 v89, v[64:67] offset:1024\n"
        "s_waitcnt lgkmcnt(10)\n"
        "ds_write_b128 v89, v[68:71] offset:2048\n"
        "s_waitcnt lgkmcnt(9)\n"
        "ds_write_b128 v89, v[72:75] offset:3072\n"
        "s_waitcnt lgkmcnt(8)\n"
        "ds_write_b128 v89, v[76:79] offset:4096\n"
        "s_waitcnt lgkmcnt(7)\n"
        "ds_write_b128 v89, v[80:83] offset:5120\n"
        "s_waitcnt lgkmcnt(6)\n"
        "ds_write_b128 v89, v[84:87] offset:6144\n"
        "buffer_load_dwordx4 v[152:155], v3, s[12:15], s62 offen offset:1024 sc1\n"
        "buffer_load_dwordx4 v[156:159], v3, s[12:15], s62 offen offset:2048 sc1\n"
        "s_mul_i32 s43, s33, 0x1c00\n"
        "s_add_u32 s43, s43, 0x15000\n"
        "v_add_u32_e32 v88, s43, v4\n"
        "v_add_u32_e32 v89, s43, v3\n"
        "ds_read_b64_tr_b16 v[60:61], v88\n"
        "ds_read_b64_tr_b16 v[62:63], v88 offset:896\n"
        "ds_read_b64_tr_b16 v[64:65], v88 offset:32\n"
        "ds_read_b64_tr_b16 v[66:67], v88 offset:928\n"
        "ds_read_b64_tr_b16 v[68:69], v88 offset:64\n"
        "ds_read_b64_tr_b16 v[70:71], v88 offset:960\n"
        "ds_read_b64_tr_b16 v[72:73], v88 offset:96\n"
        "ds_read_b64_tr_b16 v[74:75], v88 offset:992\n"
        "ds_read_b64_tr_b16 v[76:77], v88 offset:128\n"
        "ds_read_b64_tr_b16 v[78:79], v88 offset:1024\n"
        "ds_read_b64_tr_b16 v[80:81], v88 offset:160\n"
        "ds_read_b64_tr_b16 v[82:83], v88 offset:1056\n"
        "ds_read_b64_tr_b16 v[84:85], v88 offset:192\n"
        "ds_read_b64_tr_b16 v[86:87], v88 offset:1088\n"
        "s_waitcnt lgkmcnt(12)\n"
        "ds_write_b128 v89, v[60:63]\n"
        "s_waitcnt lgkmcnt(11)\n"
        "ds_write_b128 v89, v[64:67] offset:1024\n"
        "s_waitcnt lgkmcnt(10)\n"
        "ds_write_b128 v89, v[68:71] offset:2048\n"
        "s_waitcnt lgkmcnt(9)\n"
        "ds_write_b128 v89, v[72:75] offset:3072\n"
        "s_waitcnt lgkmcnt(8)\n"
        "ds_write_b128 v89, v[76:79] offset:4096\n"
        "s_waitcnt lgkmcnt(7)\n"
        "ds_write_b128 v89, v[80:83] offset:5120\n"
        "s_waitcnt lgkmcnt(6)\n"
        "ds_write_b128 v89, v[84:87] offset:6144\n"
        ".Lk109:\n"
        "s_waitcnt lgkmcnt(0)\n"
        "s_barrier\n"
        "v_mul_u32_u24_e32 v4, 3, v2\n"
        "s_lshr_b32 s42, s34, 1\n"
        "s_lshr_b32 s43, s35, 3\n"
        "s_lshl2_add_u32 s42, s42, s43\n"
        "s_bfe_u32 s43, s35, 0x10002\n"
        "s_lshl_b32 s43, s43, 1\n"
        "s_lshl3_add_u32 s45, s42, s43\n"
        "s_lshl3_add_u32 s48, s45, s33\n"
        "s_mul_i32 s48, s48, 0x1e00\n"
        "s_add_u32 s48, s48, 0x808000\n"
        "s_lshl4_add_u32 s46, s55, s41\n"
        "s_mul_i32 s46, s46, 0xf00\n"
        "s_add_u32 s46, s46, 0x808000\n"
        "s_sub_u32 s42, s41, 10\n"
        "s_cmp_lt_u32 s42, 4\n"
        "s_cselect_b32 s41, 0, -1\n"
        "s_mov_b32 s52, 0\n"
        "s_cmp_lt_u32 s33, 4\n"
        "s_cbranch_scc0 .Lk110\n"
        "s_setprio 1\n"
        "ds_read_b128 v[60:63], v5 offset:50176\n"
        "ds_read_b128 v[64:67], v5 offset:51200\n"
        "ds_read_b128 v[68:71], v5 offset:52224\n"
        "ds_read_b128 v[72:75], v5 offset:53248\n"
        "ds_read_b128 v[76:79], v5 offset:54272\n"
        "ds_read_b128 v[80:83], v5 offset:55296\n"
        "ds_read_b128 v[84:87], v5 offset:56320\n"
        "s_waitcnt vmcnt(9)\n"
        "v_add3_u32 v6, v112, v113, s58\n"
        "v_cmp_ne_u32_e32 vcc, v6, v114\n"
        "s_waitcnt vmcnt(8)\n"
        "v_add3_u32 v7, v8, v9, v10\n"
        "s_lshl_b32 s42, s58, 2\n"
        "v_add_u32_e32 v7, v7, v11\n"
        "v_mov_b32_e32 v6, s42\n"
        "s_cbranch_vccnz .Lk111\n"
        ".Lk112:\n"
        "s_waitcnt vmcnt(7) lgkmcnt(6)\n"
        "v_mfma_f32_16x16x32_bf16 v[88:91], v[60:63], v[12:15], 0\n"
        "ds_read_b128 v[60:63], v5 offset:57344\n"
        "buffer_load_dwordx4 v[44:47], v3, s[12:15], s59 offen sc1\n"
        "s_waitcnt lgkmcnt(6)\n"
        "v_mfma_f32_16x16x32_bf16 v[92:95], v[64:67], v[12:15], 0\n"
        "ds_read_b128 v[64:67], v5 offset:58368\n"
        "s_waitcnt lgkmcnt(5)\n"
        "v_mfma_f32_16x16x32_bf16 v[96:99], v[68:71], v[12:15], 0\n"
        "ds_read_b128 v[68:71], v5 offset:59392\n"
        "v_add3_u32 v6, v6, v12, v13\n"
        "v_mfma_f32_16x16x32_bf16 v[100:103], v[72:75], v[12:15], 0\n"
        "ds_read_b128 v[72:75], v5 offset:60416\n"
        "s_waitcnt lgkmcnt(5)\n"
        "v_mfma_f32_16x16x32_bf16 v[104:107], v[76:79], v[12:15], 0\n"
        "ds_read_b128 v[76:79], v5 offset:61440\n"
        "buffer_load_dwordx4 v[48:51], v3, s[12:15], s59 offen offset:1024 sc1\n"
        "v_mfma_f32_16x16x32_bf16 v[108:111], v[80:83], v[12:15], 0\n"
        "ds_read_b128 v[80:83], v5 offset:62464\n"
        "s_waitcnt vmcnt(8) lgkmcnt(5)\n"
        "v_mfma_f32_16x16x32_bf16 v[112:115], v[84:87], v[12:15], 0\n"
        "ds_read_b128 v[84:87], v5 offset:63488\n"
        "v_add3_u32 v6, v6, v14, v15\n"
        "v_mfma_f32_16x16x32_bf16 v[88:91], v[60:63], v[16:19], v[88:91]\n"
        "ds_read_b128 v[60:63], v1\n"
        "s_waitcnt lgkmcnt(5)\n"
        "v_mfma_f32_16x16x32_bf16 v[92:95], v[64:67], v[16:19], v[92:95]\n"
        "ds_read_b128 v[64:67], v1 offset:1024\n"
        "buffer_load_dwordx4 v[52:55], v3, s[12:15], s59 offen offset:2048 sc1\n"
        "v_mfma_f32_16x16x32_bf16 v[96:99], v[68:71], v[16:19], v[96:99]\n"
        "ds_read_b128 v[68:71], v1 offset:2048\n"
        "s_waitcnt lgkmcnt(5)\n"
        "v_mfma_f32_16x16x32_bf16 v[100:103], v[72:75], v[16:19], v[100:103]\n"
        "ds_read_b128 v[72:75], v1 offset:3072\n"
        "v_add3_u32 v6, v6, v16, v17\n"
        "v_mfma_f32_16x16x32_bf16 v[104:107], v[76:79], v[16:19], v[104:107]\n"
        "ds_read_b128 v[76:79], v1 offset:4096\n"
        "s_waitcnt vmcnt(8) lgkmcnt(5)\n"
        "v_mfma_f32_16x16x32_bf16 v[108:111], v[80:83], v[16:19], v[108:111]\n"
        "ds_read_b128 v[80:83], v1 offset:5120\n"
        "v_add3_u32 v6, v6, v18, v19\n"
        "v_mfma_f32_16x16x32_bf16 v[112:115], v[84:87], v[16:19], v[112:115]\n"
        "ds_read_b128 v[84:87], v1 offset:6144\n"
        "s_waitcnt lgkmcnt(5)\n"
        "v_mfma_f32_16x16x32_bf16 v[88:91], v[60:63], v[20:23], v[88:91]\n"
        "ds_read_b128 v[60:63], v1 offset:7168\n"
        "buffer_load_dwordx4 v[56:59], v3, s[12:15], s59 offen offset:3072 sc1\n"
        "v_mfma_f32_16x16x32_bf16 v[92:95], v[64:67], v[20:23], v[92:95]\n"
        "ds_read_b128 v[64:67], v1 offset:8192\n"
        "s_waitcnt lgkmcnt(5)\n"
        "v_mfma_f32_16x16x32_bf16 v[96:99], v[68:71], v[20:23], v[96:99]\n"
        "ds_read_b128 v[68:71], v1 offset:9216\n"
        "v_add3_u32 v6, v6, v20, v21\n"
        "v_mfma_f32_16x16x32_bf16 v[100:103], v[72:75], v[20:23], v[100:103]\n"
        "ds_read_b128 v[72:75], v1 offset:10240\n"
        "s_waitcnt lgkmcnt(5)\n"
        "v_mfma_f32_16x16x32_bf16 v[104:107], v[76:79], v[20:23], v[104:107]\n"
        "ds_read_b128 v[76:79], v1 offset:11264\n"
        "buffer_load_dwordx4 v[12:15], v3, s[12:15], s56 offen sc1\n"
        "v_mfma_f32_16x16x32_bf16 v[108:111], v[80:83], v[20:23], v[108:111]\n"
        "ds_read_b128 v[80:83], v1 offset:12288\n"
        "s_waitcnt vmcnt(9) lgkmcnt(5)\n"
        "v_mfma_f32_16x16x32_bf16 v[112:115], v[84:87], v[20:23], v[112:115]\n"
        "ds_read_b128 v[84:87], v1 offset:13312\n"
        "v_add3_u32 v6, v6, v22, v23\n"
        "v_mfma_f32_16x16x32_bf16 v[88:91], v[60:63], v[24:27], v[88:91]\n"
        "ds_read_b128 v[60:63], v3\n"
        "s_waitcnt lgkmcnt(5)\n"
        "v_mfma_f32_16x16x32_bf16 v[92:95], v[64:67], v[24:27], v[92:95]\n"
        "ds_read_b128 v[64:67], v3 offset:1024\n"
        "buffer_load_dwordx4 v[16:19], v3, s[12:15], s56 offen offset:1024 sc1\n"
        "v_mfma_f32_16x16x32_bf16 v[96:99], v[68:71], v[24:27], v[96:99]\n"
        "ds_read_b128 v[68:71], v3 offset:2048\n"
        "s_waitcnt lgkmcnt(5)\n"
        "v_mfma_f32_16x16x32_bf16 v[100:103], v[72:75], v[24:27], v[100:103]\n"
        "ds_read_b128 v[72:75], v3 offset:3072\n"
        "v_add3_u32 v6, v6, v24, v25\n"
        "v_mfma_f32_16x16x32_bf16 v[104:107], v[76:79], v[24:27], v[104:107]\n"
        "ds_read_b128 v[76:79], v3 offset:4096\n"
        "s_waitcnt vmcnt(9) lgkmcnt(5)\n"
        "v_mfma_f32_16x16x32_bf16 v[108:111], v[80:83], v[24:27], v[108:111]\n"
        "ds_read_b128 v[80:83], v3 offset:5120\n"
        "v_add3_u32 v6, v6, v26, v27\n"
        "v_mfma_f32_16x16x32_bf16 v[112:115], v[84:87], v[24:27], v[112:115]\n"
        "ds_read_b128 v[84:87], v3 offset:6144\n"
        "s_waitcnt lgkmcnt(5)\n"
        "v_mfma_f32_16x16x32_bf16 v[88:91], v[60:63], v[28:31], v[88:91]\n"
        "ds_read_b128 v[60:63], v3 offset:7168\n"
        "buffer_load_dwordx4 v[20:23], v3, s[12:15], s56 offen offset:2048 sc1\n"
        "v_mfma_f32_16x16x32_bf16 v[92:95], v[64:67], v[28:31], v[92:95]\n"
        "ds_read_b128 v[64:67], v3 offset:8192\n"
        "s_waitcnt lgkmcnt(5)\n"
        "v_mfma_f32_16x16x32_bf16 v[96:99], v[68:71], v[28:31], v[96:99]\n"
        "ds_read_b128 v[68:71], v3 offset:9216\n"
        "v_add3_u32 v6, v6, v28, v29\n"
        "v_mfma_f32_16x16x32_bf16 v[100:103], v[72:75], v[28:31], v[100:103]\n"
        "ds_read_b128 v[72:75], v3 offset:10240\n"
        "s_waitcnt lgkmcnt(5)\n"
        "v_mfma_f32_16x16x32_bf16 v[104:107], v[76:79], v[28:31], v[104:107]\n"
        "ds_read_b128 v[76:79], v3 offset:11264\n"
        "buffer_load_dwordx4 v[24:27], v3, s[12:15], s56 offen offset:3072 sc1\n"
        "v_mfma_f32_16x16x32_bf16 v[108:111], v[80:83], v[28:31], v[108:111]\n"
        "ds_read_b128 v[80:83], v3 offset:12288\n"
        "s_waitcnt vmcnt(10) lgkmcnt(5)\n"
        "v_mfma_f32_16x16x32_bf16 v[112:115], v[84:87], v[28:31], v[112:115]\n"
        "ds_read_b128 v[84:87], v3 offset:13312\n"
        "v_add3_u32 v6, v6, v30, v31\n"
        "v_mfma_f32_16x16x32_bf16 v[88:91], v[60:63], v[32:35], v[88:91]\n"
        "ds_read_b128 v[60:63], v3 offset:14336\n"
        "s_waitcnt lgkmcnt(5)\n"
        "v_mfma_f32_16x16x32_bf16 v[92:95], v[64:67], v[32:35], v[92:95]\n"
        "ds_read_b128 v[64:67], v3 offset:15360\n"
        "buffer_load_dwordx4 v[28:31], v3, s[12:15], s60 offen sc1\n"
        "v_mfma_f32_16x16x32_bf16 v[96:99], v[68:71], v[32:35], v[96:99]\n"
        "ds_read_b128 v[68:71], v3 offset:16384\n"
        "s_waitcnt lgkmcnt(5)\n"
        "v_mfma_f32_16x16x32_bf16 v[100:103], v[72:75], v[32:35], v[100:103]\n"
        "ds_read_b128 v[72:75], v3 offset:17408\n"
        "v_add3_u32 v6, v6, v32, v33\n"
        "v_mfma_f32_16x16x32_bf16 v[104:107], v[76:79], v[32:35], v[104:107]\n"
        "ds_read_b128 v[76:79], v3 offset:18432\n"
        "s_waitcnt vmcnt(10) lgkmcnt(5)\n"
        "v_mfma_f32_16x16x32_bf16 v[108:111], v[80:83], v[32:35], v[108:111]\n"
        "ds_read_b128 v[80:83], v3 offset:19456\n"
        "v_add3_u32 v6, v6, v34, v35\n"
        "v_mfma_f32_16x16x32_bf16 v[112:115], v[84:87], v[32:35], v[112:115]\n"
        "ds_read_b128 v[84:87], v3 offset:20480\n"
        "s_waitcnt lgkmcnt(5)\n"
        "v_mfma_f32_16x16x32_bf16 v[88:91], v[60:63], v[36:39], v[88:91]\n"
        "ds_read_b128 v[60:63], v3 offset:21504\n"
        "buffer_load_dwordx4 v[32:35], v3, s[12:15], s60 offen offset:1024 sc1\n"
        "v_mfma_f32_16x16x32_bf16 v[92:95], v[64:67], v[36:39], v[92:95]\n"
        "ds_read_b128 v[64:67], v3 offset:22528\n"
        "s_waitcnt lgkmcnt(5)\n"
        "v_mfma_f32_16x16x32_bf16 v[96:99], v[68:71], v[36:39], v[96:99]\n"
        "ds_read_b128 v[68:71], v3 offset:23552\n"
        "v_add3_u32 v6, v6, v36, v37\n"
        "v_mfma_f32_16x16x32_bf16 v[100:103], v[72:75], v[36:39], v[100:103]\n"
        "ds_read_b128 v[72:75], v3 offset:24576\n"
        "s_waitcnt lgkmcnt(5)\n"
        "v_mfma_f32_16x16x32_bf16 v[104:107], v[76:79], v[36:39], v[104:107]\n"
        "ds_read_b128 v[76:79], v3 offset:25600\n"
        "v_add3_u32 v6, v6, v38, v39\n"
        "v_mfma_f32_16x16x32_bf16 v[108:111], v[80:83], v[36:39], v[108:111]\n"
        "ds_read_b128 v[80:83], v3 offset:26624\n"
        "s_waitcnt vmcnt(10) lgkmcnt(5)\n"
        "v_mfma_f32_16x16x32_bf16 v[112:115], v[84:87], v[36:39], v[112:115]\n"
        "ds_read_b128 v[84:87], v3 offset:27648\n"
        "v_mfma_f32_16x16x32_bf16 v[88:91], v[60:63], v[40:43], v[88:91]\n"
        "ds_read_b128 v[60:63], v3 offset:28672\n"
        "s_waitcnt lgkmcnt(5)\n"
        "v_mfma_f32_16x16x32_bf16 v[92:95], v[64:67], v[40:43], v[92:95]\n"
        "ds_read_b128 v[64:67], v3 offset:29696\n"
        "buffer_load_dwordx4 v[36:39], v3, s[12:15], s60 offen offset:2048 sc1\n"
        "v_mfma_f32_16x16x32_bf16 v[96:99], v[68:71], v[40:43], v[96:99]\n"
        "ds_read_b128 v[68:71], v3 offset:30720\n"
        "s_waitcnt lgkmcnt(5)\n"
        "v_mfma_f32_16x16x32_bf16 v[100:103], v[72:75], v[40:43], v[100:103]\n"
        "ds_read_b128 v[72:75], v3 offset:31744\n"
        "v_add3_u32 v6, v6, v40, v41\n"
        "v_mfma_f32_16x16x32_bf16 v[104:107], v[76:79], v[40:43], v[104:107]\n"
        "ds_read_b128 v[76:79], v3 offset:32768\n"
        "s_waitcnt vmcnt(10) lgkmcnt(5)\n"
        "v_mfma_f32_16x16x32_bf16 v[108:111], v[80:83], v[40:43], v[108:111]\n"
        "ds_read_b128 v[80:83], v3 offset:33792\n"
        "v_add3_u32 v6, v6, v42, v43\n"
        "v_mfma_f32_16x16x32_bf16 v[112:115], v[84:87], v[40:43], v[112:115]\n"
        "ds_read_b128 v[84:87], v3 offset:34816\n"
        "s_waitcnt lgkmcnt(5)\n"
        "v_mfma_f32_16x16x32_bf16 v[88:91], v[60:63], v[44:47], v[88:91]\n"
        "ds_read_b128 v[60:63], v3 offset:35840\n"
        "buffer_load_dwordx4 v[40:43], v3, s[12:15], s60 offen offset:3072 sc1\n"
        "v_mfma_f32_16x16x32_bf16 v[92:95], v[64:67], v[44:47], v[92:95]\n"
        "ds_read_b128 v[64:67], v3 offset:36864\n"
        "s_waitcnt lgkmcnt(5)\n"
        "v_mfma_f32_16x16x32_bf16 v[96:99], v[68:71], v[44:47], v[96:99]\n"
        "ds_read_b128 v[68:71], v3 offset:37888\n"
        "v_add3_u32 v6, v6, v44, v45\n"
        "v_mfma_f32_16x16x32_bf16 v[100:103], v[72:75], v[44:47], v[100:103]\n"
        "ds_read_b128 v[72:75], v3 offset:38912\n"
        "s_waitcnt lgkmcnt(5)\n"
        "v_mfma_f32_16x16x32_bf16 v[104:107], v[76:79], v[44:47], v[104:107]\n"
        "ds_read_b128 v[76:79], v3 offset:39936\n"
        "v_add3_u32 v6, v6, v46, v47\n"
        "v_mfma_f32_16x16x32_bf16 v[108:111], v[80:83], v[44:47], v[108:111]\n"
        "ds_read_b128 v[80:83], v3 offset:40960\n"
        "s_waitcnt vmcnt(10) lgkmcnt(5)\n"
        "v_mfma_f32_16x16x32_bf16 v[112:115], v[84:87], v[44:47], v[112:115]\n"
        "ds_read_b128 v[84:87], v3 offset:41984\n"
        "v_mfma_f32_16x16x32_bf16 v[88:91], v[60:63], v[48:51], v[88:91]\n"
        "ds_read_b128 v[60:63], v3 offset:43008\n"
        "s_waitcnt lgkmcnt(5)\n"
        "v_mfma_f32_16x16x32_bf16 v[92:95], v[64:67], v[48:51], v[92:95]\n"
        "ds_read_b128 v[64:67], v3 offset:44032\n"
        "v_add3_u32 v6, v6, v48, v49\n"
        "v_mfma_f32_16x16x32_bf16 v[96:99], v[68:71], v[48:51], v[96:99]\n"
        "ds_read_b128 v[68:71], v3 offset:45056\n"
        "s_waitcnt lgkmcnt(5)\n"
        "v_mfma_f32_16x16x32_bf16 v[100:103], v[72:75], v[48:51], v[100:103]\n"
        "ds_read_b128 v[72:75], v3 offset:46080\n"
        "v_add3_u32 v6, v6, v50, v51\n"
        "v_mfma_f32_16x16x32_bf16 v[104:107], v[76:79], v[48:51], v[104:107]\n"
        "ds_read_b128 v[76:79], v3 offset:47104\n"
        "s_waitcnt vmcnt(9) lgkmcnt(5)\n"
        "v_mfma_f32_16x16x32_bf16 v[108:111], v[80:83], v[48:51], v[108:111]\n"
        "ds_read_b128 v[80:83], v3 offset:48128\n"
        "v_mfma_f32_16x16x32_bf16 v[112:115], v[84:87], v[48:51], v[112:115]\n"
        "ds_read_b128 v[84:87], v3 offset:49152\n"
        "s_waitcnt lgkmcnt(5)\n"
        "v_mfma_f32_16x16x32_bf16 v[88:91], v[60:63], v[52:55], v[88:91]\n"
        "ds_read_b128 v[60:63], v3 offset:50176\n"
        "v_add3_u32 v6, v6, v52, v53\n"
        "v_mfma_f32_16x16x32_bf16 v[92:95], v[64:67], v[52:55], v[92:95]\n"
        "ds_read_b128 v[64:67], v3 offset:51200\n"
        "s_waitcnt lgkmcnt(5)\n"
        "v_mfma_f32_16x16x32_bf16 v[96:99], v[68:71], v[52:55], v[96:99]\n"
        "ds_read_b128 v[68:71], v3 offset:52224\n"
        "v_add3_u32 v6, v6, v54, v55\n"
        "v_mfma_f32_16x16x32_bf16 v[100:103], v[72:75], v[52:55], v[100:103]\n"
        "ds_read_b128 v[72:75], v3 offset:53248\n"
        "s_waitcnt lgkmcnt(5)\n"
        "v_mfma_f32_16x16x32_bf16 v[104:107], v[76:79], v[52:55], v[104:107]\n"
        "ds_read_b128 v[76:79], v3 offset:54272\n"
        "v_mfma_f32_16x16x32_bf16 v[108:111], v[80:83], v[52:55], v[108:111]\n"
        "ds_read_b128 v[80:83], v3 offset:55296\n"
        "s_waitcnt vmcnt(8) lgkmcnt(5)\n"
        "v_mfma_f32_16x16x32_bf16 v[112:115], v[84:87], v[52:55], v[112:115]\n"
        "ds_read_b128 v[84:87], v3 offset:56320\n"
        "v_mfma_f32_16x16x32_bf16 v[88:91], v[60:63], v[56:59], v[88:91]\n"
        "ds_read_b128 v[60:63], v3 offset:57344\n"
        "s_waitcnt lgkmcnt(5)\n"
        "v_mfma_f32_16x16x32_bf16 v[92:95], v[64:67], v[56:59], v[92:95]\n"
        "ds_read_b128 v[64:67], v3 offset:58368\n"
        "v_add3_u32 v6, v6, v56, v57\n"
        "v_mfma_f32_16x16x32_bf16 v[96:99], v[68:71], v[56:59], v[96:99]\n"
        "ds_read_b128 v[68:71], v3 offset:59392\n"
        "s_waitcnt lgkmcnt(5)\n"
        "v_mfma_f32_16x16x32_bf16 v[100:103], v[72:75], v[56:59], v[100:103]\n"
        "ds_read_b128 v[72:75], v3 offset:60416\n"
        "v_add3_u32 v6, v6, v58, v59\n"
        "v_mfma_f32_16x16x32_bf16 v[104:107], v[76:79], v[56:59], v[104:107]\n"
        "ds_read_b128 v[76:79], v3 offset:61440\n"
        "s_waitcnt vmcnt(7) lgkmcnt(5)\n"
        "v_mfma_f32_16x16x32_bf16 v[108:111], v[80:83], v[56:59], v[108:111]\n"
        "ds_read_b128 v[80:83], v3 offset:62464\n"
        "v_mfma_f32_16x16x32_bf16 v[112:115], v[84:87], v[56:59], v[112:115]\n"
        "ds_read_b128 v[84:87], v3 offset:63488\n"
        "s_waitcnt lgkmcnt(5)\n"
        "v_mfma_f32_16x16x32_bf16 v[88:91], v[60:63], v[12:15], v[88:91]\n"
        "ds_read_b128 v[60:63], v5\n"
        "v_add3_u32 v6, v6, v12, v13\n"
        "v_mfma_f32_16x16x32_bf16 v[92:95], v[64:67], v[12:15], v[92:95]\n"
        "ds_read_b128 v[64:67], v5 offset:1024\n"
        "s_waitcnt lgkmcnt(5)\n"
        "v_mfma_f32_16x16x32_bf16 v[96:99], v[68:71], v[12:15], v[96:99]\n"
        "ds_read_b128 v[68:71], v5 offset:2048\n"
        "v_add3_u32 v6, v6, v14, v15\n"
        "v_mfma_f32_16x16x32_bf16 v[100:103], v[72:75], v[12:15], v[100:103]\n"
        "ds_read_b128 v[72:75], v5 offset:3072\n"
        "s_waitcnt lgkmcnt(5)\n"
        "v_mfma_f32_16x16x32_bf16 v[104:107], v[76:79], v[12:15], v[104:107]\n"
        "ds_read_b128 v[76:79], v5 offset:4096\n"
        "v_mfma_f32_16x16x32_bf16 v[108:111], v[80:83], v[12:15], v[108:111]\n"
        "ds_read_b128 v[80:83], v5 offset:5120\n"
        "s_waitcnt vmcnt(6) lgkmcnt(5)\n"
        "v_mfma_f32_16x16x32_bf16 v[112:115], v[84:87], v[12:15], v[112:115]\n"
        "ds_read_b128 v[84:87], v5 offset:6144\n"
        "v_mfma_f32_16x16x32_bf16 v[88:91], v[60:63], v[16:19], v[88:91]\n"
        "ds_read_b128 v[60:63], v5 offset:7168\n"
        "s_waitcnt lgkmcnt(5)\n"
        "v_mfma_f32_16x16x32_bf16 v[92:95], v[64:67], v[16:19], v[92:95]\n"
        "ds_read_b128 v[64:67], v5 offset:8192\n"
        "s_add_u32 s62, s61, 0x5000\n"
        "buffer_load_dwordx4 v[200:203], v3, s[12:15], s62 offen offset:1024 sc1\n"
        "v_mfma_f32_16x16x32_bf16 v[96:99], v[68:71], v[16:19], v[96:99]\n"
        "ds_read_b128 v[68:71], v5 offset:9216\n"
        "s_waitcnt lgkmcnt(5)\n"
        "v_mfma_f32_16x16x32_bf16 v[100:103], v[72:75], v[16:19], v[100:103]\n"
        "ds_read_b128 v[72:75], v5 offset:10240\n"
        "v_add3_u32 v6, v6, v16, v17\n"
        "v_mfma_f32_16x16x32_bf16 v[104:107], v[76:79], v[16:19], v[104:107]\n"
        "ds_read_b128 v[76:79], v5 offset:11264\n"
        "s_waitcnt vmcnt(6) lgkmcnt(5)\n"
        "v_mfma_f32_16x16x32_bf16 v[108:111], v[80:83], v[16:19], v[108:111]\n"
        "ds_read_b128 v[80:83], v5 offset:12288\n"
        "buffer_load_dwordx4 v[204:207], v3, s[12:15], s62 offen offset:2048 sc1\n"
        "v_mfma_f32_16x16x32_bf16 v[112:115], v[84:87], v[16:19], v[112:115]\n"
        "ds_read_b128 v[84:87], v5 offset:13312\n"
        "v_add3_u32 v6, v6, v18, v19\n"
        "s_waitcnt lgkmcnt(5)\n"
        "v_mfma_f32_16x16x32_bf16 v[88:91], v[60:63], v[20:23], v[88:91]\n"
        "ds_read_b128 v[60:63], v5 offset:14336\n"
        "buffer_load_dwordx4 v[208:211], v3, s[12:15], s62 offen offset:3072 sc1\n"
        "v_mfma_f32_16x16x32_bf16 v[92:95], v[64:67], v[20:23], v[92:95]\n"
        "ds_read_b128 v[64:67], v5 offset:15360\n"
        "s_waitcnt lgkmcnt(5)\n"
        "v_mfma_f32_16x16x32_bf16 v[96:99], v[68:71], v[20:23], v[96:99]\n"
        "ds_read_b128 v[68:71], v5 offset:16384\n"
        "v_add3_u32 v6, v6, v20, v21\n"
        "v_mfma_f32_16x16x32_bf16 v[100:103], v[72:75], v[20:23], v[100:103]\n"
        "ds_read_b128 v[72:75], v5 offset:17408\n"
        "s_waitcnt lgkmcnt(5)\n"
        "v_mfma_f32_16x16x32_bf16 v[104:107], v[76:79], v[20:23], v[104:107]\n"
        "ds_read_b128 v[76:79], v5 offset:18432\n"
        "s_add_u32 s62, s61, 0x6000\n"
        "buffer_load_dwordx4 v[212:215], v3, s[12:15], s62 offen sc1\n"
        "v_mfma_f32_16x16x32_bf16 v[108:111], v[80:83], v[20:23], v[108:111]\n"
        "ds_read_b128 v[80:83], v5 offset:19456\n"
        "s_waitcnt vmcnt(8) lgkmcnt(5)\n"
        "v_mfma_f32_16x16x32_bf16 v[112:115], v[84:87], v[20:23], v[112:115]\n"
        "ds_read_b128 v[84:87], v5 offset:20480\n"
        "v_add3_u32 v6, v6, v22, v23\n"
        "v_mfma_f32_16x16x32_bf16 v[88:91], v[60:63], v[24:27], v[88:91]\n"
        "ds_read_b128 v[60:63], v5 offset:21504\n"
        "s_waitcnt lgkmcnt(5)\n"
        "v_mfma_f32_16x16x32_bf16 v[92:95], v[64:67], v[24:27], v[92:95]\n"
        "ds_read_b128 v[64:67], v5 offset:22528\n"
        "buffer_load_dwordx4 v[216:219], v3, s[12:15], s62 offen offset:1024 sc1\n"
        "v_mfma_f32_16x16x32_bf16 v[96:99], v[68:71], v[24:27], v[96:99]\n"
        "ds_read_b128 v[68:71], v5 offset:23552\n"
        "s_waitcnt lgkmcnt(5)\n"
        "v_mfma_f32_16x16x32_bf16 v[100:103], v[72:75], v[24:27], v[100:103]\n"
        "ds_read_b128 v[72:75], v5 offset:24576\n"
        "v_add3_u32 v6, v6, v24, v25\n"
        "v_mfma_f32_16x16x32_bf16 v[104:107], v[76:79], v[24:27], v[104:107]\n"
        "ds_read_b128 v[76:79], v5 offset:25600\n"
        "s_waitcnt vmcnt(8) lgkmcnt(5)\n"
        "v_mfma_f32_16x16x32_bf16 v[108:111], v[80:83], v[24:27], v[108:111]\n"
        "ds_read_b128 v[80:83], v5 offset:26624\n"
        "buffer_load_dwordx4 v[220:223], v3, s[12:15], s62 offen offset:2048 sc1\n"
        "v_mfma_f32_16x16x32_bf16 v[112:115], v[84:87], v[24:27], v[112:115]\n"
        "ds_read_b128 v[84:87], v5 offset:27648\n"
        "v_add3_u32 v6, v6, v26, v27\n"
        "s_waitcnt lgkmcnt(5)\n"
        "v_mfma_f32_16x16x32_bf16 v[88:91], v[60:63], v[28:31], v[88:91]\n"
        "ds_read_b128 v[60:63], v5 offset:28672\n"
        "v_add3_u32 v6, v6, v28, v29\n"
        "v_mfma_f32_16x16x32_bf16 v[92:95], v[64:67], v[28:31], v[92:95]\n"
        "ds_read_b128 v[64:67], v5 offset:29696\n"
        "s_waitcnt lgkmcnt(5)\n"
        "v_mfma_f32_16x16x32_bf16 v[96:99], v[68:71], v[28:31], v[96:99]\n"
        "ds_read_b128 v[68:71], v5 offset:30720\n"
        "v_add3_u32 v6, v6, v30, v31\n"
        "v_mfma_f32_16x16x32_bf16 v[100:103], v[72:75], v[28:31], v[100:103]\n"
        "ds_read_b128 v[72:75], v5 offset:31744\n"
        "s_waitcnt lgkmcnt(5)\n"
        "v_mfma_f32_16x16x32_bf16 v[104:107], v[76:79], v[28:31], v[104:107]\n"
        "ds_read_b128 v[76:79], v5 offset:32768\n"
        "v_mfma_f32_16x16x32_bf16 v[108:111], v[80:83], v[28:31], v[108:111]\n"
        "ds_read_b128 v[80:83], v5 offset:33792\n"
        "s_waitcnt vmcnt(8) lgkmcnt(5)\n"
        "v_mfma_f32_16x16x32_bf16 v[112:115], v[84:87], v[28:31], v[112:115]\n"
        "ds_read_b128 v[84:87], v5 offset:34816\n"
        "v_mfma_f32_16x16x32_bf16 v[88:91], v[60:63], v[32:35], v[88:91]\n"
        "ds_read_b128 v[60:63], v5 offset:35840\n"
        "s_waitcnt lgkmcnt(5)\n"
        "v_mfma_f32_16x16x32_bf16 v[92:95], v[64:67], v[32:35], v[92:95]\n"
        "ds_read_b128 v[64:67], v5 offset:36864\n"
        "v_add3_u32 v6, v6, v32, v33\n"
        "v_mfma_f32_16x16x32_bf16 v[96:99], v[68:71], v[32:35], v[96:99]\n"
        "ds_read_b128 v[68:71], v5 offset:37888\n"
        "s_waitcnt lgkmcnt(5)\n"
        "v_mfma_f32_16x16x32_bf16 v[100:103], v[72:75], v[32:35], v[100:103]\n"
        "ds_read_b128 v[72:75], v5 offset:38912\n"
        "v_add3_u32 v6, v6, v34, v35\n"
        "v_mfma_f32_16x16x32_bf16 v[104:107], v[76:79], v[32:35], v[104:107]\n"
        "ds_read_b128 v[76:79], v5 offset:39936\n"
        "s_waitcnt vmcnt(7) lgkmcnt(5)\n"
        "v_mfma_f32_16x16x32_bf16 v[108:111], v[80:83], v[32:35], v[108:111]\n"
        "ds_read_b128 v[80:83], v5 offset:40960\n"
        "v_mfma_f32_16x16x32_bf16 v[112:115], v[84:87], v[32:35], v[112:115]\n"
        "ds_read_b128 v[84:87], v5 offset:41984\n"
        "s_waitcnt lgkmcnt(5)\n"
        "v_mfma_f32_16x16x32_bf16 v[88:91], v[60:63], v[36:39], v[88:91]\n"
        "ds_read_b128 v[60:63], v5 offset:43008\n"
        "v_add3_u32 v6, v6, v36, v37\n"
        "v_mfma_f32_16x16x32_bf16 v[92:95], v[64:67], v[36:39], v[92:95]\n"
        "ds_read_b128 v[64:67], v5 offset:44032\n"
        "s_waitcnt lgkmcnt(5)\n"
        "v_mfma_f32_16x16x32_bf16 v[96:99], v[68:71], v[36:39], v[96:99]\n"
        "ds_read_b128 v[68:71], v5 offset:45056\n"
        "v_add3_u32 v6, v6, v38, v39\n"
        "v_mfma_f32_16x16x32_bf16 v[100:103], v[72:75], v[36:39], v[100:103]\n"
        "ds_read_b128 v[72:75], v5 offset:46080\n"
        "s_waitcnt lgkmcnt(5)\n"
        "v_mfma_f32_16x16x32_bf16 v[104:107], v[76:79], v[36:39], v[104:107]\n"
        "ds_read_b128 v[76:79], v5 offset:47104\n"
        "v_mfma_f32_16x16x32_bf16 v[108:111], v[80:83], v[36:39], v[108:111]\n"
        "ds_read_b128 v[80:83], v5 offset:48128\n"
        "s_waitcnt vmcnt(6) lgkmcnt(6)\n"
        "v_mfma_f32_16x16x32_bf16 v[112:115], v[84:87], v[36:39], v[112:115]\n"
        "ds_read_b128 v[84:87], v5 offset:49152\n"
        "s_waitcnt lgkmcnt(6)\n"
        "v_mfma_f32_16x16x32_bf16 v[88:91], v[60:63], v[40:43], v[88:91]\n"
        "s_waitcnt lgkmcnt(5)\n"
        "v_mfma_f32_16x16x32_bf16 v[92:95], v[64:67], v[40:43], v[92:95]\n"
        "v_add3_u32 v6, v6, v40, v41\n"
        "s_waitcnt lgkmcnt(4)\n"
        "v_mfma_f32_16x16x32_bf16 v[96:99], v[68:71], v[40:43], v[96:99]\n"
        "s_waitcnt lgkmcnt(3)\n"
        "v_mfma_f32_16x16x32_bf16 v[100:103], v[72:75], v[40:43], v[100:103]\n"
        "v_add3_u32 v6, v6, v42, v43\n"
        "s_waitcnt lgkmcnt(2)\n"
        "v_mfma_f32_16x16x32_bf16 v[104:107], v[76:79], v[40:43], v[104:107]\n"
        "s_waitcnt lgkmcnt(1)\n"
        "v_mfma_f32_16x16x32_bf16 v[108:111], v[80:83], v[40:43], v[108:111]\n"
        "s_waitcnt lgkmcnt(0)\n"
        "v_mfma_f32_16x16x32_bf16 v[112:115], v[84:87], v[40:43], v[112:115]\n"
        "v_cmp_ne_u32_e32 vcc, v6, v7\n"
        "s_cbranch_vccnz .Lk115\n"
        ".Lk116:\n"
        "v_exp_f32_e32 v76, v88\n"
        "v_exp_f32_e32 v77, v89\n"
        "v_exp_f32_e32 v78, v90\n"
        "v_exp_f32_e32 v79, v91\n"
        "v_add_f32_e32 v76, 1.0, v76\n"
        "v_add_f32_e32 v77, 1.0, v77\n"
        "v_add_f32_e32 v78, 1.0, v78\n"
        "v_add_f32_e32 v79, 1.0, v79\n"
        "v_rcp_f32_e32 v76, v76\n"
        "v_rcp_f32_e32 v77, v77\n"
        "v_rcp_f32_e32 v78, v78\n"
        "v_rcp_f32_e32 v79, v79\n"
        "v_mul_f32_e32 v76, v88, v76\n"
        "v_mul_f32_e32 v77, v89, v77\n"
        "v_mul_f32_e32 v78, v90, v78\n"
        "v_mul_f32_e32 v79, v91, v79\n"
        "v_cvt_pk_bf16_f32 v88, v76, v77\n"
        "v_cvt_pk_bf16_f32 v89, v78, v79\n"
        "v_exp_f32_e32 v80, v92\n"
        "v_exp_f32_e32 v81, v93\n"
        "v_exp_f32_e32 v82, v94\n"
        "v_exp_f32_e32 v83, v95\n"
        "v_exp_f32_e32 v84, v96\n"
        "v_exp_f32_e32 v85, v97\n"
        "v_exp_f32_e32 v86, v98\n"
        "v_exp_f32_e32 v87, v99\n"
        "v_exp_f32_e32 v76, v100\n"
        "v_exp_f32_e32 v77, v101\n"
        "v_exp_f32_e32 v78, v102\n"
        "v_exp_f32_e32 v79, v103\n"
        "v_add_f32_e32 v80, 1.0, v80\n"
        "v_add_f32_e32 v81, 1.0, v81\n"
        "v_add_f32_e32 v82, 1.0, v82\n"
        "v_add_f32_e32 v83, 1.0, v83\n"
        "v_rcp_f32_e32 v80, v80\n"
        "v_rcp_f32_e32 v81, v81\n"
        "v_rcp_f32_e32 v82, v82\n"
        "v_rcp_f32_e32 v83, v83\n"
        "v_add_f32_e32 v84, 1.0, v84\n"
        "v_add_f32_e32 v85, 1.0, v85\n"
        "v_add_f32_e32 v86, 1.0, v86\n"
        "v_add_f32_e32 v87, 1.0, v87\n"
        "v_add_f32_e32 v76, 1.0, v76\n"
        "v_add_f32_e32 v77, 1.0, v77\n"
        "v_add_f32_e32 v78, 1.0, v78\n"
        "v_add_f32_e32 v79, 1.0, v79\n"
        "v_rcp_f32_e32 v84, v84\n"
        "v_rcp_f32_e32 v85, v85\n"
        "v_rcp_f32_e32 v86, v86\n"
        "v_rcp_f32_e32 v87, v87\n"
        "v_rcp_f32_e32 v76, v76\n"
        "v_rcp_f32_e32 v77, v77\n"
        "v_rcp_f32_e32 v78, v78\n"
        "v_rcp_f32_e32 v79, v79\n"
        "v_mul_f32_e32 v80, v92, v80\n"
        "v_mul_f32_e32 v81, v93, v81\n"
        "v_mul_f32_e32 v82, v94, v82\n"
        "v_mul_f32_e32 v83, v95, v83\n"
        "v_cvt_pk_bf16_f32 v90, v80, v81\n"
        "v_cvt_pk_bf16_f32 v91, v82, v83\n"
        "v_mul_f32_e32 v84, v96, v84\n"
        "v_mul_f32_e32 v85, v97, v85\n"
        "v_mul_f32_e32 v86, v98, v86\n"
        "v_mul_f32_e32 v87, v99, v87\n"
        "v_mul_f32_e32 v76, v100, v76\n"
        "v_mul_f32_e32 v77, v101, v77\n"
        "v_mul_f32_e32 v78, v102, v78\n"
        "v_mul_f32_e32 v79, v103, v79\n"
        "v_cvt_pk_bf16_f32 v96, v84, v85\n"
        "v_cvt_pk_bf16_f32 v97, v86, v87\n"
        "v_cvt_pk_bf16_f32 v98, v76, v77\n"
        "v_cvt_pk_bf16_f32 v99, v78, v79\n"
        "v_exp_f32_e32 v80, v104\n"
        "v_exp_f32_e32 v81, v105\n"
        "v_exp_f32_e32 v82, v106\n"
        "v_exp_f32_e32 v83, v107\n"
        "v_exp_f32_e32 v84, v108\n"
        "v_exp_f32_e32 v85, v109\n"
        "v_exp_f32_e32 v86, v110\n"
        "v_exp_f32_e32 v87, v111\n"
        "v_exp_f32_e32 v76, v112\n"
        "v_exp_f32_e32 v77, v113\n"
        "v_exp_f32_e32 v78, v114\n"
        "v_exp_f32_e32 v79, v115\n"
        "v_add_f32_e32 v80, 1.0, v80\n"
        "v_add_f32_e32 v81, 1.0, v81\n"
        "v_add_f32_e32 v82, 1.0, v82\n"
        "v_add_f32_e32 v83, 1.0, v83\n"
        "v_rcp_f32_e32 v80, v80\n"
        "v_rcp_f32_e32 v81, v81\n"
        "v_rcp_f32_e32 v82, v82\n"
        "v_rcp_f32_e32 v83, v83\n"
        "v_add_f32_e32 v84, 1.0, v84\n"
        "v_add_f32_e32 v85, 1.0, v85\n"
        "v_add_f32_e32 v86, 1.0, v86\n"
        "v_add_f32_e32 v87, 1.0, v87\n"
        "v_add_f32_e32 v76, 1.0, v76\n"
        "v_add_f32_e32 v77, 1.0, v77\n"
        "v_add_f32_e32 v78, 1.0, v78\n"
        "v_add_f32_e32 v79, 1.0, v79\n"
        "v_add3_u32 v6, v88, v89, s58\n"
        "v_rcp_f32_e32 v84, v84\n"
        "v_rcp_f32_e32 v85, v85\n"
        "v_rcp_f32_e32 v86, v86\n"
        "v_rcp_f32_e32 v87, v87\n"
        "v_rcp_f32_e32 v76, v76\n"
        "v_rcp_f32_e32 v77, v77\n"
        "v_rcp_f32_e32 v78, v78\n"
        "v_rcp_f32_e32 v79, v79\n"
        "v_add3_u32 v6, v90, v91, v6\n"
        "v_add3_u32 v6, v96, v97, v6\n"
        "v_mul_f32_e32 v80, v104, v80\n"
        "v_mul_f32_e32 v81, v105, v81\n"
        "v_mul_f32_e32 v82, v106, v82\n"
        "v_mul_f32_e32 v83, v107, v83\n"
        "v_add3_u32 v6, v98, v99, v6\n"
        "v_cvt_pk_bf16_f32 v104, v80, v81\n"
        "v_cvt_pk_bf16_f32 v105, v82, v83\n"
        "v_mul_f32_e32 v84, v108, v84\n"
        "v_mul_f32_e32 v85, v109, v85\n"
        "v_mul_f32_e32 v86, v110, v86\n"
        "v_mul_f32_e32 v87, v111, v87\n"
        "v_mul_f32_e32 v76, v112, v76\n"
        "v_mul_f32_e32 v77, v113, v77\n"
        "v_mul_f32_e32 v78, v114, v78\n"
        "v_mul_f32_e32 v79, v115, v79\n"
        "buffer_store_dwordx4 v[88:91], v3, s[12:15], s46 offen sc1\n"
        "v_add3_u32 v6, v104, v105, v6\n"
        "v_cvt_pk_bf16_f32 v106, v84, v85\n"
        "v_cvt_pk_bf16_f32 v107, v86, v87\n"
        "v_cvt_pk_bf16_f32 v112, v76, v77\n"
        "v_cvt_pk_bf16_f32 v113, v78, v79\n"
        "buffer_store_dwordx4 v[96:99], v3, s[12:15], s46 offen offset:1024 sc1\n"
        "v_add3_u32 v6, v106, v107, v6\n"
        "v_and_b32_e32 v112, s41, v112\n"
        "v_and_b32_e32 v113, s41, v113\n"
        "buffer_store_dwordx4 v[104:107], v3, s[12:15], s46 offen offset:2048 sc1\n"
        "v_add3_u32 v114, v112, v113, v6\n"
        "buffer_store_dwordx3 v[112:114], v4, s[12:15], s46 offen offset:3072 sc1\n"
        "s_barrier\n"
        "s_setprio 0\n"
        "buffer_load_dwordx4 v[224:227], v3, s[12:15], s62 offen offset:3072 sc1\n"
        "s_waitcnt vmcnt(7)\n"
        "s_add_u32 s62, s61, 0x7000\n"
        "buffer_load_dwordx4 v[228:231], v3, s[12:15], s62 offen sc1\n"
        "s_waitcnt vmcnt(7)\n"
        "buffer_load_dwordx4 v[232:235], v3, s[12:15], s62 offen offset:1024 sc1\n"
        "s_waitcnt vmcnt(7)\n"
        "buffer_load_dwordx4 v[236:239], v3, s[12:15], s62 offen offset:2048 sc1\n"
        "s_waitcnt vmcnt(7)\n"
        "buffer_load_dwordx4 v[240:243], v3, s[12:15], s62 offen offset:3072 sc1\n"
        "s_waitcnt vmcnt(7)\n"
        "s_add_u32 s62, s61, 0x8000\n"
        "buffer_load_dwordx4 v[244:247], v3, s[12:15], s62 offen sc1\n"
        "s_waitcnt vmcnt(7)\n"
        "buffer_load_dwordx4 v[248:251], v3, s[12:15], s62 offen offset:1024 sc1\n"
        "s_waitcnt vmcnt(7)\n"
        "buffer_load_dwordx4 v[252:255], v3, s[12:15], s62 offen offset:2048 sc1\n"
        "s_waitcnt vmcnt(7)\n"
        "s_branch .Lk117\n"
        ".Lk110:\n"
        "s_add_u32 s62, s61, 0x2000\n"
        "buffer_load_dwordx4 v[160:163], v3, s[12:15], s62 offen offset:3072 sc1\n"
        "s_waitcnt vmcnt(2)\n"
        "s_add_u32 s62, s61, 0x3000\n"
        "buffer_load_dwordx4 v[164:167], v3, s[12:15], s62 offen sc1\n"
        "s_waitcnt vmcnt(2)\n"
        "buffer_load_dwordx4 v[168:171], v3, s[12:15], s62 offen offset:1024 sc1\n"
        "s_waitcnt vmcnt(2)\n"
        "buffer_load_dwordx4 v[172:175], v3, s[12:15], s62 offen offset:2048 sc1\n"
        "s_waitcnt vmcnt(2)\n"
        "buffer_load_dwordx4 v[176:179], v3, s[12:15], s62 offen offset:3072 sc1\n"
        "s_waitcnt vmcnt(2)\n"
        "s_add_u32 s62, s61, 0x4000\n"
        "buffer_load_dwordx4 v[180:183], v3, s[12:15], s62 offen sc1\n"
        "s_waitcnt vmcnt(2)\n"
        "v_and_b32_e32 v108, 0xf0, v3\n"
        "buffer_load_dwordx3 v[112:114], v108, s[12:15], s63 offen offset:1024 sc1\n"
        "buffer_load_dwordx4 v[8:11], v3, s[12:15], s63 offen sc1\n"
        "buffer_load_dwordx4 v[12:15], v3, s[12:15], s57 offen sc1\n"
        "s_waitcnt vmcnt(7)\n"
        "buffer_load_dwordx4 v[16:19], v3, s[12:15], s57 offen offset:1024 sc1\n"
        "s_waitcnt vmcnt(7)\n"
        "buffer_load_dwordx4 v[20:23], v3, s[12:15], s57 offen offset:2048 sc1\n"
        "s_waitcnt vmcnt(7)\n"
        "buffer_load_dwordx4 v[24:27], v3, s[12:15], s57 offen offset:3072 sc1\n"
        "s_waitcnt vmcnt(7)\n"
        "buffer_load_dwordx4 v[28:31], v3, s[12:15], s49 offen sc1\n"
        "s_waitcnt vmcnt(7)\n"
        "buffer_load_dwordx4 v[32:35], v3, s[12:15], s49 offen offset:1024 sc1\n"
        "s_waitcnt vmcnt(7)\n"
        "buffer_load_dwordx4 v[36:39], v3, s[12:15], s49 offen offset:2048 sc1\n"
        "s_waitcnt vmcnt(7)\n"
        "buffer_load_dwordx4 v[40:43], v3, s[12:15], s49 offen offset:3072 sc1\n"
        "s_waitcnt vmcnt(7)\n"
        "buffer_load_dwordx4 v[44:47], v3, s[12:15], s59 offen sc1\n"
        "s_waitcnt vmcnt(7)\n"
        "buffer_load_dwordx4 v[48:51], v3, s[12:15], s59 offen offset:1024 sc1\n"
        "s_waitcnt vmcnt(7)\n"
        "buffer_load_dwordx4 v[52:55], v3, s[12:15], s59 offen offset:2048 sc1\n"
        "s_waitcnt vmcnt(7)\n"
        "buffer_load_dwordx4 v[56:59], v3, s[12:15], s59 offen offset:3072 sc1\n"
        "s_waitcnt vmcnt(7)\n"
        "buffer_load_dwordx4 v[184:187], v3, s[12:15], s62 offen offset:1024 sc1\n"
        "s_waitcnt vmcnt(7)\n"
        "buffer_load_dwordx4 v[188:191], v3, s[12:15], s62 offen offset:2048 sc1\n"
        "s_waitcnt vmcnt(7)\n"
        "buffer_load_dwordx4 v[192:195], v3, s[12:15], s62 offen offset:3072 sc1\n"
        "s_waitcnt vmcnt(7)\n"
        "s_add_u32 s62, s61, 0x5000\n"
        "buffer_load_dwordx4 v[196:199], v3, s[12:15], s62 offen sc1\n"
        "s_waitcnt vmcnt(7)\n"
        "buffer_load_dwordx4 v[200:203], v3, s[12:15], s62 offen offset:1024 sc1\n"
        "s_waitcnt vmcnt(7)\n"
        "buffer_load_dwordx4 v[204:207], v3, s[12:15], s62 offen offset:2048 sc1\n"
        "s_waitcnt vmcnt(7)\n"
        "buffer_load_dwordx4 v[208:211], v3, s[12:15], s62 offen offset:3072 sc1\n"
        "s_waitcnt vmcnt(7)\n"
        "s_add_u32 s62, s61, 0x6000\n"
        "buffer_load_dwordx4 v[212:215], v3, s[12:15], s62 offen sc1\n"
        "s_waitcnt vmcnt(7)\n"
        "ds_read_b128 v[60:63], v5 offset:50176\n"
        "ds_read_b128 v[64:67], v5 offset:51200\n"
        "ds_read_b128 v[68:71], v5 offset:52224\n"
        "ds_read_b128 v[72:75], v5 offset:53248\n"
        "ds_read_b128 v[76:79], v5 offset:54272\n"
        "ds_read_b128 v[80:83], v5 offset:55296\n"
        "ds_read_b128 v[84:87], v5 offset:56320\n"
        "v_add3_u32 v6, v112, v113, s58\n"
        "v_cmp_ne_u32_e32 vcc, v6, v114\n"
        "v_add3_u32 v7, v8, v9, v10\n"
        "s_lshl_b32 s42, s58, 2\n"
        "v_add_u32_e32 v7, v7, v11\n"
        "v_mov_b32_e32 v6, s42\n"
        "s_cbranch_vccnz .Lk118\n"
        ".Lk119:\n"
        "s_cmp_eq_u32 s52, 0\n"
        "s_cbranch_scc1 .Lk122\n"
        ".Lk123:\n"
        "s_setprio 1\n"
        "s_waitcnt lgkmcnt(6)\n"
        "v_mfma_f32_16x16x32_bf16 v[88:91], v[60:63], v[12:15], 0\n"
        "ds_read_b128 v[60:63], v5 offset:57344\n"
        "v_add3_u32 v6, v6, v12, v13\n"
        "s_waitcnt lgkmcnt(6)\n"
        "v_mfma_f32_16x16x32_bf16 v[92:95], v[64:67], v[12:15], 0\n"
        "ds_read_b128 v[64:67], v5 offset:58368\n"
        "s_waitcnt lgkmcnt(5)\n"
        "v_mfma_f32_16x16x32_bf16 v[96:99], v[68:71], v[12:15], 0\n"
        "ds_read_b128 v[68:71], v5 offset:59392\n"
        "v_add3_u32 v6, v6, v14, v15\n"
        "v_mfma_f32_16x16x32_bf16 v[100:103], v[72:75], v[12:15], 0\n"
        "ds_read_b128 v[72:75], v5 offset:60416\n"
        "s_waitcnt lgkmcnt(5)\n"
        "v_mfma_f32_16x16x32_bf16 v[104:107], v[76:79], v[12:15], 0\n"
        "ds_read_b128 v[76:79], v5 offset:61440\n"
        "v_mfma_f32_16x16x32_bf16 v[108:111], v[80:83], v[12:15], 0\n"
        "ds_read_b128 v[80:83], v5 offset:62464\n"
        "s_waitcnt lgkmcnt(5)\n"
        "v_mfma_f32_16x16x32_bf16 v[112:115], v[84:87], v[12:15], 0\n"
        "ds_read_b128 v[84:87], v5 offset:63488\n"
        "v_mfma_f32_16x16x32_bf16 v[88:91], v[60:63], v[16:19], v[88:91]\n"
        "ds_read_b128 v[60:63], v1\n"
        "s_waitcnt lgkmcnt(5)\n"
        "v_mfma_f32_16x16x32_bf16 v[92:95], v[64:67], v[16:19], v[92:95]\n"
        "ds_read_b128 v[64:67], v1 offset:1024\n"
        "buffer_load_dwordx4 v[12:15], v3, s[12:15], s56 offen sc1\n"
        "v_mfma_f32_16x16x32_bf16 v[96:99], v[68:71], v[16:19], v[96:99]\n"
        "ds_read_b128 v[68:71], v1 offset:2048\n"
        "s_waitcnt lgkmcnt(5)\n"
        "v_mfma_f32_16x16x32_bf16 v[100:103], v[72:75], v[16:19], v[100:103]\n"
        "ds_read_b128 v[72:75], v1 offset:3072\n"
        "v_add3_u32 v6, v6, v16, v17\n"
        "v_mfma_f32_16x16x32_bf16 v[104:107], v[76:79], v[16:19], v[104:107]\n"
        "ds_read_b128 v[76:79], v1 offset:4096\n"
        "s_waitcnt lgkmcnt(5)\n"
        "v_mfma_f32_16x16x32_bf16 v[108:111], v[80:83], v[16:19], v[108:111]\n"
        "ds_read_b128 v[80:83], v1 offset:5120\n"
        "v_add3_u32 v6, v6, v18, v19\n"
        "v_mfma_f32_16x16x32_bf16 v[112:115], v[84:87], v[16:19], v[112:115]\n"
        "ds_read_b128 v[84:87], v1 offset:6144\n"
        "s_waitcnt lgkmcnt(5)\n"
        "v_mfma_f32_16x16x32_bf16 v[88:91], v[60:63], v[20:23], v[88:91]\n"
        "ds_read_b128 v[60:63], v1 offset:7168\n"
        "buffer_load_dwordx4 v[16:19], v3, s[12:15], s56 offen offset:1024 sc1\n"
        "v_mfma_f32_16x16x32_bf16 v[92:95], v[64:67], v[20:23], v[92:95]\n"
        "ds_read_b128 v[64:67], v1 offset:8192\n"
        "s_waitcnt lgkmcnt(5)\n"
        "v_mfma_f32_16x16x32_bf16 v[96:99], v[68:71], v[20:23], v[96:99]\n"
        "ds_read_b128 v[68:71], v1 offset:9216\n"
        "v_add3_u32 v6, v6, v20, v21\n"
        "v_mfma_f32_16x16x32_bf16 v[100:103], v[72:75], v[20:23], v[100:103]\n"
        "ds_read_b128 v[72:75], v1 offset:10240\n"
        "s_waitcnt lgkmcnt(5)\n"
        "v_mfma_f32_16x16x32_bf16 v[104:107], v[76:79], v[20:23], v[104:107]\n"
        "ds_read_b128 v[76:79], v1 offset:11264\n"
        "v_add3_u32 v6, v6, v22, v23\n"
        "v_mfma_f32_16x16x32_bf16 v[108:111], v[80:83], v[20:23], v[108:111]\n"
        "ds_read_b128 v[80:83], v1 offset:12288\n"
        "s_waitcnt lgkmcnt(5)\n"
        "v_mfma_f32_16x16x32_bf16 v[112:115], v[84:87], v[20:23], v[112:115]\n"
        "ds_read_b128 v[84:87], v1 offset:13312\n"
        "v_mfma_f32_16x16x32_bf16 v[88:91], v[60:63], v[24:27], v[88:91]\n"
        "ds_read_b128 v[60:63], v3\n"
        "s_waitcnt lgkmcnt(5)\n"
        "v_mfma_f32_16x16x32_bf16 v[92:95], v[64:67], v[24:27], v[92:95]\n"
        "ds_read_b128 v[64:67], v3 offset:1024\n"
        "buffer_load_dwordx4 v[20:23], v3, s[12:15], s56 offen offset:2048 sc1\n"
        "v_mfma_f32_16x16x32_bf16 v[96:99], v[68:71], v[24:27], v[96:99]\n"
        "ds_read_b128 v[68:71], v3 offset:2048\n"
        "s_waitcnt lgkmcnt(5)\n"
        "v_mfma_f32_16x16x32_bf16 v[100:103], v[72:75], v[24:27], v[100:103]\n"
        "ds_read_b128 v[72:75], v3 offset:3072\n"
        "v_add3_u32 v6, v6, v24, v25\n"
        "v_mfma_f32_16x16x32_bf16 v[104:107], v[76:79], v[24:27], v[104:107]\n"
        "ds_read_b128 v[76:79], v3 offset:4096\n"
        "s_waitcnt lgkmcnt(5)\n"
        "v_mfma_f32_16x16x32_bf16 v[108:111], v[80:83], v[24:27], v[108:111]\n"
        "ds_read_b128 v[80:83], v3 offset:5120\n"
        "v_add3_u32 v6, v6, v26, v27\n"
        "v_mfma_f32_16x16x32_bf16 v[112:115], v[84:87], v[24:27], v[112:115]\n"
        "ds_read_b128 v[84:87], v3 offset:6144\n"
        "s_waitcnt lgkmcnt(5)\n"
        "v_mfma_f32_16x16x32_bf16 v[88:91], v[60:63], v[28:31], v[88:91]\n"
        "ds_read_b128 v[60:63], v3 offset:7168\n"
        "buffer_load_dwordx4 v[24:27], v3, s[12:15], s56 offen offset:3072 sc1\n"
        "v_mfma_f32_16x16x32_bf16 v[92:95], v[64:67], v[28:31], v[92:95]\n"
        "ds_read_b128 v[64:67], v3 offset:8192\n"
        "s_waitcnt lgkmcnt(5)\n"
        "v_mfma_f32_16x16x32_bf16 v[96:99], v[68:71], v[28:31], v[96:99]\n"
        "ds_read_b128 v[68:71], v3 offset:9216\n"
        "v_add3_u32 v6, v6, v28, v29\n"
        "v_mfma_f32_16x16x32_bf16 v[100:103], v[72:75], v[28:31], v[100:103]\n"
        "ds_read_b128 v[72:75], v3 offset:10240\n"
        "s_waitcnt lgkmcnt(5)\n"
        "v_mfma_f32_16x16x32_bf16 v[104:107], v[76:79], v[28:31], v[104:107]\n"
        "ds_read_b128 v[76:79], v3 offset:11264\n"
        "v_add3_u32 v6, v6, v30, v31\n"
        "v_mfma_f32_16x16x32_bf16 v[108:111], v[80:83], v[28:31], v[108:111]\n"
        "ds_read_b128 v[80:83], v3 offset:12288\n"
        "s_waitcnt lgkmcnt(5)\n"
        "v_mfma_f32_16x16x32_bf16 v[112:115], v[84:87], v[28:31], v[112:115]\n"
        "ds_read_b128 v[84:87], v3 offset:13312\n"
        "v_mfma_f32_16x16x32_bf16 v[88:91], v[60:63], v[32:35], v[88:91]\n"
        "ds_read_b128 v[60:63], v3 offset:14336\n"
        "s_waitcnt lgkmcnt(5)\n"
        "v_mfma_f32_16x16x32_bf16 v[92:95], v[64:67], v[32:35], v[92:95]\n"
        "ds_read_b128 v[64:67], v3 offset:15360\n"
        "buffer_load_dwordx4 v[28:31], v3, s[12:15], s60 offen sc1\n"
        "v_mfma_f32_16x16x32_bf16 v[96:99], v[68:71], v[32:35], v[96:99]\n"
        "ds_read_b128 v[68:71], v3 offset:16384\n"
        "s_waitcnt lgkmcnt(5)\n"
        "v_mfma_f32_16x16x32_bf16 v[100:103], v[72:75], v[32:35], v[100:103]\n"
        "ds_read_b128 v[72:75], v3 offset:17408\n"
        "v_add3_u32 v6, v6, v32, v33\n"
        "v_mfma_f32_16x16x32_bf16 v[104:107], v[76:79], v[32:35], v[104:107]\n"
        "ds_read_b128 v[76:79], v3 offset:18432\n"
        "s_waitcnt lgkmcnt(5)\n"
        "v_mfma_f32_16x16x32_bf16 v[108:111], v[80:83], v[32:35], v[108:111]\n"
        "ds_read_b128 v[80:83], v3 offset:19456\n"
        "v_add3_u32 v6, v6, v34, v35\n"
        "v_mfma_f32_16x16x32_bf16 v[112:115], v[84:87], v[32:35], v[112:115]\n"
        "ds_read_b128 v[84:87], v3 offset:20480\n"
        "s_waitcnt lgkmcnt(5)\n"
        "v_mfma_f32_16x16x32_bf16 v[88:91], v[60:63], v[36:39], v[88:91]\n"
        "ds_read_b128 v[60:63], v3 offset:21504\n"
        "buffer_load_dwordx4 v[32:35], v3, s[12:15], s60 offen offset:1024 sc1\n"
        "v_mfma_f32_16x16x32_bf16 v[92:95], v[64:67], v[36:39], v[92:95]\n"
        "ds_read_b128 v[64:67], v3 offset:22528\n"
        "s_waitcnt lgkmcnt(5)\n"
        "v_mfma_f32_16x16x32_bf16 v[96:99], v[68:71], v[36:39], v[96:99]\n"
        "ds_read_b128 v[68:71], v3 offset:23552\n"
        "v_add3_u32 v6, v6, v36, v37\n"
        "v_mfma_f32_16x16x32_bf16 v[100:103], v[72:75], v[36:39], v[100:103]\n"
        "ds_read_b128 v[72:75], v3 offset:24576\n"
        "s_waitcnt lgkmcnt(5)\n"
        "v_mfma_f32_16x16x32_bf16 v[104:107], v[76:79], v[36:39], v[104:107]\n"
        "ds_read_b128 v[76:79], v3 offset:25600\n"
        "v_add3_u32 v6, v6, v38, v39\n"
        "v_mfma_f32_16x16x32_bf16 v[108:111], v[80:83], v[36:39], v[108:111]\n"
        "ds_read_b128 v[80:83], v3 offset:26624\n"
        "s_waitcnt lgkmcnt(5)\n"
        "v_mfma_f32_16x16x32_bf16 v[112:115], v[84:87], v[36:39], v[112:115]\n"
        "ds_read_b128 v[84:87], v3 offset:27648\n"
        "v_mfma_f32_16x16x32_bf16 v[88:91], v[60:63], v[40:43], v[88:91]\n"
        "ds_read_b128 v[60:63], v3 offset:28672\n"
        "s_waitcnt lgkmcnt(5)\n"
        "v_mfma_f32_16x16x32_bf16 v[92:95], v[64:67], v[40:43], v[92:95]\n"
        "ds_read_b128 v[64:67], v3 offset:29696\n"
        "buffer_load_dwordx4 v[36:39], v3, s[12:15], s60 offen offset:2048 sc1\n"
        "v_mfma_f32_16x16x32_bf16 v[96:99], v[68:71], v[40:43], v[96:99]\n"
        "ds_read_b128 v[68:71], v3 offset:30720\n"
        "s_waitcnt lgkmcnt(5)\n"
        "v_mfma_f32_16x16x32_bf16 v[100:103], v[72:75], v[40:43], v[100:103]\n"
        "ds_read_b128 v[72:75], v3 offset:31744\n"
        "v_add3_u32 v6, v6, v40, v41\n"
        "v_mfma_f32_16x16x32_bf16 v[104:107], v[76:79], v[40:43], v[104:107]\n"
        "ds_read_b128 v[76:79], v3 offset:32768\n"
        "s_waitcnt lgkmcnt(5)\n"
        "v_mfma_f32_16x16x32_bf16 v[108:111], v[80:83], v[40:43], v[108:111]\n"
        "ds_read_b128 v[80:83], v3 offset:33792\n"
        "v_add3_u32 v6, v6, v42, v43\n"
        "v_mfma_f32_16x16x32_bf16 v[112:115], v[84:87], v[40:43], v[112:115]\n"
        "ds_read_b128 v[84:87], v3 offset:34816\n"
        "s_waitcnt lgkmcnt(5)\n"
        "v_mfma_f32_16x16x32_bf16 v[88:91], v[60:63], v[44:47], v[88:91]\n"
        "ds_read_b128 v[60:63], v3 offset:35840\n"
        "buffer_load_dwordx4 v[40:43], v3, s[12:15], s60 offen offset:3072 sc1\n"
        "v_mfma_f32_16x16x32_bf16 v[92:95], v[64:67], v[44:47], v[92:95]\n"
        "ds_read_b128 v[64:67], v3 offset:36864\n"
        "s_waitcnt lgkmcnt(5)\n"
        "v_mfma_f32_16x16x32_bf16 v[96:99], v[68:71], v[44:47], v[96:99]\n"
        "ds_read_b128 v[68:71], v3 offset:37888\n"
        "v_add3_u32 v6, v6, v44, v45\n"
        "v_mfma_f32_16x16x32_bf16 v[100:103], v[72:75], v[44:47], v[100:103]\n"
        "ds_read_b128 v[72:75], v3 offset:38912\n"
        "s_waitcnt lgkmcnt(5)\n"
        "v_mfma_f32_16x16x32_bf16 v[104:107], v[76:79], v[44:47], v[104:107]\n"
        "ds_read_b128 v[76:79], v3 offset:39936\n"
        "v_add3_u32 v6, v6, v46, v47\n"
        "v_mfma_f32_16x16x32_bf16 v[108:111], v[80:83], v[44:47], v[108:111]\n"
        "ds_read_b128 v[80:83], v3 offset:40960\n"
        "s_waitcnt lgkmcnt(5)\n"
        "v_mfma_f32_16x16x32_bf16 v[112:115], v[84:87], v[44:47], v[112:115]\n"
        "ds_read_b128 v[84:87], v3 offset:41984\n"
        "v_mfma_f32_16x16x32_bf16 v[88:91], v[60:63], v[48:51], v[88:91]\n"
        "ds_read_b128 v[60:63], v3 offset:43008\n"
        "s_waitcnt lgkmcnt(5)\n"
        "v_mfma_f32_16x16x32_bf16 v[92:95], v[64:67], v[48:51], v[92:95]\n"
        "ds_read_b128 v[64:67], v3 offset:44032\n"
        "v_add3_u32 v6, v6, v48, v49\n"
        "v_mfma_f32_16x16x32_bf16 v[96:99], v[68:71], v[48:51], v[96:99]\n"
        "ds_read_b128 v[68:71], v3 offset:45056\n"
        "s_waitcnt lgkmcnt(5)\n"
        "v_mfma_f32_16x16x32_bf16 v[100:103], v[72:75], v[48:51], v[100:103]\n"
        "ds_read_b128 v[72:75], v3 offset:46080\n"
        "v_add3_u32 v6, v6, v50, v51\n"
        "v_mfma_f32_16x16x32_bf16 v[104:107], v[76:79], v[48:51], v[104:107]\n"
        "ds_read_b128 v[76:79], v3 offset:47104\n"
        "s_waitcnt lgkmcnt(5)\n"
        "v_mfma_f32_16x16x32_bf16 v[108:111], v[80:83], v[48:51], v[108:111]\n"
        "ds_read_b128 v[80:83], v3 offset:48128\n"
        "v_mfma_f32_16x16x32_bf16 v[112:115], v[84:87], v[48:51], v[112:115]\n"
        "ds_read_b128 v[84:87], v3 offset:49152\n"
        "s_waitcnt lgkmcnt(5)\n"
        "v_mfma_f32_16x16x32_bf16 v[88:91], v[60:63], v[52:55], v[88:91]\n"
        "ds_read_b128 v[60:63], v3 offset:50176\n"
        "v_add3_u32 v6, v6, v52, v53\n"
        "v_mfma_f32_16x16x32_bf16 v[92:95], v[64:67], v[52:55], v[92:95]\n"
        "ds_read_b128 v[64:67], v3 offset:51200\n"
        "s_waitcnt lgkmcnt(5)\n"
        "v_mfma_f32_16x16x32_bf16 v[96:99], v[68:71], v[52:55], v[96:99]\n"
        "ds_read_b128 v[68:71], v3 offset:52224\n"
        "v_add3_u32 v6, v6, v54, v55\n"
        "v_mfma_f32_16x16x32_bf16 v[100:103], v[72:75], v[52:55], v[100:103]\n"
        "ds_read_b128 v[72:75], v3 offset:53248\n"
        "s_waitcnt lgkmcnt(5)\n"
        "v_mfma_f32_16x16x32_bf16 v[104:107], v[76:79], v[52:55], v[104:107]\n"
        "ds_read_b128 v[76:79], v3 offset:54272\n"
        "v_mfma_f32_16x16x32_bf16 v[108:111], v[80:83], v[52:55], v[108:111]\n"
        "ds_read_b128 v[80:83], v3 offset:55296\n"
        "s_waitcnt lgkmcnt(5)\n"
        "v_mfma_f32_16x16x32_bf16 v[112:115], v[84:87], v[52:55], v[112:115]\n"
        "ds_read_b128 v[84:87], v3 offset:56320\n"
        "v_mfma_f32_16x16x32_bf16 v[88:91], v[60:63], v[56:59], v[88:91]\n"
        "ds_read_b128 v[60:63], v3 offset:57344\n"
        "s_waitcnt lgkmcnt(5)\n"
        "v_mfma_f32_16x16x32_bf16 v[92:95], v[64:67], v[56:59], v[92:95]\n"
        "ds_read_b128 v[64:67], v3 offset:58368\n"
        "v_add3_u32 v6, v6, v56, v57\n"
        "v_mfma_f32_16x16x32_bf16 v[96:99], v[68:71], v[56:59], v[96:99]\n"
        "ds_read_b128 v[68:71], v3 offset:59392\n"
        "s_waitcnt lgkmcnt(5)\n"
        "v_mfma_f32_16x16x32_bf16 v[100:103], v[72:75], v[56:59], v[100:103]\n"
        "ds_read_b128 v[72:75], v3 offset:60416\n"
        "v_add3_u32 v6, v6, v58, v59\n"
        "v_mfma_f32_16x16x32_bf16 v[104:107], v[76:79], v[56:59], v[104:107]\n"
        "ds_read_b128 v[76:79], v3 offset:61440\n"
        "s_waitcnt vmcnt(7) lgkmcnt(5)\n"
        "v_mfma_f32_16x16x32_bf16 v[108:111], v[80:83], v[56:59], v[108:111]\n"
        "ds_read_b128 v[80:83], v3 offset:62464\n"
        "v_mfma_f32_16x16x32_bf16 v[112:115], v[84:87], v[56:59], v[112:115]\n"
        "ds_read_b128 v[84:87], v3 offset:63488\n"
        "s_waitcnt lgkmcnt(5)\n"
        "v_mfma_f32_16x16x32_bf16 v[88:91], v[60:63], v[12:15], v[88:91]\n"
        "ds_read_b128 v[60:63], v5\n"
        "v_add3_u32 v6, v6, v12, v13\n"
        "v_mfma_f32_16x16x32_bf16 v[92:95], v[64:67], v[12:15], v[92:95]\n"
        "ds_read_b128 v[64:67], v5 offset:1024\n"
        "s_waitcnt lgkmcnt(5)\n"
        "v_mfma_f32_16x16x32_bf16 v[96:99], v[68:71], v[12:15], v[96:99]\n"
        "ds_read_b128 v[68:71], v5 offset:2048\n"
        "v_add3_u32 v6, v6, v14, v15\n"
        "v_mfma_f32_16x16x32_bf16 v[100:103], v[72:75], v[12:15], v[100:103]\n"
        "ds_read_b128 v[72:75], v5 offset:3072\n"
        "s_waitcnt lgkmcnt(5)\n"
        "v_mfma_f32_16x16x32_bf16 v[104:107], v[76:79], v[12:15], v[104:107]\n"
        "ds_read_b128 v[76:79], v5 offset:4096\n"
        "v_mfma_f32_16x16x32_bf16 v[108:111], v[80:83], v[12:15], v[108:111]\n"
        "ds_read_b128 v[80:83], v5 offset:5120\n"
        "s_waitcnt vmcnt(6) lgkmcnt(5)\n"
        "v_mfma_f32_16x16x32_bf16 v[112:115], v[84:87], v[12:15], v[112:115]\n"
        "ds_read_b128 v[84:87], v5 offset:6144\n"
        "v_mfma_f32_16x16x32_bf16 v[88:91], v[60:63], v[16:19], v[88:91]\n"
        "ds_read_b128 v[60:63], v5 offset:7168\n"
        "s_waitcnt lgkmcnt(5)\n"
        "v_mfma_f32_16x16x32_bf16 v[92:95], v[64:67], v[16:19], v[92:95]\n"
        "ds_read_b128 v[64:67], v5 offset:8192\n"
        "v_add3_u32 v6, v6, v16, v17\n"
        "v_mfma_f32_16x16x32_bf16 v[96:99], v[68:71], v[16:19], v[96:99]\n"
        "ds_read_b128 v[68:71], v5 offset:9216\n"
        "s_waitcnt lgkmcnt(5)\n"
        "v_mfma_f32_16x16x32_bf16 v[100:103], v[72:75], v[16:19], v[100:103]\n"
        "ds_read_b128 v[72:75], v5 offset:10240\n"
        "v_add3_u32 v6, v6, v18, v19\n"
        "v_mfma_f32_16x16x32_bf16 v[104:107], v[76:79], v[16:19], v[104:107]\n"
        "ds_read_b128 v[76:79], v5 offset:11264\n"
        "s_waitcnt vmcnt(5) lgkmcnt(5)\n"
        "v_mfma_f32_16x16x32_bf16 v[108:111], v[80:83], v[16:19], v[108:111]\n"
        "ds_read_b128 v[80:83], v5 offset:12288\n"
        "v_mfma_f32_16x16x32_bf16 v[112:115], v[84:87], v[16:19], v[112:115]\n"
        "ds_read_b128 v[84:87], v5 offset:13312\n"
        "s_waitcnt lgkmcnt(5)\n"
        "v_mfma_f32_16x16x32_bf16 v[88:91], v[60:63], v[20:23], v[88:91]\n"
        "ds_read_b128 v[60:63], v5 offset:14336\n"
        "v_add3_u32 v6, v6, v20, v21\n"
        "v_mfma_f32_16x16x32_bf16 v[92:95], v[64:67], v[20:23], v[92:95]\n"
        "ds_read_b128 v[64:67], v5 offset:15360\n"
        "s_waitcnt lgkmcnt(5)\n"
        "v_mfma_f32_16x16x32_bf16 v[96:99], v[68:71], v[20:23], v[96:99]\n"
        "ds_read_b128 v[68:71], v5 offset:16384\n"
        "v_add3_u32 v6, v6, v22, v23\n"
        "v_mfma_f32_16x16x32_bf16 v[100:103], v[72:75], v[20:23], v[100:103]\n"
        "ds_read_b128 v[72:75], v5 offset:17408\n"
        "s_waitcnt lgkmcnt(5)\n"
        "v_mfma_f32_16x16x32_bf16 v[104:107], v[76:79], v[20:23], v[104:107]\n"
        "ds_read_b128 v[76:79], v5 offset:18432\n"
        "v_mfma_f32_16x16x32_bf16 v[108:111], v[80:83], v[20:23], v[108:111]\n"
        "ds_read_b128 v[80:83], v5 offset:19456\n"
        "s_waitcnt vmcnt(4) lgkmcnt(5)\n"
        "v_mfma_f32_16x16x32_bf16 v[112:115], v[84:87], v[20:23], v[112:115]\n"
        "ds_read_b128 v[84:87], v5 offset:20480\n"
        "v_mfma_f32_16x16x32_bf16 v[88:91], v[60:63], v[24:27], v[88:91]\n"
        "ds_read_b128 v[60:63], v5 offset:21504\n"
        "s_waitcnt lgkmcnt(5)\n"
        "v_mfma_f32_16x16x32_bf16 v[92:95], v[64:67], v[24:27], v[92:95]\n"
        "ds_read_b128 v[64:67], v5 offset:22528\n"
        "v_add3_u32 v6, v6, v24, v25\n"
        "v_mfma_f32_16x16x32_bf16 v[96:99], v[68:71], v[24:27], v[96:99]\n"
        "ds_read_b128 v[68:71], v5 offset:23552\n"
        "s_waitcnt lgkmcnt(5)\n"
        "v_mfma_f32_16x16x32_bf16 v[100:103], v[72:75], v[24:27], v[100:103]\n"
        "ds_read_b128 v[72:75], v5 offset:24576\n"
        "v_add3_u32 v6, v6, v26, v27\n"
        "v_mfma_f32_16x16x32_bf16 v[104:107], v[76:79], v[24:27], v[104:107]\n"
        "ds_read_b128 v[76:79], v5 offset:25600\n"
        "s_waitcnt vmcnt(3) lgkmcnt(5)\n"
        "v_mfma_f32_16x16x32_bf16 v[108:111], v[80:83], v[24:27], v[108:111]\n"
        "ds_read_b128 v[80:83], v5 offset:26624\n"
        "v_mfma_f32_16x16x32_bf16 v[112:115], v[84:87], v[24:27], v[112:115]\n"
        "ds_read_b128 v[84:87], v5 offset:27648\n"
        "s_waitcnt lgkmcnt(5)\n"
        "v_mfma_f32_16x16x32_bf16 v[88:91], v[60:63], v[28:31], v[88:91]\n"
        "ds_read_b128 v[60:63], v5 offset:28672\n"
        "v_add3_u32 v6, v6, v28, v29\n"
        "v_mfma_f32_16x16x32_bf16 v[92:95], v[64:67], v[28:31], v[92:95]\n"
        "ds_read_b128 v[64:67], v5 offset:29696\n"
        "s_waitcnt lgkmcnt(5)\n"
        "v_mfma_f32_16x16x32_bf16 v[96:99], v[68:71], v[28:31], v[96:99]\n"
        "ds_read_b128 v[68:71], v5 offset:30720\n"
        "v_add3_u32 v6, v6, v30, v31\n"
        "v_mfma_f32_16x16x32_bf16 v[100:103], v[72:75], v[28:31], v[100:103]\n"
        "ds_read_b128 v[72:75], v5 offset:31744\n"
        "s_waitcnt lgkmcnt(5)\n"
        "v_mfma_f32_16x16x32_bf16 v[104:107], v[76:79], v[28:31], v[104:107]\n"
        "ds_read_b128 v[76:79], v5 offset:32768\n"
        "v_mfma_f32_16x16x32_bf16 v[108:111], v[80:83], v[28:31], v[108:111]\n"
        "ds_read_b128 v[80:83], v5 offset:33792\n"
        "s_waitcnt vmcnt(2) lgkmcnt(5)\n"
        "v_mfma_f32_16x16x32_bf16 v[112:115], v[84:87], v[28:31], v[112:115]\n"
        "ds_read_b128 v[84:87], v5 offset:34816\n"
        "v_mfma_f32_16x16x32_bf16 v[88:91], v[60:63], v[32:35], v[88:91]\n"
        "ds_read_b128 v[60:63], v5 offset:35840\n"
        "s_waitcnt lgkmcnt(5)\n"
        "v_mfma_f32_16x16x32_bf16 v[92:95], v[64:67], v[32:35], v[92:95]\n"
        "ds_read_b128 v[64:67], v5 offset:36864\n"
        "v_add3_u32 v6, v6, v32, v33\n"
        "v_mfma_f32_16x16x32_bf16 v[96:99], v[68:71], v[32:35], v[96:99]\n"
        "ds_read_b128 v[68:71], v5 offset:37888\n"
        "s_waitcnt lgkmcnt(5)\n"
        "v_mfma_f32_16x16x32_bf16 v[100:103], v[72:75], v[32:35], v[100:103]\n"
        "ds_read_b128 v[72:75], v5 offset:38912\n"
        "v_add3_u32 v6, v6, v34, v35\n"
        "v_mfma_f32_16x16x32_bf16 v[104:107], v[76:79], v[32:35], v[104:107]\n"
        "ds_read_b128 v[76:79], v5 offset:39936\n"
        "s_waitcnt vmcnt(1) lgkmcnt(5)\n"
        "v_mfma_f32_16x16x32_bf16 v[108:111], v[80:83], v[32:35], v[108:111]\n"
        "ds_read_b128 v[80:83], v5 offset:40960\n"
        "v_mfma_f32_16x16x32_bf16 v[112:115], v[84:87], v[32:35], v[112:115]\n"
        "ds_read_b128 v[84:87], v5 offset:41984\n"
        "s_waitcnt lgkmcnt(5)\n"
        "v_mfma_f32_16x16x32_bf16 v[88:91], v[60:63], v[36:39], v[88:91]\n"
        "ds_read_b128 v[60:63], v5 offset:43008\n"
        "v_add3_u32 v6, v6, v36, v37\n"
        "v_mfma_f32_16x16x32_bf16 v[92:95], v[64:67], v[36:39], v[92:95]\n"
        "ds_read_b128 v[64:67], v5 offset:44032\n"
        "s_waitcnt lgkmcnt(5)\n"
        "v_mfma_f32_16x16x32_bf16 v[96:99], v[68:71], v[36:39], v[96:99]\n"
        "ds_read_b128 v[68:71], v5 offset:45056\n"
        "v_add3_u32 v6, v6, v38, v39\n"
        "v_mfma_f32_16x16x32_bf16 v[100:103], v[72:75], v[36:39], v[100:103]\n"
        "ds_read_b128 v[72:75], v5 offset:46080\n"
        "s_waitcnt lgkmcnt(5)\n"
        "v_mfma_f32_16x16x32_bf16 v[104:107], v[76:79], v[36:39], v[104:107]\n"
        "ds_read_b128 v[76:79], v5 offset:47104\n"
        "v_mfma_f32_16x16x32_bf16 v[108:111], v[80:83], v[36:39], v[108:111]\n"
        "ds_read_b128 v[80:83], v5 offset:48128\n"
        "s_waitcnt vmcnt(0) lgkmcnt(6)\n"
        "v_mfma_f32_16x16x32_bf16 v[112:115], v[84:87], v[36:39], v[112:115]\n"
        "ds_read_b128 v[84:87], v5 offset:49152\n"
        "s_waitcnt lgkmcnt(6)\n"
        "v_mfma_f32_16x16x32_bf16 v[88:91], v[60:63], v[40:43], v[88:91]\n"
        "s_waitcnt lgkmcnt(5)\n"
        "v_mfma_f32_16x16x32_bf16 v[92:95], v[64:67], v[40:43], v[92:95]\n"
        "v_add3_u32 v6, v6, v40, v41\n"
        "s_waitcnt lgkmcnt(4)\n"
        "v_mfma_f32_16x16x32_bf16 v[96:99], v[68:71], v[40:43], v[96:99]\n"
        "s_waitcnt lgkmcnt(3)\n"
        "v_mfma_f32_16x16x32_bf16 v[100:103], v[72:75], v[40:43], v[100:103]\n"
        "v_add3_u32 v6, v6, v42, v43\n"
        "s_waitcnt lgkmcnt(2)\n"
        "v_mfma_f32_16x16x32_bf16 v[104:107], v[76:79], v[40:43], v[104:107]\n"
        "s_waitcnt lgkmcnt(1)\n"
        "v_mfma_f32_16x16x32_bf16 v[108:111], v[80:83], v[40:43], v[108:111]\n"
        "s_waitcnt lgkmcnt(0)\n"
        "v_mfma_f32_16x16x32_bf16 v[112:115], v[84:87], v[40:43], v[112:115]\n"
        "v_cmp_ne_u32_e32 vcc, v6, v7\n"
        "s_cbranch_vccnz .Lk124\n"
        ".Lk125:\n"
        "s_setprio 1\n"
        "s_add_u32 s3, s48, 0\n"
        "buffer_load_dwordx4 v[44:47], v3, s[12:15], s3 offen sc1\n"
        "s_add_u32 s44, s3, 0xf00\n"
        "v_exp_f32_e32 v76, v88\n"
        "v_exp_f32_e32 v77, v89\n"
        "v_exp_f32_e32 v78, v90\n"
        "v_exp_f32_e32 v79, v91\n"
        "v_add_f32_e32 v76, 1.0, v76\n"
        "v_add_f32_e32 v77, 1.0, v77\n"
        "v_add_f32_e32 v78, 1.0, v78\n"
        "v_add_f32_e32 v79, 1.0, v79\n"
        "v_rcp_f32_e32 v76, v76\n"
        "v_rcp_f32_e32 v77, v77\n"
        "v_rcp_f32_e32 v78, v78\n"
        "buffer_load_dwordx4 v[48:51], v3, s[12:15], s3 offen offset:1024 sc1\n"
        "v_rcp_f32_e32 v79, v79\n"
        "v_mul_f32_e32 v76, v88, v76\n"
        "v_mul_f32_e32 v77, v89, v77\n"
        "v_mul_f32_e32 v78, v90, v78\n"
        "v_mul_f32_e32 v79, v91, v79\n"
        "v_cvt_pk_bf16_f32 v88, v76, v77\n"
        "v_cvt_pk_bf16_f32 v89, v78, v79\n"
        "v_exp_f32_e32 v80, v92\n"
        "v_exp_f32_e32 v81, v93\n"
        "v_exp_f32_e32 v82, v94\n"
        "v_exp_f32_e32 v83, v95\n"
        "v_exp_f32_e32 v84, v96\n"
        "v_exp_f32_e32 v85, v97\n"
        "buffer_load_dwordx4 v[52:55], v3, s[12:15], s3 offen offset:2048 sc1\n"
        "v_exp_f32_e32 v86, v98\n"
        "v_exp_f32_e32 v87, v99\n"
        "v_exp_f32_e32 v76, v100\n"
        "v_exp_f32_e32 v77, v101\n"
        "v_exp_f32_e32 v78, v102\n"
        "v_exp_f32_e32 v79, v103\n"
        "v_add_f32_e32 v80, 1.0, v80\n"
        "v_add_f32_e32 v81, 1.0, v81\n"
        "v_add_f32_e32 v82, 1.0, v82\n"
        "v_add_f32_e32 v83, 1.0, v83\n"
        "v_rcp_f32_e32 v80, v80\n"
        "buffer_load_dwordx3 v[56:58], v4, s[12:15], s3 offen offset:3072 sc1\n"
        "v_rcp_f32_e32 v81, v81\n"
        "v_rcp_f32_e32 v82, v82\n"
        "v_rcp_f32_e32 v83, v83\n"
        "v_add_f32_e32 v84, 1.0, v84\n"
        "v_add_f32_e32 v85, 1.0, v85\n"
        "v_add_f32_e32 v86, 1.0, v86\n"
        "v_add_f32_e32 v87, 1.0, v87\n"
        "v_add_f32_e32 v76, 1.0, v76\n"
        "v_add_f32_e32 v77, 1.0, v77\n"
        "v_add_f32_e32 v78, 1.0, v78\n"
        "v_add_f32_e32 v79, 1.0, v79\n"
        "v_rcp_f32_e32 v84, v84\n"
        "v_rcp_f32_e32 v85, v85\n"
        "v_rcp_f32_e32 v86, v86\n"
        "buffer_load_dwordx4 v[12:15], v3, s[12:15], s44 offen sc1\n"
        "v_rcp_f32_e32 v87, v87\n"
        "v_rcp_f32_e32 v76, v76\n"
        "v_rcp_f32_e32 v77, v77\n"
        "v_rcp_f32_e32 v78, v78\n"
        "v_rcp_f32_e32 v79, v79\n"
        "v_mul_f32_e32 v80, v92, v80\n"
        "v_mul_f32_e32 v81, v93, v81\n"
        "v_mul_f32_e32 v82, v94, v82\n"
        "v_mul_f32_e32 v83, v95, v83\n"
        "v_cvt_pk_bf16_f32 v90, v80, v81\n"
        "v_cvt_pk_bf16_f32 v91, v82, v83\n"
        "v_mul_f32_e32 v84, v96, v84\n"
        "v_mul_f32_e32 v85, v97, v85\n"
        "buffer_load_dwordx4 v[16:19], v3, s[12:15], s44 offen offset:1024 sc1\n"
        "v_mul_f32_e32 v86, v98, v86\n"
        "v_mul_f32_e32 v87, v99, v87\n"
        "v_mul_f32_e32 v76, v100, v76\n"
        "v_mul_f32_e32 v77, v101, v77\n"
        "v_mul_f32_e32 v78, v102, v78\n"
        "v_mul_f32_e32 v79, v103, v79\n"
        "v_cvt_pk_bf16_f32 v96, v84, v85\n"
        "v_cvt_pk_bf16_f32 v97, v86, v87\n"
        "v_cvt_pk_bf16_f32 v98, v76, v77\n"
        "v_cvt_pk_bf16_f32 v99, v78, v79\n"
        "v_exp_f32_e32 v80, v104\n"
        "v_exp_f32_e32 v81, v105\n"
        "v_exp_f32_e32 v82, v106\n"
        "v_exp_f32_e32 v83, v107\n"
        "v_exp_f32_e32 v84, v108\n"
        "buffer_load_dwordx4 v[20:23], v3, s[12:15], s44 offen offset:2048 sc1\n"
        "v_exp_f32_e32 v85, v109\n"
        "v_exp_f32_e32 v86, v110\n"
        "v_exp_f32_e32 v87, v111\n"
        "v_exp_f32_e32 v76, v112\n"
        "v_exp_f32_e32 v77, v113\n"
        "v_exp_f32_e32 v78, v114\n"
        "v_exp_f32_e32 v79, v115\n"
        "v_add_f32_e32 v80, 1.0, v80\n"
        "v_add_f32_e32 v81, 1.0, v81\n"
        "v_add_f32_e32 v82, 1.0, v82\n"
        "v_add_f32_e32 v83, 1.0, v83\n"
        "buffer_load_dwordx3 v[24:26], v4, s[12:15], s44 offen offset:3072 sc1\n"
        "v_rcp_f32_e32 v80, v80\n"
        "v_rcp_f32_e32 v81, v81\n"
        "v_rcp_f32_e32 v82, v82\n"
        "v_rcp_f32_e32 v83, v83\n"
        "v_add_f32_e32 v84, 1.0, v84\n"
        "v_add_f32_e32 v85, 1.0, v85\n"
        "v_add_f32_e32 v86, 1.0, v86\n"
        "v_add_f32_e32 v87, 1.0, v87\n"
        "v_add_f32_e32 v76, 1.0, v76\n"
        "v_add_f32_e32 v77, 1.0, v77\n"
        "v_add_f32_e32 v78, 1.0, v78\n"
        "v_add_f32_e32 v79, 1.0, v79\n"
        "v_add3_u32 v6, v88, v89, s58\n"
        "v_rcp_f32_e32 v84, v84\n"
        "v_rcp_f32_e32 v85, v85\n"
        "v_rcp_f32_e32 v86, v86\n"
        "v_rcp_f32_e32 v87, v87\n"
        "v_rcp_f32_e32 v76, v76\n"
        "v_rcp_f32_e32 v77, v77\n"
        "v_rcp_f32_e32 v78, v78\n"
        "v_rcp_f32_e32 v79, v79\n"
        "v_add3_u32 v6, v90, v91, v6\n"
        "v_add3_u32 v6, v96, v97, v6\n"
        "v_mul_f32_e32 v80, v104, v80\n"
        "v_mul_f32_e32 v81, v105, v81\n"
        "v_mul_f32_e32 v82, v106, v82\n"
        "v_mul_f32_e32 v83, v107, v83\n"
        "v_add3_u32 v6, v98, v99, v6\n"
        "v_cvt_pk_bf16_f32 v104, v80, v81\n"
        "v_cvt_pk_bf16_f32 v105, v82, v83\n"
        "v_mul_f32_e32 v84, v108, v84\n"
        "v_mul_f32_e32 v85, v109, v85\n"
        "v_mul_f32_e32 v86, v110, v86\n"
        "v_mul_f32_e32 v87, v111, v87\n"
        "v_mul_f32_e32 v76, v112, v76\n"
        "v_mul_f32_e32 v77, v113, v77\n"
        "v_mul_f32_e32 v78, v114, v78\n"
        "v_mul_f32_e32 v79, v115, v79\n"
        "buffer_store_dwordx4 v[88:91], v3, s[12:15], s46 offen sc1\n"
        "v_add3_u32 v6, v104, v105, v6\n"
        "v_cvt_pk_bf16_f32 v106, v84, v85\n"
        "v_cvt_pk_bf16_f32 v107, v86, v87\n"
        "v_cvt_pk_bf16_f32 v112, v76, v77\n"
        "v_cvt_pk_bf16_f32 v113, v78, v79\n"
        "buffer_store_dwordx4 v[96:99], v3, s[12:15], s46 offen offset:1024 sc1\n"
        "v_add3_u32 v6, v106, v107, v6\n"
        "v_and_b32_e32 v112, s41, v112\n"
        "v_and_b32_e32 v113, s41, v113\n"
        "buffer_store_dwordx4 v[104:107], v3, s[12:15], s46 offen offset:2048 sc1\n"
        "v_add3_u32 v114, v112, v113, v6\n"
        "buffer_store_dwordx3 v[112:114], v4, s[12:15], s46 offen offset:3072 sc1\n"
        "s_barrier\n"
        "s_setprio 0\n"
        "s_add_u32 s62, s61, 0x6000\n"
        "buffer_load_dwordx4 v[216:219], v3, s[12:15], s62 offen offset:1024 sc1\n"
        "buffer_load_dwordx4 v[220:223], v3, s[12:15], s62 offen offset:2048 sc1\n"
        "buffer_load_dwordx4 v[224:227], v3, s[12:15], s62 offen offset:3072 sc1\n"
        "s_add_u32 s62, s61, 0x7000\n"
        "buffer_load_dwordx4 v[228:231], v3, s[12:15], s62 offen sc1\n"
        "buffer_load_dwordx4 v[232:235], v3, s[12:15], s62 offen offset:1024 sc1\n"
        "buffer_load_dwordx4 v[236:239], v3, s[12:15], s62 offen offset:2048 sc1\n"
        "buffer_load_dwordx4 v[240:243], v3, s[12:15], s62 offen offset:3072 sc1\n"
        "s_add_u32 s62, s61, 0x8000\n"
        "buffer_load_dwordx4 v[244:247], v3, s[12:15], s62 offen sc1\n"
        "buffer_load_dwordx4 v[248:251], v3, s[12:15], s62 offen offset:1024 sc1\n"
        "buffer_load_dwordx4 v[252:255], v3, s[12:15], s62 offen offset:2048 sc1\n"
        "s_add_u32 s3, s48, 0xf000\n"
        "s_add_u32 s44, s3, 0xf00\n"
        "buffer_load_dwordx4 v[28:31], v3, s[12:15], s3 offen sc1\n"
        "buffer_load_dwordx4 v[32:35], v3, s[12:15], s3 offen offset:1024 sc1\n"
        "buffer_load_dwordx4 v[36:39], v3, s[12:15], s3 offen offset:2048 sc1\n"
        "buffer_load_dwordx3 v[40:42], v4, s[12:15], s3 offen offset:3072 sc1\n"
        "buffer_load_dwordx4 v[60:63], v3, s[12:15], s44 offen sc1\n"
        "buffer_load_dwordx4 v[64:67], v3, s[12:15], s44 offen offset:1024 sc1\n"
        "buffer_load_dwordx4 v[68:71], v3, s[12:15], s44 offen offset:2048 sc1\n"
        "buffer_load_dwordx3 v[72:74], v4, s[12:15], s44 offen offset:3072 sc1\n"
        ".Lk117:\n"
        "v_and_b32_e32 v1, 63, v0\n"
        "v_bfe_u32 v5, v0, 4, 2\n"
        "s_and_b32 s42, s33, 3\n"
        "s_lshl_b32 s42, s42, 10\n"
        "v_add_u32_e32 v7, s42, v3\n"
        "v_and_b32_e32 v11, 7, v1\n"
        "v_bfe_u32 v9, v1, 3, 1\n"
        "v_lshl_add_u32 v11, v11, 8, v9\n"
        "v_mul_u32_u24_e32 v11, 0x500, v11\n"
        "s_and_b32 s43, s2, 25\n"
        "s_lshl_b32 s67, s43, 5\n"
        "s_or_b32 s43, s43, s67\n"
        "s_bfe_u32 s43, s43, 0x30003\n"
        "s_mul_i32 s43, s43, 0xa0\n"
        "s_mul_i32 s42, s45, 0xa00\n"
        "s_add_u32 s42, s42, s43\n"
        "v_add_u32_e32 v11, s42, v11\n"
        "s_and_b32 s43, s33, 1\n"
        "s_lshl_b32 s43, s43, 6\n"
        "s_bitcmp1_b32 s35, 0\n"
        "s_cselect_b32 s42, 32, 0\n"
        "s_add_u32 s43, s43, s42\n"
        "s_bfe_u32 s42, s33, 0x10001\n"
        "s_mul_i32 s42, s42, 0xa00\n"
        "s_add_u32 s43, s43, s42\n"
        "v_lshl_add_u32 v8, v5, 4, v11\n"
        "v_add_u32_e32 v8, s43, v8\n"
        "s_bfe_u32 s53, s33, 0x10001\n"
        "s_and_b32 s43, s33, 4\n"
        "s_add_u32 s53, s53, s43\n"
        "s_bitcmp1_b32 s35, 0\n"
        "s_cselect_b32 s42, 0, 0x80\n"
        "s_mul_i32 s43, s53, 0xa00\n"
        "s_add_u32 s42, s42, s43\n"
        "v_lshl_add_u32 v9, v5, 3, v11\n"
        "v_add_u32_e32 v9, s42, v9\n"
        "s_add_u32 s53, s53, s45\n"
        "s_mul_i32 s53, s53, 0x5500\n"
        "s_add_u32 s53, s53, 0x560000\n"
        "s_add_u32 s55, s53, 0x5400\n"
        "s_add_u32 s49, s55, 0x15400\n"
        "s_and_b32 s43, s2, 25\n"
        "s_lshl_b32 s67, s43, 5\n"
        "s_or_b32 s43, s43, s67\n"
        "s_bfe_u32 s43, s43, 0x30003\n"
        "s_mul_i32 s43, s43, 5\n"
        "s_add_u32 s43, s43, 1\n"
        "s_lshr_b32 s43, s43, 1\n"
        "s_bitcmp1_b32 s35, 0\n"
        "s_cselect_b32 s42, -1, 4\n"
        "s_lshl1_add_u32 s51, s43, s42\n"
        "s_lshl_b32 s51, s51, 9\n"
        "s_add_u32 s51, s51, s53\n"
        "s_and_b32 s42, s33, 1\n"
        "s_add_u32 s54, s43, s42\n"
        "s_lshl_b32 s54, s54, 10\n"
        "s_add_u32 s54, s54, s53\n"
        "s_add_u32 s47, s54, 0x15400\n"
        "s_movk_i32 s50, 0x2800\n"
        "v_and_b32_e32 v10, 0xf0, v3\n"
        "v_lshrrev_b32_e32 v11, 1, v5\n"
        "v_lshl_add_u32 v11, v11, 8, v10\n"
        "v_and_b32_e32 v5, 1, v5\n"
        "v_lshl_add_u32 v11, v5, 3, v11\n"
        "s_cmp_lt_u32 s33, 4\n"
        "s_cbranch_scc0 .Lk126\n"
        "s_sleep 1\n"
        "s_add_u32 s3, s48, 0\n"
        "s_add_u32 s44, s3, 0xf00\n"
        "buffer_load_dwordx4 v[44:47], v3, s[12:15], s3 offen sc1\n"
        "s_waitcnt vmcnt(7)\n"
        "buffer_load_dwordx4 v[48:51], v3, s[12:15], s3 offen offset:1024 sc1\n"
        "s_waitcnt vmcnt(7)\n"
        "buffer_load_dwordx4 v[52:55], v3, s[12:15], s3 offen offset:2048 sc1\n"
        "s_waitcnt vmcnt(7)\n"
        "buffer_load_dwordx3 v[56:58], v4, s[12:15], s3 offen offset:3072 sc1\n"
        "s_waitcnt vmcnt(7)\n"
        "buffer_load_dwordx4 v[12:15], v3, s[12:15], s44 offen sc1\n"
        "s_waitcnt vmcnt(7)\n"
        "buffer_load_dwordx4 v[16:19], v3, s[12:15], s44 offen offset:1024 sc1\n"
        "s_waitcnt vmcnt(7)\n"
        "buffer_load_dwordx4 v[20:23], v3, s[12:15], s44 offen offset:2048 sc1\n"
        "s_waitcnt vmcnt(7)\n"
        "buffer_load_dwordx3 v[24:26], v4, s[12:15], s44 offen offset:3072 sc1\n"
        "s_waitcnt vmcnt(7)\n"
        "s_add_u32 s3, s48, 0xf000\n"
        "s_add_u32 s44, s3, 0xf00\n"
        "buffer_load_dwordx4 v[28:31], v3, s[12:15], s3 offen sc1\n"
        "s_waitcnt vmcnt(7)\n"
        "buffer_load_dwordx4 v[32:35], v3, s[12:15], s3 offen offset:1024 sc1\n"
        "s_waitcnt vmcnt(7)\n"
        "buffer_load_dwordx4 v[36:39], v3, s[12:15], s3 offen offset:2048 sc1\n"
        "s_waitcnt vmcnt(7)\n"
        "buffer_load_dwordx3 v[40:42], v4, s[12:15], s3 offen offset:3072 sc1\n"
        "s_waitcnt vmcnt(7)\n"
        "buffer_load_dwordx4 v[60:63], v3, s[12:15], s44 offen sc1\n"
        "s_waitcnt vmcnt(7)\n"
        "buffer_load_dwordx4 v[64:67], v3, s[12:15], s44 offen offset:1024 sc1\n"
        "s_waitcnt vmcnt(7)\n"
        "buffer_load_dwordx4 v[68:71], v3, s[12:15], s44 offen offset:2048 sc1\n"
        "s_waitcnt vmcnt(7)\n"
        "buffer_load_dwordx3 v[72:74], v4, s[12:15], s44 offen offset:3072 sc1\n"
        "s_waitcnt vmcnt(7)\n"
        "s_barrier\n"
        "v_mfma_f32_16x16x32_bf16 v[88:91], v[116:119], v[44:47], 0\n"
        "v_add3_u32 v6, v44, v45, s58\n"
        "v_mfma_f32_16x16x32_bf16 v[92:95], v[120:123], v[44:47], 0\n"
        "v_add3_u32 v6, v46, v47, v6\n"
        "v_mfma_f32_16x16x32_bf16 v[96:99], v[124:127], v[44:47], 0\n"
        "v_mfma_f32_16x16x32_bf16 v[100:103], v[128:131], v[44:47], 0\n"
        "v_mfma_f32_16x16x32_bf16 v[104:107], v[132:135], v[44:47], 0\n"
        "v_mfma_f32_16x16x32_bf16 v[88:91], v[136:139], v[48:51], v[88:91]\n"
        "v_add3_u32 v6, v48, v49, v6\n"
        "v_mfma_f32_16x16x32_bf16 v[92:95], v[140:143], v[48:51], v[92:95]\n"
        "v_mfma_f32_16x16x32_bf16 v[96:99], v[144:147], v[48:51], v[96:99]\n"
        "v_add3_u32 v6, v50, v51, v6\n"
        "v_mfma_f32_16x16x32_bf16 v[100:103], v[148:151], v[48:51], v[100:103]\n"
        "v_mfma_f32_16x16x32_bf16 v[104:107], v[152:155], v[48:51], v[104:107]\n"
        "v_mfma_f32_16x16x32_bf16 v[88:91], v[156:159], v[52:55], v[88:91]\n"
        "v_mfma_f32_16x16x32_bf16 v[92:95], v[160:163], v[52:55], v[92:95]\n"
        "v_add3_u32 v6, v52, v53, v6\n"
        "v_add3_u32 v6, v54, v55, v6\n"
        "v_mfma_f32_16x16x32_bf16 v[96:99], v[164:167], v[52:55], v[96:99]\n"
        "v_add3_u32 v6, v56, v57, v6\n"
        "v_mfma_f32_16x16x32_bf16 v[100:103], v[168:171], v[52:55], v[100:103]\n"
        "v_mfma_f32_16x16x32_bf16 v[104:107], v[172:175], v[52:55], v[104:107]\n"
        "v_mfma_f32_16x16x32_bf16 v[88:91], v[176:179], v[12:15], v[88:91]\n"
        "v_add3_u32 v1, v12, v13, s58\n"
        "v_mfma_f32_16x16x32_bf16 v[92:95], v[180:183], v[12:15], v[92:95]\n"
        "v_add3_u32 v1, v14, v15, v1\n"
        "v_mfma_f32_16x16x32_bf16 v[96:99], v[184:187], v[12:15], v[96:99]\n"
        "v_mfma_f32_16x16x32_bf16 v[100:103], v[188:191], v[12:15], v[100:103]\n"
        "v_mfma_f32_16x16x32_bf16 v[104:107], v[192:195], v[12:15], v[104:107]\n"
        "v_mfma_f32_16x16x32_bf16 v[88:91], v[196:199], v[16:19], v[88:91]\n"
        "v_add3_u32 v1, v16, v17, v1\n"
        "v_mfma_f32_16x16x32_bf16 v[92:95], v[200:203], v[16:19], v[92:95]\n"
        "v_add3_u32 v1, v18, v19, v1\n"
        "v_mfma_f32_16x16x32_bf16 v[96:99], v[204:207], v[16:19], v[96:99]\n"
        "v_mfma_f32_16x16x32_bf16 v[100:103], v[208:211], v[16:19], v[100:103]\n"
        "v_mfma_f32_16x16x32_bf16 v[104:107], v[212:215], v[16:19], v[104:107]\n"
        "v_mfma_f32_16x16x32_bf16 v[88:91], v[216:219], v[20:23], v[88:91]\n"
        "v_add3_u32 v1, v20, v21, v1\n"
        "v_mfma_f32_16x16x32_bf16 v[92:95], v[220:223], v[20:23], v[92:95]\n"
        "v_add3_u32 v1, v22, v23, v1\n"
        "v_mfma_f32_16x16x32_bf16 v[96:99], v[224:227], v[20:23], v[96:99]\n"
        "v_add3_u32 v1, v24, v25, v1\n"
        "v_xor_b32_e32 v6, v6, v58\n"
        "v_mfma_f32_16x16x32_bf16 v[100:103], v[228:231], v[20:23], v[100:103]\n"
        "v_bitop3_b32 v6, v1, v26, v6 bitop3:0xbe\n"
        "v_mov_b32_e32 v58, v24\n"
        "v_mfma_f32_16x16x32_bf16 v[104:107], v[232:235], v[20:23], v[104:107]\n"
        "v_mov_b32_e32 v59, v25\n"
        "s_nop 1\n"
        "v_mfma_f32_16x16x32_bf16 v[88:91], v[236:239], v[56:59], v[88:91]\n"
        "v_mfma_f32_16x16x32_bf16 v[92:95], v[240:243], v[56:59], v[92:95]\n"
        "v_cmp_ne_u32_e32 vcc, 0, v6\n"
        "v_mfma_f32_16x16x32_bf16 v[96:99], v[244:247], v[56:59], v[96:99]\n"
        "v_mfma_f32_16x16x32_bf16 v[100:103], v[248:251], v[56:59], v[100:103]\n"
        "v_mfma_f32_16x16x32_bf16 v[104:107], v[252:255], v[56:59], v[104:107]\n"
        "s_cbranch_vccnz .Lk127\n"
        ".Lk128:\n"
        "v_mfma_f32_16x16x32_bf16 v[108:111], v[116:119], v[28:31], 0\n"
        "v_add3_u32 v6, v28, v29, s58\n"
        "v_mfma_f32_16x16x32_bf16 v[112:115], v[120:123], v[28:31], 0\n"
        "v_add3_u32 v6, v30, v31, v6\n"
        "v_mfma_f32_16x16x32_bf16 v[76:79], v[124:127], v[28:31], 0\n"
        "ds_write_b128 v7, v[88:91]\n"
        "v_mfma_f32_16x16x32_bf16 v[80:83], v[128:131], v[28:31], 0\n"
        "v_mfma_f32_16x16x32_bf16 v[84:87], v[132:135], v[28:31], 0\n"
        "s_waitcnt vmcnt(6)\n"
        "v_mfma_f32_16x16x32_bf16 v[108:111], v[136:139], v[32:35], v[108:111]\n"
        "v_add3_u32 v6, v32, v33, v6\n"
        "v_mfma_f32_16x16x32_bf16 v[112:115], v[140:143], v[32:35], v[112:115]\n"
        "ds_write_b128 v7, v[92:95] offset:4096\n"
        "v_mfma_f32_16x16x32_bf16 v[76:79], v[144:147], v[32:35], v[76:79]\n"
        "v_add3_u32 v6, v34, v35, v6\n"
        "v_mfma_f32_16x16x32_bf16 v[80:83], v[148:151], v[32:35], v[80:83]\n"
        "v_mfma_f32_16x16x32_bf16 v[84:87], v[152:155], v[32:35], v[84:87]\n"
        "s_waitcnt vmcnt(5)\n"
        "v_mfma_f32_16x16x32_bf16 v[108:111], v[156:159], v[36:39], v[108:111]\n"
        "ds_write_b128 v7, v[96:99] offset:8192\n"
        "v_mfma_f32_16x16x32_bf16 v[112:115], v[160:163], v[36:39], v[112:115]\n"
        "v_add3_u32 v6, v36, v37, v6\n"
        "v_add3_u32 v6, v38, v39, v6\n"
        "v_mfma_f32_16x16x32_bf16 v[76:79], v[164:167], v[36:39], v[76:79]\n"
        "s_waitcnt vmcnt(4)\n"
        "v_add3_u32 v6, v40, v41, v6\n"
        "v_mfma_f32_16x16x32_bf16 v[80:83], v[168:171], v[36:39], v[80:83]\n"
        "v_mfma_f32_16x16x32_bf16 v[84:87], v[172:175], v[36:39], v[84:87]\n"
        "ds_write_b128 v7, v[100:103] offset:12288\n"
        "s_waitcnt vmcnt(3)\n"
        "v_mfma_f32_16x16x32_bf16 v[108:111], v[176:179], v[60:63], v[108:111]\n"
        "v_add3_u32 v1, v60, v61, s58\n"
        "v_mfma_f32_16x16x32_bf16 v[112:115], v[180:183], v[60:63], v[112:115]\n"
        "v_add3_u32 v1, v62, v63, v1\n"
        "v_mfma_f32_16x16x32_bf16 v[76:79], v[184:187], v[60:63], v[76:79]\n"
        "v_mfma_f32_16x16x32_bf16 v[80:83], v[188:191], v[60:63], v[80:83]\n"
        "ds_write_b128 v7, v[104:107] offset:16384\n"
        "v_mfma_f32_16x16x32_bf16 v[84:87], v[192:195], v[60:63], v[84:87]\n"
        "s_waitcnt vmcnt(2)\n"
        "v_mfma_f32_16x16x32_bf16 v[108:111], v[196:199], v[64:67], v[108:111]\n"
        "v_add3_u32 v1, v64, v65, v1\n"
        "s_add_u32 s3, s48, 0x3c000\n"
        "s_add_u32 s44, s3, 0xf00\n"
        "buffer_load_dwordx4 v[44:47], v3, s[12:15], s3 offen sc1\n"
        "v_mfma_f32_16x16x32_bf16 v[112:115], v[200:203], v[64:67], v[112:115]\n"
        "v_add3_u32 v1, v66, v67, v1\n"
        "v_mfma_f32_16x16x32_bf16 v[76:79], v[204:207], v[64:67], v[76:79]\n"
        "v_mfma_f32_16x16x32_bf16 v[80:83], v[208:211], v[64:67], v[80:83]\n"
        "v_mfma_f32_16x16x32_bf16 v[84:87], v[212:215], v[64:67], v[84:87]\n"
        "buffer_load_dwordx4 v[48:51], v3, s[12:15], s3 offen offset:1024 sc1\n"
        "s_waitcnt vmcnt(3)\n"
        "v_mfma_f32_16x16x32_bf16 v[108:111], v[216:219], v[68:71], v[108:111]\n"
        "v_add3_u32 v1, v68, v69, v1\n"
        "v_mfma_f32_16x16x32_bf16 v[112:115], v[220:223], v[68:71], v[112:115]\n"
        "v_add3_u32 v1, v70, v71, v1\n"
        "v_mfma_f32_16x16x32_bf16 v[76:79], v[224:227], v[68:71], v[76:79]\n"
        "s_waitcnt vmcnt(2)\n"
        "v_add3_u32 v1, v72, v73, v1\n"
        "v_xor_b32_e32 v6, v6, v42\n"
        "s_waitcnt lgkmcnt(0)\n"
        "s_barrier\n"
        "v_mfma_f32_16x16x32_bf16 v[80:83], v[228:231], v[68:71], v[80:83]\n"
        "v_bitop3_b32 v6, v1, v74, v6 bitop3:0xbe\n"
        "v_mov_b32_e32 v42, v72\n"
        "buffer_load_dwordx4 v[52:55], v3, s[12:15], s3 offen offset:2048 sc1\n"
        "v_mfma_f32_16x16x32_bf16 v[84:87], v[232:235], v[68:71], v[84:87]\n"
        "v_mov_b32_e32 v43, v73\n"
        "s_nop 1\n"
        "v_mfma_f32_16x16x32_bf16 v[108:111], v[236:239], v[40:43], v[108:111]\n"
        "v_mfma_f32_16x16x32_bf16 v[112:115], v[240:243], v[40:43], v[112:115]\n"
        "v_cmp_ne_u32_e32 vcc, 0, v6\n"
        "v_mfma_f32_16x16x32_bf16 v[76:79], v[244:247], v[40:43], v[76:79]\n"
        "buffer_load_dwordx3 v[56:58], v4, s[12:15], s3 offen offset:3072 sc1\n"
        "v_mfma_f32_16x16x32_bf16 v[80:83], v[248:251], v[40:43], v[80:83]\n"
        "v_mfma_f32_16x16x32_bf16 v[84:87], v[252:255], v[40:43], v[84:87]\n"
        "s_cbranch_vccnz .Lk130\n"
        ".Lk131:\n"
        "buffer_load_dwordx4 v[12:15], v3, s[12:15], s44 offen sc1\n"
        "buffer_load_dwordx4 v[16:19], v3, s[12:15], s44 offen offset:1024 sc1\n"
        "ds_write_b128 v7, v[108:111] offset:20480\n"
        "ds_write_b128 v7, v[112:115] offset:24576\n"
        "ds_write_b128 v7, v[76:79] offset:28672\n"
        "ds_write_b128 v7, v[80:83] offset:32768\n"
        "s_nop 0\n"
        "ds_write_b128 v7, v[84:87] offset:36864\n"
        "s_waitcnt lgkmcnt(0)\n"
        "s_barrier\n"
        "buffer_load_dwordx4 v[20:23], v3, s[12:15], s44 offen offset:2048 sc1\n"
        "buffer_load_dwordx3 v[24:26], v4, s[12:15], s44 offen offset:3072 sc1\n"
        "s_add_u32 s3, s48, 0x4b000\n"
        "s_add_u32 s44, s3, 0xf00\n"
        "buffer_load_dwordx4 v[28:31], v3, s[12:15], s3 offen sc1\n"
        "buffer_load_dwordx4 v[32:35], v3, s[12:15], s3 offen offset:1024 sc1\n"
        "buffer_load_dwordx4 v[36:39], v3, s[12:15], s3 offen offset:2048 sc1\n"
        "buffer_load_dwordx3 v[40:42], v4, s[12:15], s3 offen offset:3072 sc1\n"
        "buffer_load_dwordx4 v[60:63], v3, s[12:15], s44 offen sc1\n"
        "buffer_load_dwordx4 v[64:67], v3, s[12:15], s44 offen offset:1024 sc1\n"
        "buffer_load_dwordx4 v[68:71], v3, s[12:15], s44 offen offset:2048 sc1\n"
        "buffer_load_dwordx3 v[72:74], v4, s[12:15], s44 offen offset:3072 sc1\n"
        "v_add_u32_e32 v7, 0xa000, v7\n"
        "s_waitcnt vmcnt(15)\n"
        "v_mfma_f32_16x16x32_bf16 v[88:91], v[116:119], v[44:47], 0\n"
        "v_add3_u32 v6, v44, v45, s58\n"
        "v_mfma_f32_16x16x32_bf16 v[92:95], v[120:123], v[44:47], 0\n"
        "v_add3_u32 v6, v46, v47, v6\n"
        "v_mfma_f32_16x16x32_bf16 v[96:99], v[124:127], v[44:47], 0\n"
        "v_mfma_f32_16x16x32_bf16 v[100:103], v[128:131], v[44:47], 0\n"
        "v_mfma_f32_16x16x32_bf16 v[104:107], v[132:135], v[44:47], 0\n"
        "s_waitcnt vmcnt(14)\n"
        "v_mfma_f32_16x16x32_bf16 v[88:91], v[136:139], v[48:51], v[88:91]\n"
        "v_add3_u32 v6, v48, v49, v6\n"
        "v_mfma_f32_16x16x32_bf16 v[92:95], v[140:143], v[48:51], v[92:95]\n"
        "v_mfma_f32_16x16x32_bf16 v[96:99], v[144:147], v[48:51], v[96:99]\n"
        "v_add3_u32 v6, v50, v51, v6\n"
        "v_mfma_f32_16x16x32_bf16 v[100:103], v[148:151], v[48:51], v[100:103]\n"
        "v_mfma_f32_16x16x32_bf16 v[104:107], v[152:155], v[48:51], v[104:107]\n"
        "s_waitcnt vmcnt(13)\n"
        "v_mfma_f32_16x16x32_bf16 v[88:91], v[156:159], v[52:55], v[88:91]\n"
        "v_mfma_f32_16x16x32_bf16 v[92:95], v[160:163], v[52:55], v[92:95]\n"
        "v_add3_u32 v6, v52, v53, v6\n"
        "v_add3_u32 v6, v54, v55, v6\n"
        "v_mfma_f32_16x16x32_bf16 v[96:99], v[164:167], v[52:55], v[96:99]\n"
        "s_waitcnt vmcnt(12)\n"
        "v_add3_u32 v6, v56, v57, v6\n"
        "v_mfma_f32_16x16x32_bf16 v[100:103], v[168:171], v[52:55], v[100:103]\n"
        "v_mfma_f32_16x16x32_bf16 v[104:107], v[172:175], v[52:55], v[104:107]\n"
        "s_waitcnt vmcnt(11)\n"
        "v_mfma_f32_16x16x32_bf16 v[88:91], v[176:179], v[12:15], v[88:91]\n"
        "v_add3_u32 v1, v12, v13, s58\n"
        "v_mfma_f32_16x16x32_bf16 v[92:95], v[180:183], v[12:15], v[92:95]\n"
        "v_add3_u32 v1, v14, v15, v1\n"
        "v_mfma_f32_16x16x32_bf16 v[96:99], v[184:187], v[12:15], v[96:99]\n"
        "v_mfma_f32_16x16x32_bf16 v[100:103], v[188:191], v[12:15], v[100:103]\n"
        "v_mfma_f32_16x16x32_bf16 v[104:107], v[192:195], v[12:15], v[104:107]\n"
        "s_waitcnt vmcnt(10)\n"
        "v_mfma_f32_16x16x32_bf16 v[88:91], v[196:199], v[16:19], v[88:91]\n"
        "v_add3_u32 v1, v16, v17, v1\n"
        "v_mfma_f32_16x16x32_bf16 v[92:95], v[200:203], v[16:19], v[92:95]\n"
        "v_add3_u32 v1, v18, v19, v1\n"
        "v_mfma_f32_16x16x32_bf16 v[96:99], v[204:207], v[16:19], v[96:99]\n"
        "v_mfma_f32_16x16x32_bf16 v[100:103], v[208:211], v[16:19], v[100:103]\n"
        "v_mfma_f32_16x16x32_bf16 v[104:107], v[212:215], v[16:19], v[104:107]\n"
        "s_waitcnt vmcnt(9)\n"
        "v_mfma_f32_16x16x32_bf16 v[88:91], v[216:219], v[20:23], v[88:91]\n"
        "v_add3_u32 v1, v20, v21, v1\n"
        "v_mfma_f32_16x16x32_bf16 v[92:95], v[220:223], v[20:23], v[92:95]\n"
        "v_add3_u32 v1, v22, v23, v1\n"
        "v_mfma_f32_16x16x32_bf16 v[96:99], v[224:227], v[20:23], v[96:99]\n"
        "s_waitcnt vmcnt(8)\n"
        "v_add3_u32 v1, v24, v25, v1\n"
        "v_xor_b32_e32 v6, v6, v58\n"
        "v_mfma_f32_16x16x32_bf16 v[100:103], v[228:231], v[20:23], v[100:103]\n"
        "v_bitop3_b32 v6, v1, v26, v6 bitop3:0xbe\n"
        "v_mov_b32_e32 v58, v24\n"
        "v_mfma_f32_16x16x32_bf16 v[104:107], v[232:235], v[20:23], v[104:107]\n"
        "v_mov_b32_e32 v59, v25\n"
        "s_nop 1\n"
        "v_mfma_f32_16x16x32_bf16 v[88:91], v[236:239], v[56:59], v[88:91]\n"
        "v_mfma_f32_16x16x32_bf16 v[92:95], v[240:243], v[56:59], v[92:95]\n"
        "v_cmp_ne_u32_e32 vcc, 0, v6\n"
        "v_mfma_f32_16x16x32_bf16 v[96:99], v[244:247], v[56:59], v[96:99]\n"
        "v_mfma_f32_16x16x32_bf16 v[100:103], v[248:251], v[56:59], v[100:103]\n"
        "v_mfma_f32_16x16x32_bf16 v[104:107], v[252:255], v[56:59], v[104:107]\n"
        "s_cbranch_vccnz .Lk133\n"
        ".Lk134:\n"
        "s_waitcnt vmcnt(7)\n"
        "v_mfma_f32_16x16x32_bf16 v[108:111], v[116:119], v[28:31], 0\n"
        "v_add3_u32 v6, v28, v29, s58\n"
        "v_mfma_f32_16x16x32_bf16 v[112:115], v[120:123], v[28:31], 0\n"
        "v_add3_u32 v6, v30, v31, v6\n"
        "buffer_load_dwordx2 v[12:13], v10, s[12:15], s55 offen sc1\n"
        "buffer_load_dwordx2 v[16:17], v10, s[12:15], s49 offen sc1\n"
        "v_mfma_f32_16x16x32_bf16 v[76:79], v[124:127], v[28:31], 0\n"
        "ds_write_b128 v7, v[88:91]\n"
        "v_mfma_f32_16x16x32_bf16 v[80:83], v[128:131], v[28:31], 0\n"
        "v_mfma_f32_16x16x32_bf16 v[84:87], v[132:135], v[28:31], 0\n"
        "buffer_load_dwordx4 v[44:47], v3, s[12:15], s54 offen sc1\n"
        "s_waitcnt vmcnt(9)\n"
        "v_mfma_f32_16x16x32_bf16 v[108:111], v[136:139], v[32:35], v[108:111]\n"
        "v_add3_u32 v6, v32, v33, v6\n"
        "v_mfma_f32_16x16x32_bf16 v[112:115], v[140:143], v[32:35], v[112:115]\n"
        "ds_write_b128 v7, v[92:95] offset:4096\n"
        "v_mfma_f32_16x16x32_bf16 v[76:79], v[144:147], v[32:35], v[76:79]\n"
        "v_add3_u32 v6, v34, v35, v6\n"
        "v_mfma_f32_16x16x32_bf16 v[80:83], v[148:151], v[32:35], v[80:83]\n"
        "buffer_load_dwordx4 v[48:51], v3, s[12:15], s47 offen sc1\n"
        "v_mfma_f32_16x16x32_bf16 v[84:87], v[152:155], v[32:35], v[84:87]\n"
        "s_waitcnt vmcnt(9)\n"
        "v_mfma_f32_16x16x32_bf16 v[108:111], v[156:159], v[36:39], v[108:111]\n"
        "ds_write_b128 v7, v[96:99] offset:8192\n"
        "v_mfma_f32_16x16x32_bf16 v[112:115], v[160:163], v[36:39], v[112:115]\n"
        "v_add3_u32 v6, v36, v37, v6\n"
        "v_add3_u32 v6, v38, v39, v6\n"
        "v_mfma_f32_16x16x32_bf16 v[76:79], v[164:167], v[36:39], v[76:79]\n"
        "s_waitcnt vmcnt(8)\n"
        "v_add3_u32 v6, v40, v41, v6\n"
        "v_mfma_f32_16x16x32_bf16 v[80:83], v[168:171], v[36:39], v[80:83]\n"
        "buffer_load_dwordx2 v[52:53], v11, s[12:15], s51 offen sc1\n"
        "v_mfma_f32_16x16x32_bf16 v[84:87], v[172:175], v[36:39], v[84:87]\n"
        "ds_write_b128 v7, v[100:103] offset:12288\n"
        "s_waitcnt vmcnt(8)\n"
        "v_mfma_f32_16x16x32_bf16 v[108:111], v[176:179], v[60:63], v[108:111]\n"
        "v_add3_u32 v1, v60, v61, s58\n"
        "v_mfma_f32_16x16x32_bf16 v[112:115], v[180:183], v[60:63], v[112:115]\n"
        "v_add3_u32 v1, v62, v63, v1\n"
        "v_mfma_f32_16x16x32_bf16 v[76:79], v[184:187], v[60:63], v[76:79]\n"
        "v_mfma_f32_16x16x32_bf16 v[80:83], v[188:191], v[60:63], v[80:83]\n"
        "ds_write_b128 v7, v[104:107] offset:16384\n"
        "v_mfma_f32_16x16x32_bf16 v[84:87], v[192:195], v[60:63], v[84:87]\n"
        "s_waitcnt vmcnt(7)\n"
        "v_mfma_f32_16x16x32_bf16 v[108:111], v[196:199], v[64:67], v[108:111]\n"
        "v_add3_u32 v1, v64, v65, v1\n"
        "v_mfma_f32_16x16x32_bf16 v[112:115], v[200:203], v[64:67], v[112:115]\n"
        "v_add3_u32 v1, v66, v67, v1\n"
        "v_mfma_f32_16x16x32_bf16 v[76:79], v[204:207], v[64:67], v[76:79]\n"
        "v_mfma_f32_16x16x32_bf16 v[80:83], v[208:211], v[64:67], v[80:83]\n"
        "v_mfma_f32_16x16x32_bf16 v[84:87], v[212:215], v[64:67], v[84:87]\n"
        "s_waitcnt vmcnt(6)\n"
        "v_mfma_f32_16x16x32_bf16 v[108:111], v[216:219], v[68:71], v[108:111]\n"
        "v_add3_u32 v1, v68, v69, v1\n"
        "v_mfma_f32_16x16x32_bf16 v[112:115], v[220:223], v[68:71], v[112:115]\n"
        "v_add3_u32 v1, v70, v71, v1\n"
        "v_mfma_f32_16x16x32_bf16 v[76:79], v[224:227], v[68:71], v[76:79]\n"
        "s_waitcnt vmcnt(5)\n"
        "v_add3_u32 v1, v72, v73, v1\n"
        "v_xor_b32_e32 v6, v6, v42\n"
        "s_waitcnt lgkmcnt(0)\n"
        "s_barrier\n"
        "v_mfma_f32_16x16x32_bf16 v[80:83], v[228:231], v[68:71], v[80:83]\n"
        "v_bitop3_b32 v6, v1, v74, v6 bitop3:0xbe\n"
        "v_mov_b32_e32 v42, v72\n"
        "v_mfma_f32_16x16x32_bf16 v[84:87], v[232:235], v[68:71], v[84:87]\n"
        "v_mov_b32_e32 v43, v73\n"
        "s_nop 1\n"
        "v_mfma_f32_16x16x32_bf16 v[108:111], v[236:239], v[40:43], v[108:111]\n"
        "v_mfma_f32_16x16x32_bf16 v[112:115], v[240:243], v[40:43], v[112:115]\n"
        "v_cmp_ne_u32_e32 vcc, 0, v6\n"
        "v_mfma_f32_16x16x32_bf16 v[76:79], v[244:247], v[40:43], v[76:79]\n"
        "v_mfma_f32_16x16x32_bf16 v[80:83], v[248:251], v[40:43], v[80:83]\n"
        "v_mfma_f32_16x16x32_bf16 v[84:87], v[252:255], v[40:43], v[84:87]\n"
        "s_cbranch_vccnz .Lk136\n"
        ".Lk137:\n"
        "ds_write_b128 v7, v[108:111] offset:20480\n"
        "ds_write_b128 v7, v[112:115] offset:24576\n"
        "s_nop 0\n"
        "ds_write_b128 v7, v[76:79] offset:28672\n"
        "s_nop 0\n"
        "ds_write_b128 v7, v[80:83] offset:32768\n"
        "s_nop 0\n"
        "ds_write_b128 v7, v[84:87] offset:36864\n"
        "s_waitcnt lgkmcnt(0)\n"
        "s_barrier\n"
        "s_lshr_b32 s42, s33, 1\n"
        "s_mul_i32 s42, s42, 0x5000\n"
        "s_and_b32 s43, s33, 1\n"
        "s_lshl_b32 s43, s43, 13\n"
        "s_add_u32 s43, s42, s43\n"
        "v_add_u32_e32 v7, s43, v3\n"
        "ds_read_b128 v[156:159], v7\n"
        "ds_read_b128 v[160:163], v7 offset:1024\n"
        "ds_read_b128 v[164:167], v7 offset:2048\n"
        "ds_read_b128 v[168:171], v7 offset:3072\n"
        "ds_read_b128 v[172:175], v7 offset:4096\n"
        "ds_read_b128 v[176:179], v7 offset:5120\n"
        "ds_read_b128 v[180:183], v7 offset:6144\n"
        "ds_read_b128 v[184:187], v7 offset:7168\n"
        "s_waitcnt vmcnt(2)\n"
        "v_lshlrev_b32_e32 v196, 16, v44\n"
        "v_and_b32_e32 v197, 0xffff0000, v44\n"
        "v_lshlrev_b32_e32 v198, 16, v45\n"
        "v_and_b32_e32 v199, 0xffff0000, v45\n"
        "v_lshlrev_b32_e32 v200, 16, v46\n"
        "v_and_b32_e32 v201, 0xffff0000, v46\n"
        "v_lshlrev_b32_e32 v202, 16, v47\n"
        "v_and_b32_e32 v203, 0xffff0000, v47\n"
        "v_fma_f32 v196, v196, v12, v13\n"
        "v_fma_f32 v197, v197, v12, v13\n"
        "v_fma_f32 v198, v198, v12, v13\n"
        "v_fma_f32 v199, v199, v12, v13\n"
        "v_fma_f32 v200, v200, v12, v13\n"
        "v_fma_f32 v201, v201, v12, v13\n"
        "v_fma_f32 v202, v202, v12, v13\n"
        "v_fma_f32 v203, v203, v12, v13\n"
        "s_waitcnt vmcnt(1)\n"
        "v_lshlrev_b32_e32 v204, 16, v48\n"
        "v_and_b32_e32 v205, 0xffff0000, v48\n"
        "v_lshlrev_b32_e32 v206, 16, v49\n"
        "v_and_b32_e32 v207, 0xffff0000, v49\n"
        "v_lshlrev_b32_e32 v208, 16, v50\n"
        "s_waitcnt lgkmcnt(6)\n"
        "v_add_f32_e32 v156, v156, v160\n"
        "v_add_f32_e32 v157, v157, v161\n"
        "v_add_f32_e32 v158, v158, v162\n"
        "v_add_f32_e32 v159, v159, v163\n"
        "s_waitcnt lgkmcnt(4)\n"
        "v_add_f32_e32 v164, v164, v168\n"
        "v_add_f32_e32 v165, v165, v169\n"
        "v_add_f32_e32 v166, v166, v170\n"
        "v_add_f32_e32 v167, v167, v171\n"
        "s_waitcnt lgkmcnt(2)\n"
        "v_add_f32_e32 v172, v172, v176\n"
        "v_add_f32_e32 v173, v173, v177\n"
        "v_add_f32_e32 v174, v174, v178\n"
        "v_add_f32_e32 v175, v175, v179\n"
        "s_waitcnt lgkmcnt(0)\n"
        "v_add_f32_e32 v180, v180, v184\n"
        "v_add_f32_e32 v181, v181, v185\n"
        "v_add_f32_e32 v182, v182, v186\n"
        "v_add_f32_e32 v183, v183, v187\n"
        "v_add_f32_e32 v156, v156, v164\n"
        "v_add_f32_e32 v157, v157, v165\n"
        "v_add_f32_e32 v158, v158, v166\n"
        "v_add_f32_e32 v159, v159, v167\n"
        "v_add_f32_e32 v172, v172, v180\n"
        "v_add_f32_e32 v173, v173, v181\n"
        "v_add_f32_e32 v174, v174, v182\n"
        "v_add_f32_e32 v175, v175, v183\n"
        "v_fmamk_f32 v196, v156, 0xbf317218, v196\n"
        "v_fmamk_f32 v197, v157, 0xbf317218, v197\n"
        "v_fmamk_f32 v198, v158, 0xbf317218, v198\n"
        "v_fmamk_f32 v199, v159, 0xbf317218, v199\n"
        "v_fmamk_f32 v200, v172, 0xbf317218, v200\n"
        "v_fmamk_f32 v201, v173, 0xbf317218, v201\n"
        "v_fmamk_f32 v202, v174, 0xbf317218, v202\n"
        "v_fmamk_f32 v203, v175, 0xbf317218, v203\n"
        "v_cvt_pk_bf16_f32 v212, v196, v197\n"
        "v_cvt_pk_bf16_f32 v213, v198, v199\n"
        "v_cvt_pk_bf16_f32 v214, v200, v201\n"
        "v_cvt_pk_bf16_f32 v215, v202, v203\n"
        "v_and_b32_e32 v209, 0xffff0000, v50\n"
        "v_lshlrev_b32_e32 v210, 16, v51\n"
        "v_and_b32_e32 v211, 0xffff0000, v51\n"
        "buffer_store_dwordx4 v[212:215], v8, s[28:31], 0 offen\n"
        "v_fma_f32 v204, v204, v16, v17\n"
        "v_fma_f32 v205, v205, v16, v17\n"
        "v_fma_f32 v206, v206, v16, v17\n"
        "v_fma_f32 v207, v207, v16, v17\n"
        "v_fma_f32 v208, v208, v16, v17\n"
        "v_fma_f32 v209, v209, v16, v17\n"
        "v_fma_f32 v210, v210, v16, v17\n"
        "v_fma_f32 v211, v211, v16, v17\n"
        "s_bitcmp0_b32 s33, 0\n"
        "s_cbranch_scc0 .Lk139\n"
        "ds_read_b128 v[116:119], v7 offset:16384\n"
        "ds_read_b128 v[120:123], v7 offset:17408\n"
        "ds_read_b128 v[124:127], v7 offset:18432\n"
        "ds_read_b128 v[128:131], v7 offset:19456\n"
        "s_waitcnt vmcnt(1)\n"
        "v_lshlrev_b32_e32 v148, 16, v52\n"
        "v_and_b32_e32 v149, 0xffff0000, v52\n"
        "v_lshlrev_b32_e32 v150, 16, v53\n"
        "v_and_b32_e32 v151, 0xffff0000, v53\n"
        "v_fma_f32 v148, v148, v12, v13\n"
        "v_fma_f32 v149, v149, v12, v13\n"
        "v_fma_f32 v150, v150, v12, v13\n"
        "v_fma_f32 v151, v151, v12, v13\n"
        "s_waitcnt lgkmcnt(2)\n"
        "v_add_f32_e32 v116, v116, v120\n"
        "v_add_f32_e32 v117, v117, v121\n"
        "s_waitcnt lgkmcnt(0)\n"
        "v_add_f32_e32 v124, v124, v128\n"
        "v_add_f32_e32 v125, v125, v129\n"
        "v_add_f32_e32 v118, v118, v122\n"
        "v_add_f32_e32 v119, v119, v123\n"
        "v_add_f32_e32 v126, v126, v130\n"
        "v_add_f32_e32 v127, v127, v131\n"
        "v_add_f32_e32 v116, v116, v124\n"
        "v_add_f32_e32 v117, v117, v125\n"
        "v_add_f32_e32 v118, v118, v126\n"
        "v_add_f32_e32 v119, v119, v127\n"
        "v_fmamk_f32 v148, v116, 0xbf317218, v148\n"
        "v_fmamk_f32 v149, v117, 0xbf317218, v149\n"
        "v_fmamk_f32 v150, v118, 0xbf317218, v150\n"
        "v_fmamk_f32 v151, v119, 0xbf317218, v151\n"
        "v_cvt_pk_bf16_f32 v148, v148, v149\n"
        "v_cvt_pk_bf16_f32 v149, v150, v151\n"
        "buffer_store_dwordx2 v[148:149], v9, s[28:31], 0 offen\n"
        ".Lk139:\n"
        "s_add_u32 s43, s43, 0xa000\n"
        "v_add_u32_e32 v7, s43, v3\n"
        "s_waitcnt lgkmcnt(0)\n"
        "s_barrier\n"
        "ds_read_b128 v[156:159], v7\n"
        "ds_read_b128 v[160:163], v7 offset:1024\n"
        "ds_read_b128 v[164:167], v7 offset:2048\n"
        "ds_read_b128 v[168:171], v7 offset:3072\n"
        "ds_read_b128 v[172:175], v7 offset:4096\n"
        "ds_read_b128 v[176:179], v7 offset:5120\n"
        "ds_read_b128 v[180:183], v7 offset:6144\n"
        "ds_read_b128 v[184:187], v7 offset:7168\n"
        "s_waitcnt lgkmcnt(6)\n"
        "v_add_f32_e32 v156, v156, v160\n"
        "v_add_f32_e32 v157, v157, v161\n"
        "v_add_f32_e32 v158, v158, v162\n"
        "v_add_f32_e32 v159, v159, v163\n"
        "s_waitcnt lgkmcnt(4)\n"
        "v_add_f32_e32 v164, v164, v168\n"
        "v_add_f32_e32 v165, v165, v169\n"
        "v_add_f32_e32 v166, v166, v170\n"
        "v_add_f32_e32 v167, v167, v171\n"
        "s_waitcnt lgkmcnt(2)\n"
        "v_add_f32_e32 v172, v172, v176\n"
        "v_add_f32_e32 v173, v173, v177\n"
        "v_add_f32_e32 v174, v174, v178\n"
        "v_add_f32_e32 v175, v175, v179\n"
        "s_waitcnt lgkmcnt(0)\n"
        "v_add_f32_e32 v180, v180, v184\n"
        "v_add_f32_e32 v181, v181, v185\n"
        "v_add_f32_e32 v182, v182, v186\n"
        "v_add_f32_e32 v183, v183, v187\n"
        "v_add_f32_e32 v156, v156, v164\n"
        "v_add_f32_e32 v157, v157, v165\n"
        "v_add_f32_e32 v158, v158, v166\n"
        "v_add_f32_e32 v159, v159, v167\n"
        "v_add_f32_e32 v172, v172, v180\n"
        "v_add_f32_e32 v173, v173, v181\n"
        "v_add_f32_e32 v174, v174, v182\n"
        "v_add_f32_e32 v175, v175, v183\n"
        "v_fmamk_f32 v204, v156, 0xbf317218, v204\n"
        "v_fmamk_f32 v205, v157, 0xbf317218, v205\n"
        "v_fmamk_f32 v206, v158, 0xbf317218, v206\n"
        "v_fmamk_f32 v207, v159, 0xbf317218, v207\n"
        "v_fmamk_f32 v208, v172, 0xbf317218, v208\n"
        "v_fmamk_f32 v209, v173, 0xbf317218, v209\n"
        "v_fmamk_f32 v210, v174, 0xbf317218, v210\n"
        "v_fmamk_f32 v211, v175, 0xbf317218, v211\n"
        "v_cvt_pk_bf16_f32 v216, v204, v205\n"
        "v_cvt_pk_bf16_f32 v217, v206, v207\n"
        "v_cvt_pk_bf16_f32 v218, v208, v209\n"
        "v_cvt_pk_bf16_f32 v219, v210, v211\n"
        "buffer_store_dwordx4 v[216:219], v8, s[28:31], s50 offen\n"
        "s_branch .Lk140\n"
        ".Lk126:\n"
        "s_barrier\n"
        "ds_read_b128 v[88:91], v7\n"
        "ds_read_b128 v[92:95], v7 offset:4096\n"
        "ds_read_b128 v[96:99], v7 offset:8192\n"
        "ds_read_b128 v[100:103], v7 offset:12288\n"
        "ds_read_b128 v[104:107], v7 offset:16384\n"
        "s_waitcnt vmcnt(29) lgkmcnt(4)\n"
        "v_mfma_f32_16x16x32_bf16 v[88:91], v[116:119], v[44:47], v[88:91]\n"
        "v_add3_u32 v6, v44, v45, s58\n"
        "s_waitcnt lgkmcnt(3)\n"
        "v_mfma_f32_16x16x32_bf16 v[92:95], v[120:123], v[44:47], v[92:95]\n"
        "v_add3_u32 v6, v46, v47, v6\n"
        "s_waitcnt lgkmcnt(2)\n"
        "v_mfma_f32_16x16x32_bf16 v[96:99], v[124:127], v[44:47], v[96:99]\n"
        "s_waitcnt lgkmcnt(1)\n"
        "v_mfma_f32_16x16x32_bf16 v[100:103], v[128:131], v[44:47], v[100:103]\n"
        "s_waitcnt lgkmcnt(0)\n"
        "v_mfma_f32_16x16x32_bf16 v[104:107], v[132:135], v[44:47], v[104:107]\n"
        "s_waitcnt vmcnt(28)\n"
        "v_mfma_f32_16x16x32_bf16 v[88:91], v[136:139], v[48:51], v[88:91]\n"
        "v_add3_u32 v6, v48, v49, v6\n"
        "v_mfma_f32_16x16x32_bf16 v[92:95], v[140:143], v[48:51], v[92:95]\n"
        "v_mfma_f32_16x16x32_bf16 v[96:99], v[144:147], v[48:51], v[96:99]\n"
        "v_add3_u32 v6, v50, v51, v6\n"
        "v_mfma_f32_16x16x32_bf16 v[100:103], v[148:151], v[48:51], v[100:103]\n"
        "v_mfma_f32_16x16x32_bf16 v[104:107], v[152:155], v[48:51], v[104:107]\n"
        "s_waitcnt vmcnt(27)\n"
        "v_mfma_f32_16x16x32_bf16 v[88:91], v[156:159], v[52:55], v[88:91]\n"
        "v_mfma_f32_16x16x32_bf16 v[92:95], v[160:163], v[52:55], v[92:95]\n"
        "v_add3_u32 v6, v52, v53, v6\n"
        "v_add3_u32 v6, v54, v55, v6\n"
        "v_mfma_f32_16x16x32_bf16 v[96:99], v[164:167], v[52:55], v[96:99]\n"
        "s_waitcnt vmcnt(26)\n"
        "v_add3_u32 v6, v56, v57, v6\n"
        "v_mfma_f32_16x16x32_bf16 v[100:103], v[168:171], v[52:55], v[100:103]\n"
        "v_mfma_f32_16x16x32_bf16 v[104:107], v[172:175], v[52:55], v[104:107]\n"
        "s_waitcnt vmcnt(25)\n"
        "v_mfma_f32_16x16x32_bf16 v[88:91], v[176:179], v[12:15], v[88:91]\n"
        "v_add3_u32 v1, v12, v13, s58\n"
        "v_mfma_f32_16x16x32_bf16 v[92:95], v[180:183], v[12:15], v[92:95]\n"
        "v_add3_u32 v1, v14, v15, v1\n"
        "v_mfma_f32_16x16x32_bf16 v[96:99], v[184:187], v[12:15], v[96:99]\n"
        "v_mfma_f32_16x16x32_bf16 v[100:103], v[188:191], v[12:15], v[100:103]\n"
        "v_mfma_f32_16x16x32_bf16 v[104:107], v[192:195], v[12:15], v[104:107]\n"
        "s_waitcnt vmcnt(24)\n"
        "v_mfma_f32_16x16x32_bf16 v[88:91], v[196:199], v[16:19], v[88:91]\n"
        "v_add3_u32 v1, v16, v17, v1\n"
        "v_mfma_f32_16x16x32_bf16 v[92:95], v[200:203], v[16:19], v[92:95]\n"
        "v_add3_u32 v1, v18, v19, v1\n"
        "v_mfma_f32_16x16x32_bf16 v[96:99], v[204:207], v[16:19], v[96:99]\n"
        "v_mfma_f32_16x16x32_bf16 v[100:103], v[208:211], v[16:19], v[100:103]\n"
        "v_mfma_f32_16x16x32_bf16 v[104:107], v[212:215], v[16:19], v[104:107]\n"
        "s_waitcnt vmcnt(17)\n"
        "v_mfma_f32_16x16x32_bf16 v[88:91], v[216:219], v[20:23], v[88:91]\n"
        "v_add3_u32 v1, v20, v21, v1\n"
        "s_waitcnt vmcnt(16)\n"
        "v_mfma_f32_16x16x32_bf16 v[92:95], v[220:223], v[20:23], v[92:95]\n"
        "v_add3_u32 v1, v22, v23, v1\n"
        "s_barrier\n"
        "s_setprio 1\n"
        "s_waitcnt vmcnt(15)\n"
        "v_mfma_f32_16x16x32_bf16 v[96:99], v[224:227], v[20:23], v[96:99]\n"
        "v_add3_u32 v1, v24, v25, v1\n"
        "v_xor_b32_e32 v6, v6, v58\n"
        "ds_read_b128 v[108:111], v7 offset:20480\n"
        "s_waitcnt vmcnt(14)\n"
        "v_mfma_f32_16x16x32_bf16 v[100:103], v[228:231], v[20:23], v[100:103]\n"
        "v_bitop3_b32 v6, v1, v26, v6 bitop3:0xbe\n"
        "v_mov_b32_e32 v58, v24\n"
        "ds_read_b128 v[112:115], v7 offset:24576\n"
        "s_waitcnt vmcnt(13)\n"
        "v_mfma_f32_16x16x32_bf16 v[104:107], v[232:235], v[20:23], v[104:107]\n"
        "v_mov_b32_e32 v59, v25\n"
        "ds_read_b128 v[76:79], v7 offset:28672\n"
        "s_waitcnt vmcnt(12)\n"
        "v_mfma_f32_16x16x32_bf16 v[88:91], v[236:239], v[56:59], v[88:91]\n"
        "ds_read_b128 v[80:83], v7 offset:32768\n"
        "s_waitcnt vmcnt(11)\n"
        "v_mfma_f32_16x16x32_bf16 v[92:95], v[240:243], v[56:59], v[92:95]\n"
        "v_cmp_ne_u32_e32 vcc, 0, v6\n"
        "ds_read_b128 v[84:87], v7 offset:36864\n"
        "s_waitcnt vmcnt(10)\n"
        "v_mfma_f32_16x16x32_bf16 v[96:99], v[244:247], v[56:59], v[96:99]\n"
        "s_waitcnt vmcnt(9)\n"
        "v_mfma_f32_16x16x32_bf16 v[100:103], v[248:251], v[56:59], v[100:103]\n"
        "s_waitcnt vmcnt(8)\n"
        "v_mfma_f32_16x16x32_bf16 v[104:107], v[252:255], v[56:59], v[104:107]\n"
        "s_cbranch_vccnz .Lk141\n"
        ".Lk142:\n"
        "s_waitcnt vmcnt(7) lgkmcnt(4)\n"
        "v_mfma_f32_16x16x32_bf16 v[108:111], v[116:119], v[28:31], v[108:111]\n"
        "v_add3_u32 v6, v28, v29, s58\n"
        "s_waitcnt lgkmcnt(3)\n"
        "v_mfma_f32_16x16x32_bf16 v[112:115], v[120:123], v[28:31], v[112:115]\n"
        "v_add3_u32 v6, v30, v31, v6\n"
        "s_waitcnt lgkmcnt(2)\n"
        "v_mfma_f32_16x16x32_bf16 v[76:79], v[124:127], v[28:31], v[76:79]\n"
        "s_waitcnt lgkmcnt(1)\n"
        "v_mfma_f32_16x16x32_bf16 v[80:83], v[128:131], v[28:31], v[80:83]\n"
        "s_waitcnt lgkmcnt(0)\n"
        "v_mfma_f32_16x16x32_bf16 v[84:87], v[132:135], v[28:31], v[84:87]\n"
        "s_waitcnt vmcnt(6)\n"
        "v_mfma_f32_16x16x32_bf16 v[108:111], v[136:139], v[32:35], v[108:111]\n"
        "v_add3_u32 v6, v32, v33, v6\n"
        "v_mfma_f32_16x16x32_bf16 v[112:115], v[140:143], v[32:35], v[112:115]\n"
        "v_mfma_f32_16x16x32_bf16 v[76:79], v[144:147], v[32:35], v[76:79]\n"
        "v_add3_u32 v6, v34, v35, v6\n"
        "v_mfma_f32_16x16x32_bf16 v[80:83], v[148:151], v[32:35], v[80:83]\n"
        "v_mfma_f32_16x16x32_bf16 v[84:87], v[152:155], v[32:35], v[84:87]\n"
        "s_waitcnt vmcnt(5)\n"
        "v_mfma_f32_16x16x32_bf16 v[108:111], v[156:159], v[36:39], v[108:111]\n"
        "v_mfma_f32_16x16x32_bf16 v[112:115], v[160:163], v[36:39], v[112:115]\n"
        "v_add3_u32 v6, v36, v37, v6\n"
        "v_add3_u32 v6, v38, v39, v6\n"
        "v_mfma_f32_16x16x32_bf16 v[76:79], v[164:167], v[36:39], v[76:79]\n"
        "s_waitcnt vmcnt(4)\n"
        "v_add3_u32 v6, v40, v41, v6\n"
        "v_mfma_f32_16x16x32_bf16 v[80:83], v[168:171], v[36:39], v[80:83]\n"
        "v_mfma_f32_16x16x32_bf16 v[84:87], v[172:175], v[36:39], v[84:87]\n"
        "s_waitcnt vmcnt(3)\n"
        "v_mfma_f32_16x16x32_bf16 v[108:111], v[176:179], v[60:63], v[108:111]\n"
        "v_add3_u32 v1, v60, v61, s58\n"
        "v_mfma_f32_16x16x32_bf16 v[112:115], v[180:183], v[60:63], v[112:115]\n"
        "v_add3_u32 v1, v62, v63, v1\n"
        "v_mfma_f32_16x16x32_bf16 v[76:79], v[184:187], v[60:63], v[76:79]\n"
        "v_mfma_f32_16x16x32_bf16 v[80:83], v[188:191], v[60:63], v[80:83]\n"
        "v_mfma_f32_16x16x32_bf16 v[84:87], v[192:195], v[60:63], v[84:87]\n"
        "s_waitcnt vmcnt(2)\n"
        "v_mfma_f32_16x16x32_bf16 v[108:111], v[196:199], v[64:67], v[108:111]\n"
        "v_add3_u32 v1, v64, v65, v1\n"
        "s_add_u32 s3, s48, 0x3c000\n"
        "s_add_u32 s44, s3, 0xf00\n"
        "buffer_load_dwordx4 v[44:47], v3, s[12:15], s3 offen sc1\n"
        "v_mfma_f32_16x16x32_bf16 v[112:115], v[200:203], v[64:67], v[112:115]\n"
        "v_add3_u32 v1, v66, v67, v1\n"
        "v_mfma_f32_16x16x32_bf16 v[76:79], v[204:207], v[64:67], v[76:79]\n"
        "v_mfma_f32_16x16x32_bf16 v[80:83], v[208:211], v[64:67], v[80:83]\n"
        "v_mfma_f32_16x16x32_bf16 v[84:87], v[212:215], v[64:67], v[84:87]\n"
        "buffer_load_dwordx4 v[48:51], v3, s[12:15], s3 offen offset:1024 sc1\n"
        "s_waitcnt vmcnt(3)\n"
        "v_mfma_f32_16x16x32_bf16 v[108:111], v[216:219], v[68:71], v[108:111]\n"
        "v_add3_u32 v1, v68, v69, v1\n"
        "v_mfma_f32_16x16x32_bf16 v[112:115], v[220:223], v[68:71], v[112:115]\n"
        "v_add3_u32 v1, v70, v71, v1\n"
        "v_mfma_f32_16x16x32_bf16 v[76:79], v[224:227], v[68:71], v[76:79]\n"
        "s_waitcnt vmcnt(2)\n"
        "v_add3_u32 v1, v72, v73, v1\n"
        "v_xor_b32_e32 v6, v6, v42\n"
        "v_mfma_f32_16x16x32_bf16 v[80:83], v[228:231], v[68:71], v[80:83]\n"
        "v_bitop3_b32 v6, v1, v74, v6 bitop3:0xbe\n"
        "v_mov_b32_e32 v42, v72\n"
        "buffer_load_dwordx4 v[52:55], v3, s[12:15], s3 offen offset:2048 sc1\n"
        "v_mfma_f32_16x16x32_bf16 v[84:87], v[232:235], v[68:71], v[84:87]\n"
        "v_mov_b32_e32 v43, v73\n"
        "s_nop 1\n"
        "v_mfma_f32_16x16x32_bf16 v[108:111], v[236:239], v[40:43], v[108:111]\n"
        "v_mfma_f32_16x16x32_bf16 v[112:115], v[240:243], v[40:43], v[112:115]\n"
        "v_cmp_ne_u32_e32 vcc, 0, v6\n"
        "v_mfma_f32_16x16x32_bf16 v[76:79], v[244:247], v[40:43], v[76:79]\n"
        "buffer_load_dwordx3 v[56:58], v4, s[12:15], s3 offen offset:3072 sc1\n"
        "v_mfma_f32_16x16x32_bf16 v[80:83], v[248:251], v[40:43], v[80:83]\n"
        "v_mfma_f32_16x16x32_bf16 v[84:87], v[252:255], v[40:43], v[84:87]\n"
        "s_cbranch_vccnz .Lk144\n"
        ".Lk145:\n"
        "buffer_load_dwordx4 v[12:15], v3, s[12:15], s44 offen sc1\n"
        "buffer_load_dwordx4 v[16:19], v3, s[12:15], s44 offen offset:1024 sc1\n"
        "buffer_load_dwordx4 v[20:23], v3, s[12:15], s44 offen offset:2048 sc1\n"
        "ds_write_b128 v7, v[88:91]\n"
        "ds_write_b128 v7, v[92:95] offset:4096\n"
        "buffer_load_dwordx3 v[24:26], v4, s[12:15], s44 offen offset:3072 sc1\n"
        "ds_write_b128 v7, v[96:99] offset:8192\n"
        "ds_write_b128 v7, v[100:103] offset:12288\n"
        "s_add_u32 s3, s48, 0x4b000\n"
        "s_add_u32 s44, s3, 0xf00\n"
        "buffer_load_dwordx4 v[28:31], v3, s[12:15], s3 offen sc1\n"
        "ds_write_b128 v7, v[104:107] offset:16384\n"
        "buffer_load_dwordx4 v[32:35], v3, s[12:15], s3 offen offset:1024 sc1\n"
        "ds_write_b128 v7, v[108:111] offset:20480\n"
        "buffer_load_dwordx4 v[36:39], v3, s[12:15], s3 offen offset:2048 sc1\n"
        "ds_write_b128 v7, v[112:115] offset:24576\n"
        "buffer_load_dwordx3 v[40:42], v4, s[12:15], s3 offen offset:3072 sc1\n"
        "ds_write_b128 v7, v[76:79] offset:28672\n"
        "buffer_load_dwordx4 v[60:63], v3, s[12:15], s44 offen sc1\n"
        "ds_write_b128 v7, v[80:83] offset:32768\n"
        "buffer_load_dwordx4 v[64:67], v3, s[12:15], s44 offen offset:1024 sc1\n"
        "ds_write_b128 v7, v[84:87] offset:36864\n"
        "v_add_u32_e32 v7, 0xa000, v7\n"
        "s_setprio 0\n"
        "s_waitcnt lgkmcnt(0)\n"
        "s_barrier\n"
        "ds_read_b128 v[88:91], v7\n"
        "ds_read_b128 v[92:95], v7 offset:4096\n"
        "ds_read_b128 v[96:99], v7 offset:8192\n"
        "ds_read_b128 v[100:103], v7 offset:12288\n"
        "ds_read_b128 v[104:107], v7 offset:16384\n"
        "buffer_load_dwordx4 v[68:71], v3, s[12:15], s44 offen offset:2048 sc1\n"
        "buffer_load_dwordx3 v[72:74], v4, s[12:15], s44 offen offset:3072 sc1\n"
        "s_waitcnt vmcnt(15) lgkmcnt(4)\n"
        "v_mfma_f32_16x16x32_bf16 v[88:91], v[116:119], v[44:47], v[88:91]\n"
        "v_add3_u32 v6, v44, v45, s58\n"
        "s_waitcnt lgkmcnt(3)\n"
        "v_mfma_f32_16x16x32_bf16 v[92:95], v[120:123], v[44:47], v[92:95]\n"
        "v_add3_u32 v6, v46, v47, v6\n"
        "s_waitcnt lgkmcnt(2)\n"
        "v_mfma_f32_16x16x32_bf16 v[96:99], v[124:127], v[44:47], v[96:99]\n"
        "s_waitcnt lgkmcnt(1)\n"
        "v_mfma_f32_16x16x32_bf16 v[100:103], v[128:131], v[44:47], v[100:103]\n"
        "s_waitcnt lgkmcnt(0)\n"
        "v_mfma_f32_16x16x32_bf16 v[104:107], v[132:135], v[44:47], v[104:107]\n"
        "s_waitcnt vmcnt(14)\n"
        "v_mfma_f32_16x16x32_bf16 v[88:91], v[136:139], v[48:51], v[88:91]\n"
        "v_add3_u32 v6, v48, v49, v6\n"
        "v_mfma_f32_16x16x32_bf16 v[92:95], v[140:143], v[48:51], v[92:95]\n"
        "v_mfma_f32_16x16x32_bf16 v[96:99], v[144:147], v[48:51], v[96:99]\n"
        "v_add3_u32 v6, v50, v51, v6\n"
        "v_mfma_f32_16x16x32_bf16 v[100:103], v[148:151], v[48:51], v[100:103]\n"
        "v_mfma_f32_16x16x32_bf16 v[104:107], v[152:155], v[48:51], v[104:107]\n"
        "s_waitcnt vmcnt(13)\n"
        "v_mfma_f32_16x16x32_bf16 v[88:91], v[156:159], v[52:55], v[88:91]\n"
        "v_mfma_f32_16x16x32_bf16 v[92:95], v[160:163], v[52:55], v[92:95]\n"
        "v_add3_u32 v6, v52, v53, v6\n"
        "v_add3_u32 v6, v54, v55, v6\n"
        "v_mfma_f32_16x16x32_bf16 v[96:99], v[164:167], v[52:55], v[96:99]\n"
        "s_waitcnt vmcnt(12)\n"
        "v_add3_u32 v6, v56, v57, v6\n"
        "v_mfma_f32_16x16x32_bf16 v[100:103], v[168:171], v[52:55], v[100:103]\n"
        "v_mfma_f32_16x16x32_bf16 v[104:107], v[172:175], v[52:55], v[104:107]\n"
        "s_waitcnt vmcnt(11)\n"
        "v_mfma_f32_16x16x32_bf16 v[88:91], v[176:179], v[12:15], v[88:91]\n"
        "v_add3_u32 v1, v12, v13, s58\n"
        "v_mfma_f32_16x16x32_bf16 v[92:95], v[180:183], v[12:15], v[92:95]\n"
        "v_add3_u32 v1, v14, v15, v1\n"
        "v_mfma_f32_16x16x32_bf16 v[96:99], v[184:187], v[12:15], v[96:99]\n"
        "v_mfma_f32_16x16x32_bf16 v[100:103], v[188:191], v[12:15], v[100:103]\n"
        "v_mfma_f32_16x16x32_bf16 v[104:107], v[192:195], v[12:15], v[104:107]\n"
        "s_waitcnt vmcnt(10)\n"
        "v_mfma_f32_16x16x32_bf16 v[88:91], v[196:199], v[16:19], v[88:91]\n"
        "v_add3_u32 v1, v16, v17, v1\n"
        "v_mfma_f32_16x16x32_bf16 v[92:95], v[200:203], v[16:19], v[92:95]\n"
        "v_add3_u32 v1, v18, v19, v1\n"
        "v_mfma_f32_16x16x32_bf16 v[96:99], v[204:207], v[16:19], v[96:99]\n"
        "v_mfma_f32_16x16x32_bf16 v[100:103], v[208:211], v[16:19], v[100:103]\n"
        "v_mfma_f32_16x16x32_bf16 v[104:107], v[212:215], v[16:19], v[104:107]\n"
        "s_waitcnt vmcnt(9)\n"
        "v_mfma_f32_16x16x32_bf16 v[88:91], v[216:219], v[20:23], v[88:91]\n"
        "v_add3_u32 v1, v20, v21, v1\n"
        "v_mfma_f32_16x16x32_bf16 v[92:95], v[220:223], v[20:23], v[92:95]\n"
        "v_add3_u32 v1, v22, v23, v1\n"
        "s_barrier\n"
        "s_setprio 1\n"
        "v_mfma_f32_16x16x32_bf16 v[96:99], v[224:227], v[20:23], v[96:99]\n"
        "s_waitcnt vmcnt(8)\n"
        "v_add3_u32 v1, v24, v25, v1\n"
        "v_xor_b32_e32 v6, v6, v58\n"
        "ds_read_b128 v[108:111], v7 offset:20480\n"
        "v_mfma_f32_16x16x32_bf16 v[100:103], v[228:231], v[20:23], v[100:103]\n"
        "v_bitop3_b32 v6, v1, v26, v6 bitop3:0xbe\n"
        "v_mov_b32_e32 v58, v24\n"
        "ds_read_b128 v[112:115], v7 offset:24576\n"
        "v_mfma_f32_16x16x32_bf16 v[104:107], v[232:235], v[20:23], v[104:107]\n"
        "v_mov_b32_e32 v59, v25\n"
        "ds_read_b128 v[76:79], v7 offset:28672\n"
        "s_nop 0\n"
        "v_mfma_f32_16x16x32_bf16 v[88:91], v[236:239], v[56:59], v[88:91]\n"
        "ds_read_b128 v[80:83], v7 offset:32768\n"
        "v_mfma_f32_16x16x32_bf16 v[92:95], v[240:243], v[56:59], v[92:95]\n"
        "v_cmp_ne_u32_e32 vcc, 0, v6\n"
        "ds_read_b128 v[84:87], v7 offset:36864\n"
        "v_mfma_f32_16x16x32_bf16 v[96:99], v[244:247], v[56:59], v[96:99]\n"
        "v_mfma_f32_16x16x32_bf16 v[100:103], v[248:251], v[56:59], v[100:103]\n"
        "v_mfma_f32_16x16x32_bf16 v[104:107], v[252:255], v[56:59], v[104:107]\n"
        "s_cbranch_vccnz .Lk147\n"
        ".Lk148:\n"
        "s_waitcnt vmcnt(7) lgkmcnt(4)\n"
        "v_mfma_f32_16x16x32_bf16 v[108:111], v[116:119], v[28:31], v[108:111]\n"
        "v_add3_u32 v6, v28, v29, s58\n"
        "s_waitcnt lgkmcnt(3)\n"
        "v_mfma_f32_16x16x32_bf16 v[112:115], v[120:123], v[28:31], v[112:115]\n"
        "v_add3_u32 v6, v30, v31, v6\n"
        "buffer_load_dwordx2 v[52:53], v11, s[12:15], s51 offen sc1\n"
        "s_waitcnt lgkmcnt(2)\n"
        "v_mfma_f32_16x16x32_bf16 v[76:79], v[124:127], v[28:31], v[76:79]\n"
        "ds_write_b128 v7, v[88:91]\n"
        "s_waitcnt lgkmcnt(2)\n"
        "v_mfma_f32_16x16x32_bf16 v[80:83], v[128:131], v[28:31], v[80:83]\n"
        "s_waitcnt vmcnt(7)\n"
        "v_add3_u32 v6, v32, v33, v6\n"
        "s_waitcnt lgkmcnt(1)\n"
        "v_mfma_f32_16x16x32_bf16 v[84:87], v[132:135], v[28:31], v[84:87]\n"
        "v_add3_u32 v6, v34, v35, v6\n"
        "buffer_load_dwordx2 v[12:13], v10, s[12:15], s55 offen sc1\n"
        "v_mfma_f32_16x16x32_bf16 v[108:111], v[136:139], v[32:35], v[108:111]\n"
        "s_waitcnt vmcnt(7)\n"
        "v_add3_u32 v6, v36, v37, v6\n"
        "v_mfma_f32_16x16x32_bf16 v[112:115], v[140:143], v[32:35], v[112:115]\n"
        "ds_write_b128 v7, v[92:95] offset:4096\n"
        "v_mfma_f32_16x16x32_bf16 v[76:79], v[144:147], v[32:35], v[76:79]\n"
        "v_add3_u32 v6, v38, v39, v6\n"
        "v_mfma_f32_16x16x32_bf16 v[80:83], v[148:151], v[32:35], v[80:83]\n"
        "s_waitcnt vmcnt(6)\n"
        "v_add3_u32 v6, v40, v41, v6\n"
        "v_mfma_f32_16x16x32_bf16 v[84:87], v[152:155], v[32:35], v[84:87]\n"
        "s_waitcnt vmcnt(5)\n"
        "v_add3_u32 v1, v60, v61, s58\n"
        "v_add3_u32 v1, v62, v63, v1\n"
        "v_mfma_f32_16x16x32_bf16 v[108:111], v[156:159], v[36:39], v[108:111]\n"
        "ds_write_b128 v7, v[96:99] offset:8192\n"
        "v_mfma_f32_16x16x32_bf16 v[112:115], v[160:163], v[36:39], v[112:115]\n"
        "s_waitcnt vmcnt(4)\n"
        "v_add3_u32 v1, v64, v65, v1\n"
        "v_add3_u32 v1, v66, v67, v1\n"
        "v_mfma_f32_16x16x32_bf16 v[76:79], v[164:167], v[36:39], v[76:79]\n"
        "s_waitcnt vmcnt(3)\n"
        "v_add3_u32 v1, v68, v69, v1\n"
        "v_add3_u32 v1, v70, v71, v1\n"
        "v_mfma_f32_16x16x32_bf16 v[80:83], v[168:171], v[36:39], v[80:83]\n"
        "s_waitcnt vmcnt(2)\n"
        "v_add3_u32 v1, v72, v73, v1\n"
        "v_xor_b32_e32 v6, v6, v42\n"
        "v_mfma_f32_16x16x32_bf16 v[84:87], v[172:175], v[36:39], v[84:87]\n"
        "ds_write_b128 v7, v[100:103] offset:12288\n"
        "v_mfma_f32_16x16x32_bf16 v[108:111], v[176:179], v[60:63], v[108:111]\n"
        "v_bitop3_b32 v6, v1, v74, v6 bitop3:0xbe\n"
        "v_mov_b32_e32 v42, v72\n"
        "v_mfma_f32_16x16x32_bf16 v[108:111], v[196:199], v[64:67], v[108:111]\n"
        "v_mov_b32_e32 v43, v73\n"
        "v_mfma_f32_16x16x32_bf16 v[108:111], v[216:219], v[68:71], v[108:111]\n"
        "v_cmp_ne_u32_e32 vcc, 0, v6\n"
        "v_mfma_f32_16x16x32_bf16 v[108:111], v[236:239], v[40:43], v[108:111]\n"
        "ds_write_b128 v7, v[104:107] offset:16384\n"
        "v_mfma_f32_16x16x32_bf16 v[112:115], v[180:183], v[60:63], v[112:115]\n"
        "v_mfma_f32_16x16x32_bf16 v[112:115], v[200:203], v[64:67], v[112:115]\n"
        "s_cbranch_vccnz .Lk150\n"
        ".Lk151:\n"
        "v_mfma_f32_16x16x32_bf16 v[112:115], v[220:223], v[68:71], v[112:115]\n"
        "v_mfma_f32_16x16x32_bf16 v[112:115], v[240:243], v[40:43], v[112:115]\n"
        "ds_write_b128 v7, v[108:111] offset:20480\n"
        "v_mfma_f32_16x16x32_bf16 v[76:79], v[184:187], v[60:63], v[76:79]\n"
        "v_mfma_f32_16x16x32_bf16 v[76:79], v[204:207], v[64:67], v[76:79]\n"
        "v_mfma_f32_16x16x32_bf16 v[76:79], v[224:227], v[68:71], v[76:79]\n"
        "v_mfma_f32_16x16x32_bf16 v[76:79], v[244:247], v[40:43], v[76:79]\n"
        "ds_write_b128 v7, v[112:115] offset:24576\n"
        "v_mfma_f32_16x16x32_bf16 v[80:83], v[188:191], v[60:63], v[80:83]\n"
        "v_mfma_f32_16x16x32_bf16 v[80:83], v[208:211], v[64:67], v[80:83]\n"
        "v_mfma_f32_16x16x32_bf16 v[80:83], v[228:231], v[68:71], v[80:83]\n"
        "v_mfma_f32_16x16x32_bf16 v[80:83], v[248:251], v[40:43], v[80:83]\n"
        "ds_write_b128 v7, v[76:79] offset:28672\n"
        "v_mfma_f32_16x16x32_bf16 v[84:87], v[192:195], v[60:63], v[84:87]\n"
        "v_mfma_f32_16x16x32_bf16 v[84:87], v[212:215], v[64:67], v[84:87]\n"
        "v_mfma_f32_16x16x32_bf16 v[84:87], v[232:235], v[68:71], v[84:87]\n"
        "v_mfma_f32_16x16x32_bf16 v[84:87], v[252:255], v[40:43], v[84:87]\n"
        "ds_write_b128 v7, v[80:83] offset:32768\n"
        "s_nop 6\n"
        "ds_write_b128 v7, v[84:87] offset:36864\n"
        "s_bfe_u32 s42, s33, 0x10001\n"
        "s_mul_i32 s42, s42, 0x5000\n"
        "s_add_u32 s42, s42, 0xa000\n"
        "v_add_u32_e32 v7, s42, v3\n"
        "s_waitcnt lgkmcnt(0)\n"
        "s_barrier\n"
        "s_bitcmp0_b32 s33, 0\n"
        "s_cbranch_scc0 .Lk153\n"
        "ds_read_b128 v[116:119], v7 offset:16384\n"
        "ds_read_b128 v[120:123], v7 offset:17408\n"
        "ds_read_b128 v[124:127], v7 offset:18432\n"
        "ds_read_b128 v[128:131], v7 offset:19456\n"
        "s_waitcnt vmcnt(1)\n"
        "v_lshlrev_b32_e32 v148, 16, v52\n"
        "v_and_b32_e32 v149, 0xffff0000, v52\n"
        "v_lshlrev_b32_e32 v150, 16, v53\n"
        "v_and_b32_e32 v151, 0xffff0000, v53\n"
        "s_waitcnt vmcnt(0)\n"
        "v_fma_f32 v148, v148, v12, v13\n"
        "v_fma_f32 v149, v149, v12, v13\n"
        "v_fma_f32 v150, v150, v12, v13\n"
        "v_fma_f32 v151, v151, v12, v13\n"
        "s_waitcnt lgkmcnt(2)\n"
        "v_add_f32_e32 v116, v116, v120\n"
        "v_add_f32_e32 v117, v117, v121\n"
        "s_waitcnt lgkmcnt(0)\n"
        "v_add_f32_e32 v124, v124, v128\n"
        "v_add_f32_e32 v125, v125, v129\n"
        "v_add_f32_e32 v118, v118, v122\n"
        "v_add_f32_e32 v119, v119, v123\n"
        "v_add_f32_e32 v126, v126, v130\n"
        "v_add_f32_e32 v127, v127, v131\n"
        "v_add_f32_e32 v116, v116, v124\n"
        "v_add_f32_e32 v117, v117, v125\n"
        "v_add_f32_e32 v118, v118, v126\n"
        "v_add_f32_e32 v119, v119, v127\n"
        "v_fmamk_f32 v148, v116, 0xbf317218, v148\n"
        "v_fmamk_f32 v149, v117, 0xbf317218, v149\n"
        "v_fmamk_f32 v150, v118, 0xbf317218, v150\n"
        "v_fmamk_f32 v151, v119, 0xbf317218, v151\n"
        "v_cvt_pk_bf16_f32 v148, v148, v149\n"
        "v_cvt_pk_bf16_f32 v149, v150, v151\n"
        "buffer_store_dwordx2 v[148:149], v9, s[28:31], 0 offen\n"
        ".Lk153:\n"
        ".Lk140:\n"
        "s_endpgm\n"
        ".Lk46:\n"
        "s_sleep 4\n"
        "s_nop 7\n"
        "buffer_load_dwordx2 v[46:47], v42, s[12:15], s49 offen sc1\n"
        "s_waitcnt vmcnt(0)\n"
        "v_xor_b32_e32 v6, s39, v46\n"
        "v_cmp_ne_u32_e32 vcc, 0, v6\n"
        "s_waitcnt vmcnt(0)\n"
        "s_nop 1\n"
        "s_cbranch_vccz .Lk47\n"
        "s_sub_u32 s38, s38, 1\n"
        "s_cbranch_scc0 .Lk46\n"
        "s_mov_b32 s38, 0\n"
        "s_branch .Lk47\n"
        ".Lk50:\n"
        "s_sleep 4\n"
        "s_nop 7\n"
        "buffer_load_dwordx4 v[72:75], v86, s[12:15], s46 offen sc1\n"
        "buffer_load_dwordx4 v[76:79], v86, s[12:15], s47 offen sc1\n"
        "s_waitcnt vmcnt(1)\n"
        "v_max3_u32 v6, v72, v73, v74\n"
        "s_waitcnt vmcnt(0)\n"
        "v_max3_u32 v6, v6, v75, v76\n"
        "v_max3_u32 v6, v6, v77, v78\n"
        "v_max_u32_e32 v6, v6, v79\n"
        "v_cmp_eq_u32_e32 vcc, -1, v6\n"
        "s_waitcnt vmcnt(0)\n"
        "s_nop 1\n"
        "s_cbranch_vccz .Lk51\n"
        "s_sub_u32 s38, s38, 1\n"
        "s_cbranch_scc0 .Lk50\n"
        "s_mov_b32 s38, 0\n"
        "s_branch .Lk51\n"
        ".Lk56:\n"
        "s_sleep 8\n"
        "s_nop 7\n"
        "s_lshl_b32 s48, s34, 12\n"
        "s_add_u32 s48, s48, 0x58000\n"
        "v_min_u32_e32 v82, 0x350, v3\n"
        "buffer_load_dwordx2 v[18:19], v82, s[12:15], s48 offen sc1\n"
        "buffer_load_dwordx2 v[20:21], v82, s[12:15], s48 offen offset:848 sc1\n"
        "buffer_load_dwordx2 v[22:23], v82, s[12:15], s48 offen offset:1696 sc1\n"
        "s_waitcnt vmcnt(2)\n"
        "v_xor_b32_e32 v6, s39, v18\n"
        "s_waitcnt vmcnt(1)\n"
        "v_bitop3_b32 v6, s39, v20, v6 bitop3:0xbe\n"
        "s_waitcnt vmcnt(0)\n"
        "v_bitop3_b32 v6, s39, v22, v6 bitop3:0xbe\n"
        "v_cmp_ne_u32_e32 vcc, 0, v6\n"
        "s_waitcnt vmcnt(0)\n"
        "s_nop 1\n"
        "s_cbranch_vccz .Lk57\n"
        "s_sub_u32 s38, s38, 1\n"
        "s_cbranch_scc0 .Lk56\n"
        "s_mov_b32 s38, 0\n"
        "s_branch .Lk57\n"
        ".Lk71:\n"
        "s_sleep 8\n"
        "s_nop 7\n"
        "buffer_load_dwordx2 v[18:19], off, s[12:15], s47 sc1\n"
        "s_waitcnt vmcnt(0)\n"
        "v_xor_b32_e32 v6, s39, v18\n"
        "v_cmp_ne_u32_e32 vcc, 0, v6\n"
        "s_waitcnt vmcnt(0)\n"
        "s_nop 1\n"
        "s_cbranch_vccz .Lk72\n"
        "s_sub_u32 s38, s38, 1\n"
        "s_cbranch_scc0 .Lk71\n"
        "s_mov_b32 s38, 0\n"
        "s_branch .Lk72\n"
        ".Lk75:\n"
        "s_sleep 8\n"
        "s_nop 7\n"
        "s_lshl_b32 s48, s34, 12\n"
        "s_add_u32 s48, s48, 0x58000\n"
        "v_min_u32_e32 v82, 0x350, v3\n"
        "buffer_load_dwordx2 v[18:19], v82, s[12:15], s48 offen sc1\n"
        "buffer_load_dwordx2 v[20:21], v82, s[12:15], s48 offen offset:848 sc1\n"
        "buffer_load_dwordx2 v[22:23], v82, s[12:15], s48 offen offset:1696 sc1\n"
        "s_waitcnt vmcnt(2)\n"
        "v_xor_b32_e32 v6, s39, v18\n"
        "s_waitcnt vmcnt(1)\n"
        "v_bitop3_b32 v6, s39, v20, v6 bitop3:0xbe\n"
        "s_waitcnt vmcnt(0)\n"
        "v_bitop3_b32 v6, s39, v22, v6 bitop3:0xbe\n"
        "v_cmp_ne_u32_e32 vcc, 0, v6\n"
        "s_waitcnt vmcnt(0)\n"
        "s_nop 1\n"
        "s_cbranch_vccz .Lk76\n"
        "s_sub_u32 s38, s38, 1\n"
        "s_cbranch_scc0 .Lk75\n"
        "s_mov_b32 s38, 0\n"
        "s_branch .Lk76\n"
        ".Lk91:\n"
        "s_sleep 4\n"
        "s_nop 7\n"
        "buffer_load_dword v72, v12, s[12:15], s63 offen sc1\n"
        "buffer_load_dword v73, v12, s[12:15], s3 offen sc1\n"
        "buffer_load_dword v74, v12, s[12:15], s44 offen sc1\n"
        "buffer_load_dword v75, v12, s[12:15], s45 offen sc1\n"
        "s_waitcnt vmcnt(1)\n"
        "v_max3_u32 v6, v72, v73, v74\n"
        "s_waitcnt vmcnt(0)\n"
        "v_max_u32_e32 v6, v6, v75\n"
        "v_cmp_eq_u32_e32 vcc, -1, v6\n"
        "s_waitcnt vmcnt(0)\n"
        "s_nop 1\n"
        "s_cbranch_vccz .Lk92\n"
        "s_sub_u32 s38, s38, 1\n"
        "s_cbranch_scc0 .Lk91\n"
        "s_mov_b32 s38, 0\n"
        "s_branch .Lk92\n"
        ".Lk94:\n"
        "s_sleep 4\n"
        "s_nop 7\n"
        "buffer_load_dword v76, v45, s[12:15], s63 offen sc1\n"
        "buffer_load_dword v77, v45, s[12:15], s3 offen sc1\n"
        "buffer_load_dword v78, v45, s[12:15], s44 offen sc1\n"
        "buffer_load_dword v79, v45, s[12:15], s45 offen sc1\n"
        "s_waitcnt vmcnt(1)\n"
        "v_max3_u32 v6, v76, v77, v78\n"
        "s_waitcnt vmcnt(0)\n"
        "v_max_u32_e32 v6, v6, v79\n"
        "v_cmp_eq_u32_e32 vcc, -1, v6\n"
        "s_waitcnt vmcnt(0)\n"
        "s_nop 1\n"
        "s_cbranch_vccz .Lk95\n"
        "s_sub_u32 s38, s38, 1\n"
        "s_cbranch_scc0 .Lk94\n"
        "s_mov_b32 s38, 0\n"
        "s_branch .Lk95\n"
        ".Lk97:\n"
        "s_sleep 4\n"
        "s_nop 7\n"
        "buffer_load_dword v80, v82, s[12:15], s48 offen sc1\n"
        "buffer_load_dword v81, v82, s[12:15], s48 offen offset:64 sc1\n"
        "s_waitcnt vmcnt(1)\n"
        "v_xor_b32_e32 v6, s39, v80\n"
        "s_waitcnt vmcnt(0)\n"
        "v_bitop3_b32 v6, s39, v81, v6 bitop3:0xbe\n"
        "v_cmp_ne_u32_e32 vcc, 0, v6\n"
        "s_waitcnt vmcnt(0)\n"
        "s_nop 1\n"
        "s_cbranch_vccz .Lk98\n"
        "s_sub_u32 s38, s38, 1\n"
        "s_cbranch_scc0 .Lk97\n"
        "s_mov_b32 s38, 0\n"
        "s_branch .Lk98\n"
        ".Lk111:\n"
        "s_sleep 4\n"
        "s_nop 7\n"
        ".Lk113:\n"
        "v_and_b32_e32 v108, 0xf0, v3\n"
        "buffer_load_dwordx3 v[112:114], v108, s[12:15], s63 offen offset:1024 sc1\n"
        "buffer_load_dwordx4 v[8:11], v3, s[12:15], s63 offen sc1\n"
        "s_waitcnt vmcnt(1)\n"
        "v_add3_u32 v6, v112, v113, s58\n"
        "v_cmp_ne_u32_e32 vcc, v6, v114\n"
        "s_waitcnt vmcnt(0)\n"
        "v_add3_u32 v7, v8, v9, v10\n"
        "s_lshl_b32 s42, s58, 2\n"
        "v_add_u32_e32 v7, v7, v11\n"
        "v_mov_b32_e32 v6, s42\n"
        "s_cbranch_vccz .Lk114\n"
        "s_sleep 8\n"
        "s_sub_u32 s38, s38, 1\n"
        "s_cbranch_scc0 .Lk113\n"
        "s_mov_b32 s38, 0\n"
        ".Lk114:\n"
        "v_and_b32_e32 v108, 0xf0, v3\n"
        "buffer_load_dwordx3 v[112:114], v108, s[12:15], s63 offen offset:1024 sc1\n"
        "buffer_load_dwordx4 v[8:11], v3, s[12:15], s63 offen sc1\n"
        "buffer_load_dwordx4 v[12:15], v3, s[12:15], s57 offen sc1\n"
        "buffer_load_dwordx4 v[16:19], v3, s[12:15], s57 offen offset:1024 sc1\n"
        "buffer_load_dwordx4 v[20:23], v3, s[12:15], s57 offen offset:2048 sc1\n"
        "buffer_load_dwordx4 v[24:27], v3, s[12:15], s57 offen offset:3072 sc1\n"
        "buffer_load_dwordx4 v[28:31], v3, s[12:15], s49 offen sc1\n"
        "buffer_load_dwordx4 v[32:35], v3, s[12:15], s49 offen offset:1024 sc1\n"
        "buffer_load_dwordx4 v[36:39], v3, s[12:15], s49 offen offset:2048 sc1\n"
        "buffer_load_dwordx4 v[40:43], v3, s[12:15], s49 offen offset:3072 sc1\n"
        "s_waitcnt lgkmcnt(0)\n"
        "ds_read_b128 v[60:63], v5 offset:50176\n"
        "ds_read_b128 v[64:67], v5 offset:51200\n"
        "ds_read_b128 v[68:71], v5 offset:52224\n"
        "ds_read_b128 v[72:75], v5 offset:53248\n"
        "ds_read_b128 v[76:79], v5 offset:54272\n"
        "ds_read_b128 v[80:83], v5 offset:55296\n"
        "ds_read_b128 v[84:87], v5 offset:56320\n"
        "s_waitcnt vmcnt(0) lgkmcnt(0)\n"
        "v_add3_u32 v6, v112, v113, s58\n"
        "v_cmp_ne_u32_e32 vcc, v6, v114\n"
        "v_add3_u32 v7, v8, v9, v10\n"
        "s_lshl_b32 s42, s58, 2\n"
        "v_add_u32_e32 v7, v7, v11\n"
        "v_mov_b32_e32 v6, s42\n"
        "s_waitcnt vmcnt(0)\n"
        "s_nop 1\n"
        "s_cbranch_vccz .Lk112\n"
        "s_sub_u32 s38, s38, 1\n"
        "s_cbranch_scc0 .Lk111\n"
        "s_mov_b32 s38, 0\n"
        "s_branch .Lk112\n"
        ".Lk115:\n"
        "s_waitcnt vmcnt(0) lgkmcnt(0)\n"
        "s_sub_u32 s38, s38, 8\n"
        "s_cbranch_scc0 .Lk111\n"
        "s_mov_b32 s38, 0\n"
        "s_branch .Lk116\n"
        ".Lk118:\n"
        "s_sleep 4\n"
        "s_nop 7\n"
        ".Lk120:\n"
        "v_and_b32_e32 v108, 0xf0, v3\n"
        "buffer_load_dwordx3 v[112:114], v108, s[12:15], s63 offen offset:1024 sc1\n"
        "buffer_load_dwordx4 v[8:11], v3, s[12:15], s63 offen sc1\n"
        "s_waitcnt vmcnt(1)\n"
        "v_add3_u32 v6, v112, v113, s58\n"
        "v_cmp_ne_u32_e32 vcc, v6, v114\n"
        "s_waitcnt vmcnt(0)\n"
        "v_add3_u32 v7, v8, v9, v10\n"
        "s_lshl_b32 s42, s58, 2\n"
        "v_add_u32_e32 v7, v7, v11\n"
        "v_mov_b32_e32 v6, s42\n"
        "s_cbranch_vccz .Lk121\n"
        "s_sleep 8\n"
        "s_sub_u32 s38, s38, 1\n"
        "s_cbranch_scc0 .Lk120\n"
        "s_mov_b32 s38, 0\n"
        ".Lk121:\n"
        "v_and_b32_e32 v108, 0xf0, v3\n"
        "buffer_load_dwordx3 v[112:114], v108, s[12:15], s63 offen offset:1024 sc1\n"
        "buffer_load_dwordx4 v[8:11], v3, s[12:15], s63 offen sc1\n"
        "buffer_load_dwordx4 v[12:15], v3, s[12:15], s57 offen sc1\n"
        "buffer_load_dwordx4 v[16:19], v3, s[12:15], s57 offen offset:1024 sc1\n"
        "buffer_load_dwordx4 v[20:23], v3, s[12:15], s57 offen offset:2048 sc1\n"
        "buffer_load_dwordx4 v[24:27], v3, s[12:15], s57 offen offset:3072 sc1\n"
        "buffer_load_dwordx4 v[28:31], v3, s[12:15], s49 offen sc1\n"
        "buffer_load_dwordx4 v[32:35], v3, s[12:15], s49 offen offset:1024 sc1\n"
        "buffer_load_dwordx4 v[36:39], v3, s[12:15], s49 offen offset:2048 sc1\n"
        "buffer_load_dwordx4 v[40:43], v3, s[12:15], s49 offen offset:3072 sc1\n"
        "buffer_load_dwordx4 v[44:47], v3, s[12:15], s59 offen sc1\n"
        "buffer_load_dwordx4 v[48:51], v3, s[12:15], s59 offen offset:1024 sc1\n"
        "buffer_load_dwordx4 v[52:55], v3, s[12:15], s59 offen offset:2048 sc1\n"
        "buffer_load_dwordx4 v[56:59], v3, s[12:15], s59 offen offset:3072 sc1\n"
        "s_waitcnt lgkmcnt(0)\n"
        "ds_read_b128 v[60:63], v5 offset:50176\n"
        "ds_read_b128 v[64:67], v5 offset:51200\n"
        "ds_read_b128 v[68:71], v5 offset:52224\n"
        "ds_read_b128 v[72:75], v5 offset:53248\n"
        "ds_read_b128 v[76:79], v5 offset:54272\n"
        "ds_read_b128 v[80:83], v5 offset:55296\n"
        "ds_read_b128 v[84:87], v5 offset:56320\n"
        "s_waitcnt vmcnt(0) lgkmcnt(0)\n"
        "v_add3_u32 v6, v112, v113, s58\n"
        "v_cmp_ne_u32_e32 vcc, v6, v114\n"
        "v_add3_u32 v7, v8, v9, v10\n"
        "s_lshl_b32 s42, s58, 2\n"
        "v_add_u32_e32 v7, v7, v11\n"
        "v_mov_b32_e32 v6, s42\n"
        "s_waitcnt vmcnt(0)\n"
        "s_nop 1\n"
        "s_cbranch_vccz .Lk119\n"
        "s_sub_u32 s38, s38, 1\n"
        "s_cbranch_scc0 .Lk118\n"
        "s_mov_b32 s38, 0\n"
        "s_branch .Lk119\n"
        ".Lk122:\n"
        "s_barrier\n"
        "s_mov_b32 s52, 1\n"
        "s_branch .Lk123\n"
        ".Lk124:\n"
        "s_waitcnt vmcnt(0) lgkmcnt(0)\n"
        "s_sub_u32 s38, s38, 8\n"
        "s_cbranch_scc0 .Lk118\n"
        "s_mov_b32 s38, 0\n"
        "s_branch .Lk125\n"
        ".Lk127:\n"
        "s_sleep 12\n"
        "s_nop 7\n"
        "s_add_u32 s3, s48, 0\n"
        "s_add_u32 s44, s3, 0xf00\n"
        "buffer_load_dwordx4 v[44:47], v3, s[12:15], s3 offen sc1\n"
        "buffer_load_dwordx4 v[48:51], v3, s[12:15], s3 offen offset:1024 sc1\n"
        "buffer_load_dwordx4 v[52:55], v3, s[12:15], s3 offen offset:2048 sc1\n"
        "buffer_load_dwordx3 v[56:58], v4, s[12:15], s3 offen offset:3072 sc1\n"
        "buffer_load_dwordx4 v[12:15], v3, s[12:15], s44 offen sc1\n"
        "buffer_load_dwordx4 v[16:19], v3, s[12:15], s44 offen offset:1024 sc1\n"
        "buffer_load_dwordx4 v[20:23], v3, s[12:15], s44 offen offset:2048 sc1\n"
        "buffer_load_dwordx3 v[24:26], v4, s[12:15], s44 offen offset:3072 sc1\n"
        "s_waitcnt vmcnt(7)\n"
        "v_add3_u32 v6, v44, v45, s58\n"
        "v_add3_u32 v6, v46, v47, v6\n"
        "s_waitcnt vmcnt(6)\n"
        "v_add3_u32 v6, v48, v49, v6\n"
        "v_add3_u32 v6, v50, v51, v6\n"
        "s_waitcnt vmcnt(5)\n"
        "v_add3_u32 v6, v52, v53, v6\n"
        "v_add3_u32 v6, v54, v55, v6\n"
        "s_waitcnt vmcnt(4)\n"
        "v_add3_u32 v6, v56, v57, v6\n"
        "s_waitcnt vmcnt(3)\n"
        "v_add3_u32 v1, v12, v13, s58\n"
        "v_add3_u32 v1, v14, v15, v1\n"
        "s_waitcnt vmcnt(2)\n"
        "v_add3_u32 v1, v16, v17, v1\n"
        "v_add3_u32 v1, v18, v19, v1\n"
        "s_waitcnt vmcnt(1)\n"
        "v_add3_u32 v1, v20, v21, v1\n"
        "v_add3_u32 v1, v22, v23, v1\n"
        "s_waitcnt vmcnt(0)\n"
        "v_add3_u32 v1, v24, v25, v1\n"
        "v_xor_b32_e32 v6, v6, v58\n"
        "v_bitop3_b32 v6, v1, v26, v6 bitop3:0xbe\n"
        "v_mov_b32_e32 v58, v24\n"
        "v_mov_b32_e32 v59, v25\n"
        "v_cmp_ne_u32_e32 vcc, 0, v6\n"
        "s_waitcnt vmcnt(0)\n"
        "s_cbranch_vccz .Lk129\n"
        "s_sub_u32 s38, s38, 1\n"
        "s_cbranch_scc0 .Lk127\n"
        "s_mov_b32 s38, 0\n"
        ".Lk129:\n"
        "s_waitcnt lgkmcnt(0)\n"
        "v_mfma_f32_16x16x32_bf16 v[88:91], v[116:119], v[44:47], 0\n"
        "v_mfma_f32_16x16x32_bf16 v[92:95], v[120:123], v[44:47], 0\n"
        "v_mfma_f32_16x16x32_bf16 v[96:99], v[124:127], v[44:47], 0\n"
        "v_mfma_f32_16x16x32_bf16 v[100:103], v[128:131], v[44:47], 0\n"
        "v_mfma_f32_16x16x32_bf16 v[104:107], v[132:135], v[44:47], 0\n"
        "v_mfma_f32_16x16x32_bf16 v[88:91], v[136:139], v[48:51], v[88:91]\n"
        "v_mfma_f32_16x16x32_bf16 v[92:95], v[140:143], v[48:51], v[92:95]\n"
        "v_mfma_f32_16x16x32_bf16 v[96:99], v[144:147], v[48:51], v[96:99]\n"
        "v_mfma_f32_16x16x32_bf16 v[100:103], v[148:151], v[48:51], v[100:103]\n"
        "v_mfma_f32_16x16x32_bf16 v[104:107], v[152:155], v[48:51], v[104:107]\n"
        "v_mfma_f32_16x16x32_bf16 v[88:91], v[156:159], v[52:55], v[88:91]\n"
        "v_mfma_f32_16x16x32_bf16 v[92:95], v[160:163], v[52:55], v[92:95]\n"
        "v_mfma_f32_16x16x32_bf16 v[96:99], v[164:167], v[52:55], v[96:99]\n"
        "v_mfma_f32_16x16x32_bf16 v[100:103], v[168:171], v[52:55], v[100:103]\n"
        "v_mfma_f32_16x16x32_bf16 v[104:107], v[172:175], v[52:55], v[104:107]\n"
        "v_mfma_f32_16x16x32_bf16 v[88:91], v[176:179], v[12:15], v[88:91]\n"
        "v_mfma_f32_16x16x32_bf16 v[92:95], v[180:183], v[12:15], v[92:95]\n"
        "v_mfma_f32_16x16x32_bf16 v[96:99], v[184:187], v[12:15], v[96:99]\n"
        "v_mfma_f32_16x16x32_bf16 v[100:103], v[188:191], v[12:15], v[100:103]\n"
        "v_mfma_f32_16x16x32_bf16 v[104:107], v[192:195], v[12:15], v[104:107]\n"
        "v_mfma_f32_16x16x32_bf16 v[88:91], v[196:199], v[16:19], v[88:91]\n"
        "v_mfma_f32_16x16x32_bf16 v[92:95], v[200:203], v[16:19], v[92:95]\n"
        "v_mfma_f32_16x16x32_bf16 v[96:99], v[204:207], v[16:19], v[96:99]\n"
        "v_mfma_f32_16x16x32_bf16 v[100:103], v[208:211], v[16:19], v[100:103]\n"
        "v_mfma_f32_16x16x32_bf16 v[104:107], v[212:215], v[16:19], v[104:107]\n"
        "v_mfma_f32_16x16x32_bf16 v[88:91], v[216:219], v[20:23], v[88:91]\n"
        "v_mfma_f32_16x16x32_bf16 v[92:95], v[220:223], v[20:23], v[92:95]\n"
        "v_mfma_f32_16x16x32_bf16 v[96:99], v[224:227], v[20:23], v[96:99]\n"
        "v_mfma_f32_16x16x32_bf16 v[100:103], v[228:231], v[20:23], v[100:103]\n"
        "v_mfma_f32_16x16x32_bf16 v[104:107], v[232:235], v[20:23], v[104:107]\n"
        "v_mfma_f32_16x16x32_bf16 v[88:91], v[236:239], v[56:59], v[88:91]\n"
        "v_mfma_f32_16x16x32_bf16 v[92:95], v[240:243], v[56:59], v[92:95]\n"
        "v_mfma_f32_16x16x32_bf16 v[96:99], v[244:247], v[56:59], v[96:99]\n"
        "v_mfma_f32_16x16x32_bf16 v[100:103], v[248:251], v[56:59], v[100:103]\n"
        "v_mfma_f32_16x16x32_bf16 v[104:107], v[252:255], v[56:59], v[104:107]\n"
        "s_nop 7\n"
        "s_branch .Lk128\n"
        ".Lk130:\n"
        "s_sleep 12\n"
        "s_nop 7\n"
        "s_add_u32 s3, s48, 0xf000\n"
        "s_add_u32 s44, s3, 0xf00\n"
        "buffer_load_dwordx4 v[28:31], v3, s[12:15], s3 offen sc1\n"
        "buffer_load_dwordx4 v[32:35], v3, s[12:15], s3 offen offset:1024 sc1\n"
        "buffer_load_dwordx4 v[36:39], v3, s[12:15], s3 offen offset:2048 sc1\n"
        "buffer_load_dwordx3 v[40:42], v4, s[12:15], s3 offen offset:3072 sc1\n"
        "buffer_load_dwordx4 v[60:63], v3, s[12:15], s44 offen sc1\n"
        "buffer_load_dwordx4 v[64:67], v3, s[12:15], s44 offen offset:1024 sc1\n"
        "buffer_load_dwordx4 v[68:71], v3, s[12:15], s44 offen offset:2048 sc1\n"
        "buffer_load_dwordx3 v[72:74], v4, s[12:15], s44 offen offset:3072 sc1\n"
        "s_waitcnt vmcnt(7)\n"
        "v_add3_u32 v6, v28, v29, s58\n"
        "v_add3_u32 v6, v30, v31, v6\n"
        "s_waitcnt vmcnt(6)\n"
        "v_add3_u32 v6, v32, v33, v6\n"
        "v_add3_u32 v6, v34, v35, v6\n"
        "s_waitcnt vmcnt(5)\n"
        "v_add3_u32 v6, v36, v37, v6\n"
        "v_add3_u32 v6, v38, v39, v6\n"
        "s_waitcnt vmcnt(4)\n"
        "v_add3_u32 v6, v40, v41, v6\n"
        "s_waitcnt vmcnt(3)\n"
        "v_add3_u32 v1, v60, v61, s58\n"
        "v_add3_u32 v1, v62, v63, v1\n"
        "s_waitcnt vmcnt(2)\n"
        "v_add3_u32 v1, v64, v65, v1\n"
        "v_add3_u32 v1, v66, v67, v1\n"
        "s_waitcnt vmcnt(1)\n"
        "v_add3_u32 v1, v68, v69, v1\n"
        "v_add3_u32 v1, v70, v71, v1\n"
        "s_waitcnt vmcnt(0)\n"
        "v_add3_u32 v1, v72, v73, v1\n"
        "v_xor_b32_e32 v6, v6, v42\n"
        "v_bitop3_b32 v6, v1, v74, v6 bitop3:0xbe\n"
        "v_mov_b32_e32 v42, v72\n"
        "v_mov_b32_e32 v43, v73\n"
        "v_cmp_ne_u32_e32 vcc, 0, v6\n"
        "s_waitcnt vmcnt(0)\n"
        "s_cbranch_vccz .Lk132\n"
        "s_sub_u32 s38, s38, 1\n"
        "s_cbranch_scc0 .Lk130\n"
        "s_mov_b32 s38, 0\n"
        ".Lk132:\n"
        "s_waitcnt lgkmcnt(0)\n"
        "v_mfma_f32_16x16x32_bf16 v[108:111], v[116:119], v[28:31], 0\n"
        "v_mfma_f32_16x16x32_bf16 v[112:115], v[120:123], v[28:31], 0\n"
        "v_mfma_f32_16x16x32_bf16 v[76:79], v[124:127], v[28:31], 0\n"
        "v_mfma_f32_16x16x32_bf16 v[80:83], v[128:131], v[28:31], 0\n"
        "v_mfma_f32_16x16x32_bf16 v[84:87], v[132:135], v[28:31], 0\n"
        "v_mfma_f32_16x16x32_bf16 v[108:111], v[136:139], v[32:35], v[108:111]\n"
        "v_mfma_f32_16x16x32_bf16 v[112:115], v[140:143], v[32:35], v[112:115]\n"
        "v_mfma_f32_16x16x32_bf16 v[76:79], v[144:147], v[32:35], v[76:79]\n"
        "v_mfma_f32_16x16x32_bf16 v[80:83], v[148:151], v[32:35], v[80:83]\n"
        "v_mfma_f32_16x16x32_bf16 v[84:87], v[152:155], v[32:35], v[84:87]\n"
        "v_mfma_f32_16x16x32_bf16 v[108:111], v[156:159], v[36:39], v[108:111]\n"
        "v_mfma_f32_16x16x32_bf16 v[112:115], v[160:163], v[36:39], v[112:115]\n"
        "v_mfma_f32_16x16x32_bf16 v[76:79], v[164:167], v[36:39], v[76:79]\n"
        "v_mfma_f32_16x16x32_bf16 v[80:83], v[168:171], v[36:39], v[80:83]\n"
        "v_mfma_f32_16x16x32_bf16 v[84:87], v[172:175], v[36:39], v[84:87]\n"
        "v_mfma_f32_16x16x32_bf16 v[108:111], v[176:179], v[60:63], v[108:111]\n"
        "v_mfma_f32_16x16x32_bf16 v[112:115], v[180:183], v[60:63], v[112:115]\n"
        "v_mfma_f32_16x16x32_bf16 v[76:79], v[184:187], v[60:63], v[76:79]\n"
        "v_mfma_f32_16x16x32_bf16 v[80:83], v[188:191], v[60:63], v[80:83]\n"
        "v_mfma_f32_16x16x32_bf16 v[84:87], v[192:195], v[60:63], v[84:87]\n"
        "v_mfma_f32_16x16x32_bf16 v[108:111], v[196:199], v[64:67], v[108:111]\n"
        "v_mfma_f32_16x16x32_bf16 v[112:115], v[200:203], v[64:67], v[112:115]\n"
        "v_mfma_f32_16x16x32_bf16 v[76:79], v[204:207], v[64:67], v[76:79]\n"
        "v_mfma_f32_16x16x32_bf16 v[80:83], v[208:211], v[64:67], v[80:83]\n"
        "v_mfma_f32_16x16x32_bf16 v[84:87], v[212:215], v[64:67], v[84:87]\n"
        "v_mfma_f32_16x16x32_bf16 v[108:111], v[216:219], v[68:71], v[108:111]\n"
        "v_mfma_f32_16x16x32_bf16 v[112:115], v[220:223], v[68:71], v[112:115]\n"
        "v_mfma_f32_16x16x32_bf16 v[76:79], v[224:227], v[68:71], v[76:79]\n"
        "v_mfma_f32_16x16x32_bf16 v[80:83], v[228:231], v[68:71], v[80:83]\n"
        "v_mfma_f32_16x16x32_bf16 v[84:87], v[232:235], v[68:71], v[84:87]\n"
        "v_mfma_f32_16x16x32_bf16 v[108:111], v[236:239], v[40:43], v[108:111]\n"
        "v_mfma_f32_16x16x32_bf16 v[112:115], v[240:243], v[40:43], v[112:115]\n"
        "v_mfma_f32_16x16x32_bf16 v[76:79], v[244:247], v[40:43], v[76:79]\n"
        "v_mfma_f32_16x16x32_bf16 v[80:83], v[248:251], v[40:43], v[80:83]\n"
        "v_mfma_f32_16x16x32_bf16 v[84:87], v[252:255], v[40:43], v[84:87]\n"
        "s_add_u32 s3, s48, 0x3c000\n"
        "s_add_u32 s44, s3, 0xf00\n"
        "s_nop 7\n"
        "s_branch .Lk131\n"
        ".Lk133:\n"
        "s_sleep 12\n"
        "s_nop 7\n"
        "s_add_u32 s3, s48, 0x3c000\n"
        "s_add_u32 s44, s3, 0xf00\n"
        "buffer_load_dwordx4 v[44:47], v3, s[12:15], s3 offen sc1\n"
        "buffer_load_dwordx4 v[48:51], v3, s[12:15], s3 offen offset:1024 sc1\n"
        "buffer_load_dwordx4 v[52:55], v3, s[12:15], s3 offen offset:2048 sc1\n"
        "buffer_load_dwordx3 v[56:58], v4, s[12:15], s3 offen offset:3072 sc1\n"
        "buffer_load_dwordx4 v[12:15], v3, s[12:15], s44 offen sc1\n"
        "buffer_load_dwordx4 v[16:19], v3, s[12:15], s44 offen offset:1024 sc1\n"
        "buffer_load_dwordx4 v[20:23], v3, s[12:15], s44 offen offset:2048 sc1\n"
        "buffer_load_dwordx3 v[24:26], v4, s[12:15], s44 offen offset:3072 sc1\n"
        "s_waitcnt vmcnt(7)\n"
        "v_add3_u32 v6, v44, v45, s58\n"
        "v_add3_u32 v6, v46, v47, v6\n"
        "s_waitcnt vmcnt(6)\n"
        "v_add3_u32 v6, v48, v49, v6\n"
        "v_add3_u32 v6, v50, v51, v6\n"
        "s_waitcnt vmcnt(5)\n"
        "v_add3_u32 v6, v52, v53, v6\n"
        "v_add3_u32 v6, v54, v55, v6\n"
        "s_waitcnt vmcnt(4)\n"
        "v_add3_u32 v6, v56, v57, v6\n"
        "s_waitcnt vmcnt(3)\n"
        "v_add3_u32 v1, v12, v13, s58\n"
        "v_add3_u32 v1, v14, v15, v1\n"
        "s_waitcnt vmcnt(2)\n"
        "v_add3_u32 v1, v16, v17, v1\n"
        "v_add3_u32 v1, v18, v19, v1\n"
        "s_waitcnt vmcnt(1)\n"
        "v_add3_u32 v1, v20, v21, v1\n"
        "v_add3_u32 v1, v22, v23, v1\n"
        "s_waitcnt vmcnt(0)\n"
        "v_add3_u32 v1, v24, v25, v1\n"
        "v_xor_b32_e32 v6, v6, v58\n"
        "v_bitop3_b32 v6, v1, v26, v6 bitop3:0xbe\n"
        "v_mov_b32_e32 v58, v24\n"
        "v_mov_b32_e32 v59, v25\n"
        "v_cmp_ne_u32_e32 vcc, 0, v6\n"
        "s_waitcnt vmcnt(0)\n"
        "s_cbranch_vccz .Lk135\n"
        "s_sub_u32 s38, s38, 1\n"
        "s_cbranch_scc0 .Lk133\n"
        "s_mov_b32 s38, 0\n"
        ".Lk135:\n"
        "s_waitcnt lgkmcnt(0)\n"
        "v_mfma_f32_16x16x32_bf16 v[88:91], v[116:119], v[44:47], 0\n"
        "v_mfma_f32_16x16x32_bf16 v[92:95], v[120:123], v[44:47], 0\n"
        "v_mfma_f32_16x16x32_bf16 v[96:99], v[124:127], v[44:47], 0\n"
        "v_mfma_f32_16x16x32_bf16 v[100:103], v[128:131], v[44:47], 0\n"
        "v_mfma_f32_16x16x32_bf16 v[104:107], v[132:135], v[44:47], 0\n"
        "v_mfma_f32_16x16x32_bf16 v[88:91], v[136:139], v[48:51], v[88:91]\n"
        "v_mfma_f32_16x16x32_bf16 v[92:95], v[140:143], v[48:51], v[92:95]\n"
        "v_mfma_f32_16x16x32_bf16 v[96:99], v[144:147], v[48:51], v[96:99]\n"
        "v_mfma_f32_16x16x32_bf16 v[100:103], v[148:151], v[48:51], v[100:103]\n"
        "v_mfma_f32_16x16x32_bf16 v[104:107], v[152:155], v[48:51], v[104:107]\n"
        "v_mfma_f32_16x16x32_bf16 v[88:91], v[156:159], v[52:55], v[88:91]\n"
        "v_mfma_f32_16x16x32_bf16 v[92:95], v[160:163], v[52:55], v[92:95]\n"
        "v_mfma_f32_16x16x32_bf16 v[96:99], v[164:167], v[52:55], v[96:99]\n"
        "v_mfma_f32_16x16x32_bf16 v[100:103], v[168:171], v[52:55], v[100:103]\n"
        "v_mfma_f32_16x16x32_bf16 v[104:107], v[172:175], v[52:55], v[104:107]\n"
        "v_mfma_f32_16x16x32_bf16 v[88:91], v[176:179], v[12:15], v[88:91]\n"
        "v_mfma_f32_16x16x32_bf16 v[92:95], v[180:183], v[12:15], v[92:95]\n"
        "v_mfma_f32_16x16x32_bf16 v[96:99], v[184:187], v[12:15], v[96:99]\n"
        "v_mfma_f32_16x16x32_bf16 v[100:103], v[188:191], v[12:15], v[100:103]\n"
        "v_mfma_f32_16x16x32_bf16 v[104:107], v[192:195], v[12:15], v[104:107]\n"
        "v_mfma_f32_16x16x32_bf16 v[88:91], v[196:199], v[16:19], v[88:91]\n"
        "v_mfma_f32_16x16x32_bf16 v[92:95], v[200:203], v[16:19], v[92:95]\n"
        "v_mfma_f32_16x16x32_bf16 v[96:99], v[204:207], v[16:19], v[96:99]\n"
        "v_mfma_f32_16x16x32_bf16 v[100:103], v[208:211], v[16:19], v[100:103]\n"
        "v_mfma_f32_16x16x32_bf16 v[104:107], v[212:215], v[16:19], v[104:107]\n"
        "v_mfma_f32_16x16x32_bf16 v[88:91], v[216:219], v[20:23], v[88:91]\n"
        "v_mfma_f32_16x16x32_bf16 v[92:95], v[220:223], v[20:23], v[92:95]\n"
        "v_mfma_f32_16x16x32_bf16 v[96:99], v[224:227], v[20:23], v[96:99]\n"
        "v_mfma_f32_16x16x32_bf16 v[100:103], v[228:231], v[20:23], v[100:103]\n"
        "v_mfma_f32_16x16x32_bf16 v[104:107], v[232:235], v[20:23], v[104:107]\n"
        "v_mfma_f32_16x16x32_bf16 v[88:91], v[236:239], v[56:59], v[88:91]\n"
        "v_mfma_f32_16x16x32_bf16 v[92:95], v[240:243], v[56:59], v[92:95]\n"
        "v_mfma_f32_16x16x32_bf16 v[96:99], v[244:247], v[56:59], v[96:99]\n"
        "v_mfma_f32_16x16x32_bf16 v[100:103], v[248:251], v[56:59], v[100:103]\n"
        "v_mfma_f32_16x16x32_bf16 v[104:107], v[252:255], v[56:59], v[104:107]\n"
        "s_nop 7\n"
        "s_branch .Lk134\n"
        ".Lk136:\n"
        "s_sleep 12\n"
        "s_nop 7\n"
        "s_add_u32 s3, s48, 0x4b000\n"
        "s_add_u32 s44, s3, 0xf00\n"
        "buffer_load_dwordx4 v[28:31], v3, s[12:15], s3 offen sc1\n"
        "buffer_load_dwordx4 v[32:35], v3, s[12:15], s3 offen offset:1024 sc1\n"
        "buffer_load_dwordx4 v[36:39], v3, s[12:15], s3 offen offset:2048 sc1\n"
        "buffer_load_dwordx3 v[40:42], v4, s[12:15], s3 offen offset:3072 sc1\n"
        "buffer_load_dwordx4 v[60:63], v3, s[12:15], s44 offen sc1\n"
        "buffer_load_dwordx4 v[64:67], v3, s[12:15], s44 offen offset:1024 sc1\n"
        "buffer_load_dwordx4 v[68:71], v3, s[12:15], s44 offen offset:2048 sc1\n"
        "buffer_load_dwordx3 v[72:74], v4, s[12:15], s44 offen offset:3072 sc1\n"
        "s_waitcnt vmcnt(7)\n"
        "v_add3_u32 v6, v28, v29, s58\n"
        "v_add3_u32 v6, v30, v31, v6\n"
        "s_waitcnt vmcnt(6)\n"
        "v_add3_u32 v6, v32, v33, v6\n"
        "v_add3_u32 v6, v34, v35, v6\n"
        "s_waitcnt vmcnt(5)\n"
        "v_add3_u32 v6, v36, v37, v6\n"
        "v_add3_u32 v6, v38, v39, v6\n"
        "s_waitcnt vmcnt(4)\n"
        "v_add3_u32 v6, v40, v41, v6\n"
        "s_waitcnt vmcnt(3)\n"
        "v_add3_u32 v1, v60, v61, s58\n"
        "v_add3_u32 v1, v62, v63, v1\n"
        "s_waitcnt vmcnt(2)\n"
        "v_add3_u32 v1, v64, v65, v1\n"
        "v_add3_u32 v1, v66, v67, v1\n"
        "s_waitcnt vmcnt(1)\n"
        "v_add3_u32 v1, v68, v69, v1\n"
        "v_add3_u32 v1, v70, v71, v1\n"
        "s_waitcnt vmcnt(0)\n"
        "v_add3_u32 v1, v72, v73, v1\n"
        "v_xor_b32_e32 v6, v6, v42\n"
        "v_bitop3_b32 v6, v1, v74, v6 bitop3:0xbe\n"
        "v_mov_b32_e32 v42, v72\n"
        "v_mov_b32_e32 v43, v73\n"
        "v_cmp_ne_u32_e32 vcc, 0, v6\n"
        "s_waitcnt vmcnt(0)\n"
        "s_cbranch_vccz .Lk138\n"
        "s_sub_u32 s38, s38, 1\n"
        "s_cbranch_scc0 .Lk136\n"
        "s_mov_b32 s38, 0\n"
        ".Lk138:\n"
        "s_waitcnt lgkmcnt(0)\n"
        "v_mfma_f32_16x16x32_bf16 v[108:111], v[116:119], v[28:31], 0\n"
        "v_mfma_f32_16x16x32_bf16 v[112:115], v[120:123], v[28:31], 0\n"
        "v_mfma_f32_16x16x32_bf16 v[76:79], v[124:127], v[28:31], 0\n"
        "v_mfma_f32_16x16x32_bf16 v[80:83], v[128:131], v[28:31], 0\n"
        "v_mfma_f32_16x16x32_bf16 v[84:87], v[132:135], v[28:31], 0\n"
        "v_mfma_f32_16x16x32_bf16 v[108:111], v[136:139], v[32:35], v[108:111]\n"
        "v_mfma_f32_16x16x32_bf16 v[112:115], v[140:143], v[32:35], v[112:115]\n"
        "v_mfma_f32_16x16x32_bf16 v[76:79], v[144:147], v[32:35], v[76:79]\n"
        "v_mfma_f32_16x16x32_bf16 v[80:83], v[148:151], v[32:35], v[80:83]\n"
        "v_mfma_f32_16x16x32_bf16 v[84:87], v[152:155], v[32:35], v[84:87]\n"
        "v_mfma_f32_16x16x32_bf16 v[108:111], v[156:159], v[36:39], v[108:111]\n"
        "v_mfma_f32_16x16x32_bf16 v[112:115], v[160:163], v[36:39], v[112:115]\n"
        "v_mfma_f32_16x16x32_bf16 v[76:79], v[164:167], v[36:39], v[76:79]\n"
        "v_mfma_f32_16x16x32_bf16 v[80:83], v[168:171], v[36:39], v[80:83]\n"
        "v_mfma_f32_16x16x32_bf16 v[84:87], v[172:175], v[36:39], v[84:87]\n"
        "v_mfma_f32_16x16x32_bf16 v[108:111], v[176:179], v[60:63], v[108:111]\n"
        "v_mfma_f32_16x16x32_bf16 v[112:115], v[180:183], v[60:63], v[112:115]\n"
        "v_mfma_f32_16x16x32_bf16 v[76:79], v[184:187], v[60:63], v[76:79]\n"
        "v_mfma_f32_16x16x32_bf16 v[80:83], v[188:191], v[60:63], v[80:83]\n"
        "v_mfma_f32_16x16x32_bf16 v[84:87], v[192:195], v[60:63], v[84:87]\n"
        "v_mfma_f32_16x16x32_bf16 v[108:111], v[196:199], v[64:67], v[108:111]\n"
        "v_mfma_f32_16x16x32_bf16 v[112:115], v[200:203], v[64:67], v[112:115]\n"
        "v_mfma_f32_16x16x32_bf16 v[76:79], v[204:207], v[64:67], v[76:79]\n"
        "v_mfma_f32_16x16x32_bf16 v[80:83], v[208:211], v[64:67], v[80:83]\n"
        "v_mfma_f32_16x16x32_bf16 v[84:87], v[212:215], v[64:67], v[84:87]\n"
        "v_mfma_f32_16x16x32_bf16 v[108:111], v[216:219], v[68:71], v[108:111]\n"
        "v_mfma_f32_16x16x32_bf16 v[112:115], v[220:223], v[68:71], v[112:115]\n"
        "v_mfma_f32_16x16x32_bf16 v[76:79], v[224:227], v[68:71], v[76:79]\n"
        "v_mfma_f32_16x16x32_bf16 v[80:83], v[228:231], v[68:71], v[80:83]\n"
        "v_mfma_f32_16x16x32_bf16 v[84:87], v[232:235], v[68:71], v[84:87]\n"
        "v_mfma_f32_16x16x32_bf16 v[108:111], v[236:239], v[40:43], v[108:111]\n"
        "v_mfma_f32_16x16x32_bf16 v[112:115], v[240:243], v[40:43], v[112:115]\n"
        "v_mfma_f32_16x16x32_bf16 v[76:79], v[244:247], v[40:43], v[76:79]\n"
        "v_mfma_f32_16x16x32_bf16 v[80:83], v[248:251], v[40:43], v[80:83]\n"
        "v_mfma_f32_16x16x32_bf16 v[84:87], v[252:255], v[40:43], v[84:87]\n"
        "s_nop 7\n"
        "s_branch .Lk137\n"
        ".Lk141:\n"
        "s_sleep 12\n"
        "s_nop 7\n"
        "s_add_u32 s3, s48, 0\n"
        "s_add_u32 s44, s3, 0xf00\n"
        "buffer_load_dwordx4 v[44:47], v3, s[12:15], s3 offen sc1\n"
        "buffer_load_dwordx4 v[48:51], v3, s[12:15], s3 offen offset:1024 sc1\n"
        "buffer_load_dwordx4 v[52:55], v3, s[12:15], s3 offen offset:2048 sc1\n"
        "buffer_load_dwordx3 v[56:58], v4, s[12:15], s3 offen offset:3072 sc1\n"
        "buffer_load_dwordx4 v[12:15], v3, s[12:15], s44 offen sc1\n"
        "buffer_load_dwordx4 v[16:19], v3, s[12:15], s44 offen offset:1024 sc1\n"
        "buffer_load_dwordx4 v[20:23], v3, s[12:15], s44 offen offset:2048 sc1\n"
        "buffer_load_dwordx3 v[24:26], v4, s[12:15], s44 offen offset:3072 sc1\n"
        "s_waitcnt vmcnt(7)\n"
        "v_add3_u32 v6, v44, v45, s58\n"
        "v_add3_u32 v6, v46, v47, v6\n"
        "s_waitcnt vmcnt(6)\n"
        "v_add3_u32 v6, v48, v49, v6\n"
        "v_add3_u32 v6, v50, v51, v6\n"
        "s_waitcnt vmcnt(5)\n"
        "v_add3_u32 v6, v52, v53, v6\n"
        "v_add3_u32 v6, v54, v55, v6\n"
        "s_waitcnt vmcnt(4)\n"
        "v_add3_u32 v6, v56, v57, v6\n"
        "s_waitcnt vmcnt(3)\n"
        "v_add3_u32 v1, v12, v13, s58\n"
        "v_add3_u32 v1, v14, v15, v1\n"
        "s_waitcnt vmcnt(2)\n"
        "v_add3_u32 v1, v16, v17, v1\n"
        "v_add3_u32 v1, v18, v19, v1\n"
        "s_waitcnt vmcnt(1)\n"
        "v_add3_u32 v1, v20, v21, v1\n"
        "v_add3_u32 v1, v22, v23, v1\n"
        "s_waitcnt vmcnt(0)\n"
        "v_add3_u32 v1, v24, v25, v1\n"
        "v_xor_b32_e32 v6, v6, v58\n"
        "v_bitop3_b32 v6, v1, v26, v6 bitop3:0xbe\n"
        "v_mov_b32_e32 v58, v24\n"
        "v_mov_b32_e32 v59, v25\n"
        "v_cmp_ne_u32_e32 vcc, 0, v6\n"
        "s_waitcnt vmcnt(0)\n"
        "s_cbranch_vccz .Lk143\n"
        "s_sub_u32 s38, s38, 1\n"
        "s_cbranch_scc0 .Lk141\n"
        "s_mov_b32 s38, 0\n"
        ".Lk143:\n"
        "ds_read_b128 v[88:91], v7\n"
        "ds_read_b128 v[92:95], v7 offset:4096\n"
        "ds_read_b128 v[96:99], v7 offset:8192\n"
        "ds_read_b128 v[100:103], v7 offset:12288\n"
        "ds_read_b128 v[104:107], v7 offset:16384\n"
        "s_waitcnt lgkmcnt(0)\n"
        "v_mfma_f32_16x16x32_bf16 v[88:91], v[116:119], v[44:47], v[88:91]\n"
        "v_mfma_f32_16x16x32_bf16 v[92:95], v[120:123], v[44:47], v[92:95]\n"
        "v_mfma_f32_16x16x32_bf16 v[96:99], v[124:127], v[44:47], v[96:99]\n"
        "v_mfma_f32_16x16x32_bf16 v[100:103], v[128:131], v[44:47], v[100:103]\n"
        "v_mfma_f32_16x16x32_bf16 v[104:107], v[132:135], v[44:47], v[104:107]\n"
        "v_mfma_f32_16x16x32_bf16 v[88:91], v[136:139], v[48:51], v[88:91]\n"
        "v_mfma_f32_16x16x32_bf16 v[92:95], v[140:143], v[48:51], v[92:95]\n"
        "v_mfma_f32_16x16x32_bf16 v[96:99], v[144:147], v[48:51], v[96:99]\n"
        "v_mfma_f32_16x16x32_bf16 v[100:103], v[148:151], v[48:51], v[100:103]\n"
        "v_mfma_f32_16x16x32_bf16 v[104:107], v[152:155], v[48:51], v[104:107]\n"
        "v_mfma_f32_16x16x32_bf16 v[88:91], v[156:159], v[52:55], v[88:91]\n"
        "v_mfma_f32_16x16x32_bf16 v[92:95], v[160:163], v[52:55], v[92:95]\n"
        "v_mfma_f32_16x16x32_bf16 v[96:99], v[164:167], v[52:55], v[96:99]\n"
        "v_mfma_f32_16x16x32_bf16 v[100:103], v[168:171], v[52:55], v[100:103]\n"
        "v_mfma_f32_16x16x32_bf16 v[104:107], v[172:175], v[52:55], v[104:107]\n"
        "v_mfma_f32_16x16x32_bf16 v[88:91], v[176:179], v[12:15], v[88:91]\n"
        "v_mfma_f32_16x16x32_bf16 v[92:95], v[180:183], v[12:15], v[92:95]\n"
        "v_mfma_f32_16x16x32_bf16 v[96:99], v[184:187], v[12:15], v[96:99]\n"
        "v_mfma_f32_16x16x32_bf16 v[100:103], v[188:191], v[12:15], v[100:103]\n"
        "v_mfma_f32_16x16x32_bf16 v[104:107], v[192:195], v[12:15], v[104:107]\n"
        "v_mfma_f32_16x16x32_bf16 v[88:91], v[196:199], v[16:19], v[88:91]\n"
        "v_mfma_f32_16x16x32_bf16 v[92:95], v[200:203], v[16:19], v[92:95]\n"
        "v_mfma_f32_16x16x32_bf16 v[96:99], v[204:207], v[16:19], v[96:99]\n"
        "v_mfma_f32_16x16x32_bf16 v[100:103], v[208:211], v[16:19], v[100:103]\n"
        "v_mfma_f32_16x16x32_bf16 v[104:107], v[212:215], v[16:19], v[104:107]\n"
        "v_mfma_f32_16x16x32_bf16 v[88:91], v[216:219], v[20:23], v[88:91]\n"
        "v_mfma_f32_16x16x32_bf16 v[92:95], v[220:223], v[20:23], v[92:95]\n"
        "v_mfma_f32_16x16x32_bf16 v[96:99], v[224:227], v[20:23], v[96:99]\n"
        "v_mfma_f32_16x16x32_bf16 v[100:103], v[228:231], v[20:23], v[100:103]\n"
        "v_mfma_f32_16x16x32_bf16 v[104:107], v[232:235], v[20:23], v[104:107]\n"
        "v_mfma_f32_16x16x32_bf16 v[88:91], v[236:239], v[56:59], v[88:91]\n"
        "v_mfma_f32_16x16x32_bf16 v[92:95], v[240:243], v[56:59], v[92:95]\n"
        "v_mfma_f32_16x16x32_bf16 v[96:99], v[244:247], v[56:59], v[96:99]\n"
        "v_mfma_f32_16x16x32_bf16 v[100:103], v[248:251], v[56:59], v[100:103]\n"
        "v_mfma_f32_16x16x32_bf16 v[104:107], v[252:255], v[56:59], v[104:107]\n"
        "s_nop 7\n"
        "s_branch .Lk142\n"
        ".Lk144:\n"
        "s_sleep 12\n"
        "s_nop 7\n"
        "s_add_u32 s3, s48, 0xf000\n"
        "s_add_u32 s44, s3, 0xf00\n"
        "buffer_load_dwordx4 v[28:31], v3, s[12:15], s3 offen sc1\n"
        "buffer_load_dwordx4 v[32:35], v3, s[12:15], s3 offen offset:1024 sc1\n"
        "buffer_load_dwordx4 v[36:39], v3, s[12:15], s3 offen offset:2048 sc1\n"
        "buffer_load_dwordx3 v[40:42], v4, s[12:15], s3 offen offset:3072 sc1\n"
        "buffer_load_dwordx4 v[60:63], v3, s[12:15], s44 offen sc1\n"
        "buffer_load_dwordx4 v[64:67], v3, s[12:15], s44 offen offset:1024 sc1\n"
        "buffer_load_dwordx4 v[68:71], v3, s[12:15], s44 offen offset:2048 sc1\n"
        "buffer_load_dwordx3 v[72:74], v4, s[12:15], s44 offen offset:3072 sc1\n"
        "s_waitcnt vmcnt(7)\n"
        "v_add3_u32 v6, v28, v29, s58\n"
        "v_add3_u32 v6, v30, v31, v6\n"
        "s_waitcnt vmcnt(6)\n"
        "v_add3_u32 v6, v32, v33, v6\n"
        "v_add3_u32 v6, v34, v35, v6\n"
        "s_waitcnt vmcnt(5)\n"
        "v_add3_u32 v6, v36, v37, v6\n"
        "v_add3_u32 v6, v38, v39, v6\n"
        "s_waitcnt vmcnt(4)\n"
        "v_add3_u32 v6, v40, v41, v6\n"
        "s_waitcnt vmcnt(3)\n"
        "v_add3_u32 v1, v60, v61, s58\n"
        "v_add3_u32 v1, v62, v63, v1\n"
        "s_waitcnt vmcnt(2)\n"
        "v_add3_u32 v1, v64, v65, v1\n"
        "v_add3_u32 v1, v66, v67, v1\n"
        "s_waitcnt vmcnt(1)\n"
        "v_add3_u32 v1, v68, v69, v1\n"
        "v_add3_u32 v1, v70, v71, v1\n"
        "s_waitcnt vmcnt(0)\n"
        "v_add3_u32 v1, v72, v73, v1\n"
        "v_xor_b32_e32 v6, v6, v42\n"
        "v_bitop3_b32 v6, v1, v74, v6 bitop3:0xbe\n"
        "v_mov_b32_e32 v42, v72\n"
        "v_mov_b32_e32 v43, v73\n"
        "v_cmp_ne_u32_e32 vcc, 0, v6\n"
        "s_waitcnt vmcnt(0)\n"
        "s_cbranch_vccz .Lk146\n"
        "s_sub_u32 s38, s38, 1\n"
        "s_cbranch_scc0 .Lk144\n"
        "s_mov_b32 s38, 0\n"
        ".Lk146:\n"
        "ds_read_b128 v[108:111], v7 offset:20480\n"
        "ds_read_b128 v[112:115], v7 offset:24576\n"
        "ds_read_b128 v[76:79], v7 offset:28672\n"
        "ds_read_b128 v[80:83], v7 offset:32768\n"
        "ds_read_b128 v[84:87], v7 offset:36864\n"
        "s_waitcnt lgkmcnt(0)\n"
        "v_mfma_f32_16x16x32_bf16 v[108:111], v[116:119], v[28:31], v[108:111]\n"
        "v_mfma_f32_16x16x32_bf16 v[112:115], v[120:123], v[28:31], v[112:115]\n"
        "v_mfma_f32_16x16x32_bf16 v[76:79], v[124:127], v[28:31], v[76:79]\n"
        "v_mfma_f32_16x16x32_bf16 v[80:83], v[128:131], v[28:31], v[80:83]\n"
        "v_mfma_f32_16x16x32_bf16 v[84:87], v[132:135], v[28:31], v[84:87]\n"
        "v_mfma_f32_16x16x32_bf16 v[108:111], v[136:139], v[32:35], v[108:111]\n"
        "v_mfma_f32_16x16x32_bf16 v[112:115], v[140:143], v[32:35], v[112:115]\n"
        "v_mfma_f32_16x16x32_bf16 v[76:79], v[144:147], v[32:35], v[76:79]\n"
        "v_mfma_f32_16x16x32_bf16 v[80:83], v[148:151], v[32:35], v[80:83]\n"
        "v_mfma_f32_16x16x32_bf16 v[84:87], v[152:155], v[32:35], v[84:87]\n"
        "v_mfma_f32_16x16x32_bf16 v[108:111], v[156:159], v[36:39], v[108:111]\n"
        "v_mfma_f32_16x16x32_bf16 v[112:115], v[160:163], v[36:39], v[112:115]\n"
        "v_mfma_f32_16x16x32_bf16 v[76:79], v[164:167], v[36:39], v[76:79]\n"
        "v_mfma_f32_16x16x32_bf16 v[80:83], v[168:171], v[36:39], v[80:83]\n"
        "v_mfma_f32_16x16x32_bf16 v[84:87], v[172:175], v[36:39], v[84:87]\n"
        "v_mfma_f32_16x16x32_bf16 v[108:111], v[176:179], v[60:63], v[108:111]\n"
        "v_mfma_f32_16x16x32_bf16 v[112:115], v[180:183], v[60:63], v[112:115]\n"
        "v_mfma_f32_16x16x32_bf16 v[76:79], v[184:187], v[60:63], v[76:79]\n"
        "v_mfma_f32_16x16x32_bf16 v[80:83], v[188:191], v[60:63], v[80:83]\n"
        "v_mfma_f32_16x16x32_bf16 v[84:87], v[192:195], v[60:63], v[84:87]\n"
        "v_mfma_f32_16x16x32_bf16 v[108:111], v[196:199], v[64:67], v[108:111]\n"
        "v_mfma_f32_16x16x32_bf16 v[112:115], v[200:203], v[64:67], v[112:115]\n"
        "v_mfma_f32_16x16x32_bf16 v[76:79], v[204:207], v[64:67], v[76:79]\n"
        "v_mfma_f32_16x16x32_bf16 v[80:83], v[208:211], v[64:67], v[80:83]\n"
        "v_mfma_f32_16x16x32_bf16 v[84:87], v[212:215], v[64:67], v[84:87]\n"
        "v_mfma_f32_16x16x32_bf16 v[108:111], v[216:219], v[68:71], v[108:111]\n"
        "v_mfma_f32_16x16x32_bf16 v[112:115], v[220:223], v[68:71], v[112:115]\n"
        "v_mfma_f32_16x16x32_bf16 v[76:79], v[224:227], v[68:71], v[76:79]\n"
        "v_mfma_f32_16x16x32_bf16 v[80:83], v[228:231], v[68:71], v[80:83]\n"
        "v_mfma_f32_16x16x32_bf16 v[84:87], v[232:235], v[68:71], v[84:87]\n"
        "v_mfma_f32_16x16x32_bf16 v[108:111], v[236:239], v[40:43], v[108:111]\n"
        "v_mfma_f32_16x16x32_bf16 v[112:115], v[240:243], v[40:43], v[112:115]\n"
        "v_mfma_f32_16x16x32_bf16 v[76:79], v[244:247], v[40:43], v[76:79]\n"
        "v_mfma_f32_16x16x32_bf16 v[80:83], v[248:251], v[40:43], v[80:83]\n"
        "v_mfma_f32_16x16x32_bf16 v[84:87], v[252:255], v[40:43], v[84:87]\n"
        "s_add_u32 s3, s48, 0x3c000\n"
        "s_add_u32 s44, s3, 0xf00\n"
        "s_nop 7\n"
        "s_branch .Lk145\n"
        ".Lk147:\n"
        "s_sleep 12\n"
        "s_nop 7\n"
        "s_add_u32 s3, s48, 0x3c000\n"
        "s_add_u32 s44, s3, 0xf00\n"
        "buffer_load_dwordx4 v[44:47], v3, s[12:15], s3 offen sc1\n"
        "buffer_load_dwordx4 v[48:51], v3, s[12:15], s3 offen offset:1024 sc1\n"
        "buffer_load_dwordx4 v[52:55], v3, s[12:15], s3 offen offset:2048 sc1\n"
        "buffer_load_dwordx3 v[56:58], v4, s[12:15], s3 offen offset:3072 sc1\n"
        "buffer_load_dwordx4 v[12:15], v3, s[12:15], s44 offen sc1\n"
        "buffer_load_dwordx4 v[16:19], v3, s[12:15], s44 offen offset:1024 sc1\n"
        "buffer_load_dwordx4 v[20:23], v3, s[12:15], s44 offen offset:2048 sc1\n"
        "buffer_load_dwordx3 v[24:26], v4, s[12:15], s44 offen offset:3072 sc1\n"
        "s_waitcnt vmcnt(7)\n"
        "v_add3_u32 v6, v44, v45, s58\n"
        "v_add3_u32 v6, v46, v47, v6\n"
        "s_waitcnt vmcnt(6)\n"
        "v_add3_u32 v6, v48, v49, v6\n"
        "v_add3_u32 v6, v50, v51, v6\n"
        "s_waitcnt vmcnt(5)\n"
        "v_add3_u32 v6, v52, v53, v6\n"
        "v_add3_u32 v6, v54, v55, v6\n"
        "s_waitcnt vmcnt(4)\n"
        "v_add3_u32 v6, v56, v57, v6\n"
        "s_waitcnt vmcnt(3)\n"
        "v_add3_u32 v1, v12, v13, s58\n"
        "v_add3_u32 v1, v14, v15, v1\n"
        "s_waitcnt vmcnt(2)\n"
        "v_add3_u32 v1, v16, v17, v1\n"
        "v_add3_u32 v1, v18, v19, v1\n"
        "s_waitcnt vmcnt(1)\n"
        "v_add3_u32 v1, v20, v21, v1\n"
        "v_add3_u32 v1, v22, v23, v1\n"
        "s_waitcnt vmcnt(0)\n"
        "v_add3_u32 v1, v24, v25, v1\n"
        "v_xor_b32_e32 v6, v6, v58\n"
        "v_bitop3_b32 v6, v1, v26, v6 bitop3:0xbe\n"
        "v_mov_b32_e32 v58, v24\n"
        "v_mov_b32_e32 v59, v25\n"
        "v_cmp_ne_u32_e32 vcc, 0, v6\n"
        "s_waitcnt vmcnt(0)\n"
        "s_cbranch_vccz .Lk149\n"
        "s_sub_u32 s38, s38, 1\n"
        "s_cbranch_scc0 .Lk147\n"
        "s_mov_b32 s38, 0\n"
        ".Lk149:\n"
        "ds_read_b128 v[88:91], v7\n"
        "ds_read_b128 v[92:95], v7 offset:4096\n"
        "ds_read_b128 v[96:99], v7 offset:8192\n"
        "ds_read_b128 v[100:103], v7 offset:12288\n"
        "ds_read_b128 v[104:107], v7 offset:16384\n"
        "s_waitcnt lgkmcnt(0)\n"
        "v_mfma_f32_16x16x32_bf16 v[88:91], v[116:119], v[44:47], v[88:91]\n"
        "v_mfma_f32_16x16x32_bf16 v[92:95], v[120:123], v[44:47], v[92:95]\n"
        "v_mfma_f32_16x16x32_bf16 v[96:99], v[124:127], v[44:47], v[96:99]\n"
        "v_mfma_f32_16x16x32_bf16 v[100:103], v[128:131], v[44:47], v[100:103]\n"
        "v_mfma_f32_16x16x32_bf16 v[104:107], v[132:135], v[44:47], v[104:107]\n"
        "v_mfma_f32_16x16x32_bf16 v[88:91], v[136:139], v[48:51], v[88:91]\n"
        "v_mfma_f32_16x16x32_bf16 v[92:95], v[140:143], v[48:51], v[92:95]\n"
        "v_mfma_f32_16x16x32_bf16 v[96:99], v[144:147], v[48:51], v[96:99]\n"
        "v_mfma_f32_16x16x32_bf16 v[100:103], v[148:151], v[48:51], v[100:103]\n"
        "v_mfma_f32_16x16x32_bf16 v[104:107], v[152:155], v[48:51], v[104:107]\n"
        "v_mfma_f32_16x16x32_bf16 v[88:91], v[156:159], v[52:55], v[88:91]\n"
        "v_mfma_f32_16x16x32_bf16 v[92:95], v[160:163], v[52:55], v[92:95]\n"
        "v_mfma_f32_16x16x32_bf16 v[96:99], v[164:167], v[52:55], v[96:99]\n"
        "v_mfma_f32_16x16x32_bf16 v[100:103], v[168:171], v[52:55], v[100:103]\n"
        "v_mfma_f32_16x16x32_bf16 v[104:107], v[172:175], v[52:55], v[104:107]\n"
        "v_mfma_f32_16x16x32_bf16 v[88:91], v[176:179], v[12:15], v[88:91]\n"
        "v_mfma_f32_16x16x32_bf16 v[92:95], v[180:183], v[12:15], v[92:95]\n"
        "v_mfma_f32_16x16x32_bf16 v[96:99], v[184:187], v[12:15], v[96:99]\n"
        "v_mfma_f32_16x16x32_bf16 v[100:103], v[188:191], v[12:15], v[100:103]\n"
        "v_mfma_f32_16x16x32_bf16 v[104:107], v[192:195], v[12:15], v[104:107]\n"
        "v_mfma_f32_16x16x32_bf16 v[88:91], v[196:199], v[16:19], v[88:91]\n"
        "v_mfma_f32_16x16x32_bf16 v[92:95], v[200:203], v[16:19], v[92:95]\n"
        "v_mfma_f32_16x16x32_bf16 v[96:99], v[204:207], v[16:19], v[96:99]\n"
        "v_mfma_f32_16x16x32_bf16 v[100:103], v[208:211], v[16:19], v[100:103]\n"
        "v_mfma_f32_16x16x32_bf16 v[104:107], v[212:215], v[16:19], v[104:107]\n"
        "v_mfma_f32_16x16x32_bf16 v[88:91], v[216:219], v[20:23], v[88:91]\n"
        "v_mfma_f32_16x16x32_bf16 v[92:95], v[220:223], v[20:23], v[92:95]\n"
        "v_mfma_f32_16x16x32_bf16 v[96:99], v[224:227], v[20:23], v[96:99]\n"
        "v_mfma_f32_16x16x32_bf16 v[100:103], v[228:231], v[20:23], v[100:103]\n"
        "v_mfma_f32_16x16x32_bf16 v[104:107], v[232:235], v[20:23], v[104:107]\n"
        "v_mfma_f32_16x16x32_bf16 v[88:91], v[236:239], v[56:59], v[88:91]\n"
        "v_mfma_f32_16x16x32_bf16 v[92:95], v[240:243], v[56:59], v[92:95]\n"
        "v_mfma_f32_16x16x32_bf16 v[96:99], v[244:247], v[56:59], v[96:99]\n"
        "v_mfma_f32_16x16x32_bf16 v[100:103], v[248:251], v[56:59], v[100:103]\n"
        "v_mfma_f32_16x16x32_bf16 v[104:107], v[252:255], v[56:59], v[104:107]\n"
        "s_nop 7\n"
        "s_branch .Lk148\n"
        ".Lk150:\n"
        "s_sleep 12\n"
        "s_nop 7\n"
        "s_add_u32 s3, s48, 0x4b000\n"
        "s_add_u32 s44, s3, 0xf00\n"
        "buffer_load_dwordx4 v[28:31], v3, s[12:15], s3 offen sc1\n"
        "buffer_load_dwordx4 v[32:35], v3, s[12:15], s3 offen offset:1024 sc1\n"
        "buffer_load_dwordx4 v[36:39], v3, s[12:15], s3 offen offset:2048 sc1\n"
        "buffer_load_dwordx3 v[40:42], v4, s[12:15], s3 offen offset:3072 sc1\n"
        "buffer_load_dwordx4 v[60:63], v3, s[12:15], s44 offen sc1\n"
        "buffer_load_dwordx4 v[64:67], v3, s[12:15], s44 offen offset:1024 sc1\n"
        "buffer_load_dwordx4 v[68:71], v3, s[12:15], s44 offen offset:2048 sc1\n"
        "buffer_load_dwordx3 v[72:74], v4, s[12:15], s44 offen offset:3072 sc1\n"
        "s_waitcnt vmcnt(7)\n"
        "v_add3_u32 v6, v28, v29, s58\n"
        "v_add3_u32 v6, v30, v31, v6\n"
        "s_waitcnt vmcnt(6)\n"
        "v_add3_u32 v6, v32, v33, v6\n"
        "v_add3_u32 v6, v34, v35, v6\n"
        "s_waitcnt vmcnt(5)\n"
        "v_add3_u32 v6, v36, v37, v6\n"
        "v_add3_u32 v6, v38, v39, v6\n"
        "s_waitcnt vmcnt(4)\n"
        "v_add3_u32 v6, v40, v41, v6\n"
        "s_waitcnt vmcnt(3)\n"
        "v_add3_u32 v1, v60, v61, s58\n"
        "v_add3_u32 v1, v62, v63, v1\n"
        "s_waitcnt vmcnt(2)\n"
        "v_add3_u32 v1, v64, v65, v1\n"
        "v_add3_u32 v1, v66, v67, v1\n"
        "s_waitcnt vmcnt(1)\n"
        "v_add3_u32 v1, v68, v69, v1\n"
        "v_add3_u32 v1, v70, v71, v1\n"
        "s_waitcnt vmcnt(0)\n"
        "v_add3_u32 v1, v72, v73, v1\n"
        "v_xor_b32_e32 v6, v6, v42\n"
        "v_bitop3_b32 v6, v1, v74, v6 bitop3:0xbe\n"
        "v_mov_b32_e32 v42, v72\n"
        "v_mov_b32_e32 v43, v73\n"
        "v_cmp_ne_u32_e32 vcc, 0, v6\n"
        "s_waitcnt vmcnt(0)\n"
        "s_cbranch_vccz .Lk152\n"
        "s_sub_u32 s38, s38, 1\n"
        "s_cbranch_scc0 .Lk150\n"
        "s_mov_b32 s38, 0\n"
        ".Lk152:\n"
        "ds_read_b128 v[108:111], v7 offset:20480\n"
        "ds_read_b128 v[112:115], v7 offset:24576\n"
        "ds_read_b128 v[76:79], v7 offset:28672\n"
        "ds_read_b128 v[80:83], v7 offset:32768\n"
        "ds_read_b128 v[84:87], v7 offset:36864\n"
        "s_waitcnt lgkmcnt(0)\n"
        "v_mfma_f32_16x16x32_bf16 v[108:111], v[116:119], v[28:31], v[108:111]\n"
        "v_mfma_f32_16x16x32_bf16 v[112:115], v[120:123], v[28:31], v[112:115]\n"
        "v_mfma_f32_16x16x32_bf16 v[76:79], v[124:127], v[28:31], v[76:79]\n"
        "v_mfma_f32_16x16x32_bf16 v[80:83], v[128:131], v[28:31], v[80:83]\n"
        "v_mfma_f32_16x16x32_bf16 v[84:87], v[132:135], v[28:31], v[84:87]\n"
        "v_mfma_f32_16x16x32_bf16 v[108:111], v[136:139], v[32:35], v[108:111]\n"
        "v_mfma_f32_16x16x32_bf16 v[112:115], v[140:143], v[32:35], v[112:115]\n"
        "v_mfma_f32_16x16x32_bf16 v[76:79], v[144:147], v[32:35], v[76:79]\n"
        "v_mfma_f32_16x16x32_bf16 v[80:83], v[148:151], v[32:35], v[80:83]\n"
        "v_mfma_f32_16x16x32_bf16 v[84:87], v[152:155], v[32:35], v[84:87]\n"
        "v_mfma_f32_16x16x32_bf16 v[108:111], v[156:159], v[36:39], v[108:111]\n"
        "v_mfma_f32_16x16x32_bf16 v[112:115], v[160:163], v[36:39], v[112:115]\n"
        "v_mfma_f32_16x16x32_bf16 v[76:79], v[164:167], v[36:39], v[76:79]\n"
        "v_mfma_f32_16x16x32_bf16 v[80:83], v[168:171], v[36:39], v[80:83]\n"
        "v_mfma_f32_16x16x32_bf16 v[84:87], v[172:175], v[36:39], v[84:87]\n"
        "v_mfma_f32_16x16x32_bf16 v[108:111], v[176:179], v[60:63], v[108:111]\n"
        "v_mfma_f32_16x16x32_bf16 v[108:111], v[196:199], v[64:67], v[108:111]\n"
        "v_mfma_f32_16x16x32_bf16 v[108:111], v[216:219], v[68:71], v[108:111]\n"
        "v_mfma_f32_16x16x32_bf16 v[108:111], v[236:239], v[40:43], v[108:111]\n"
        "v_mfma_f32_16x16x32_bf16 v[112:115], v[180:183], v[60:63], v[112:115]\n"
        "v_mfma_f32_16x16x32_bf16 v[112:115], v[200:203], v[64:67], v[112:115]\n"
        "s_nop 7\n"
        "s_branch .Lk151\n"
        ".Lkend:\n"
        :
        : "{s[0:1]}"(__builtin_amdgcn_kernarg_segment_ptr()), "{s2}"(blockIdx.x), "{v0}"(threadIdx.x), "{s[4:5]}"(ktab)
        : "v1", "v2", "v3", "v4", "v5", "v6", "v7", "v8", "v9", "v10", "v11", "v12", "v13", "v14", "v15", "v16", "v17", "v18", "v19", "v20", "v21", "v22", "v23", "v24", "v25", "v26", "v27", "v28", "v29", "v30", "v31", "v32", "v33", "v34", "v35", "v36", "v37", "v38", "v39", "v40", "v41", "v42", "v43", "v44", "v45", "v46", "v47", "v48", "v49", "v50", "v51", "v52", "v53", "v54", "v55", "v56", "v57", "v58", "v59", "v60", "v61", "v62", "v63", "v64", "v65", "v66", "v67", "v68", "v69", "v70", "v71", "v72", "v73", "v74", "v75", "v76", "v77", "v78", "v79", "v80", "v81", "v82", "v83", "v84", "v85", "v86", "v87", "v88", "v89", "v90", "v91", "v92", "v93", "v94", "v95", "v96", "v97", "v98", "v99", "v100", "v101", "v102", "v103", "v104", "v105", "v106", "v107", "v108", "v109", "v110", "v111", "v112", "v113", "v114", "v115", "v116", "v117", "v118", "v119", "v120", "v121", "v122", "v123", "v124", "v125", "v126", "v127", "v128", "v129", "v130", "v131", "v132", "v133", "v134", "v135", "v136", "v137", "v138", "v139", "v140", "v141", "v142", "v143", "v144", "v145", "v146", "v147", "v148", "v149", "v150", "v151", "v152", "v153", "v154", "v155", "v156", "v157", "v158", "v159", "v160", "v161", "v162", "v163", "v164", "v165", "v166", "v167", "v168", "v169", "v170", "v171", "v172", "v173", "v174", "v175", "v176", "v177", "v178", "v179", "v180", "v181", "v182", "v183", "v184", "v185", "v186", "v187", "v188", "v189", "v190", "v191", "v192", "v193", "v194", "v195", "v196", "v197", "v198", "v199", "v200", "v201", "v202", "v203", "v204", "v205", "v206", "v207", "v208", "v209", "v210", "v211", "v212", "v213", "v214", "v215", "v216", "v217", "v218", "v219", "v220", "v221", "v222", "v223", "v224", "v225", "v226", "v227", "v228", "v229", "v230", "v231", "v232", "v233", "v234", "v235", "v236", "v237", "v238", "v239", "v240", "v241", "v242", "v243", "v244", "v245", "v246", "v247", "v248", "v249", "v250", "v251", "v252", "v253", "v254", "v255", "s3", "s6", "s7", "s8", "s9", "s10", "s11", "s12", "s13", "s14", "s15", "s16", "s17", "s18", "s19", "s20", "s21", "s22", "s23", "s24", "s25", "s26", "s27", "s28", "s29", "s30", "s31", "s33", "s34", "s35", "s36", "s37", "s38", "s39", "s40", "s41", "s42", "s43", "s44", "s45", "s46", "s47", "s48", "s49", "s50", "s51", "s52", "s53", "s54", "s55", "s56", "s57", "s58", "s59", "s60", "s61", "s62", "s63", "s64", "s65", "s66", "s67", "vcc", "scc", "memory");
#endif
}

extern "C" void kernel_launch(void* const* d_in, const int* in_sizes, int n_in, void* d_out, int out_size, void* d_ws,
                              size_t ws_size, hipStream_t stream) {
    constexpr u32 RAW = 0x00020000u, XB = 2048 * 640 * 2, WB = 640 * 1728 * 2;
    (void)hipFuncSetAttribute((const void*)k_fused, hipFuncAttributeMaxDynamicSharedMemorySize, 163840);
    k_fused<<<256, 512, 163840, stream>>>(d_in[0], XB, RAW, (char*)d_ws + ((size_t)0 << 20), 18579456u, RAW, d_in[1], d_in[2], d_in[5], WB, RAW, d_in[4], WB,
                                          RAW, d_out, XB, RAW);
}
```
